```python
import torch

def _softmax(x, axis=-1):
    return torch.softmax(x, dim=axis)
def _gelu(x):
    return torch.nn.functional.gelu(x, approximate="tanh")
def _silu(x):
    return torch.nn.functional.silu(x)
def _sigmoid(x):
    return torch.sigmoid(x)
def _layernorm(x, axis=-1, eps=1e-5):
    m = x.mean(dim=axis, keepdim=True)
    v = x.var(dim=axis, keepdim=True, unbiased=False)
    return (x - m) / torch.sqrt(v + eps)
def _scatter_add(tgt, idx, src, axis=0):
    return tgt.clone().index_add_(axis, idx, src)
def _segment_sum(src, seg, n):
    out = torch.zeros((n,) + src.shape[1:], dtype=src.dtype)
    return out.index_add_(0, seg, src)
def _pad(x, pw):
    flat = tuple(v for pair in reversed(pw) for v in pair)
    return torch.nn.functional.pad(x, flat)

kernel_name = 'recurrent_scan__gqa_block_cached__dense_activation_silu__decode'

def setup_inputs():

    _w = (lambda a: (lambda t: t.to(torch.bfloat16) if t.is_floating_point() else t)(torch.as_tensor(a)))
    def _A_setup():
        B, Sq, D = 2, 1, 512
        Hq, Hkv, Sk, Dh = 32, 8, 32768, 128
        assert Hq % Hkv == 0, (Hq, Hkv)
        return {
            "x": _w((lambda *s: torch.randn(*s, dtype=torch.bfloat16) * 0.4)(B, Sq, D)),
            "k_cache": _w((lambda *s: torch.randn(*s, dtype=torch.bfloat16) * 1.6)(B, Hkv, Sk, Dh)),
            "v_cache": _w((lambda *s: torch.randn(*s, dtype=torch.bfloat16) * 1.6)(B, Hkv, Sk, Dh)),
            "wq": _w((lambda *s: torch.randn(*s, dtype=torch.bfloat16) * 1.6)(D, Hq * Dh) * (D ** -0.5)),
            "wo": _w((lambda *s: torch.randn(*s, dtype=torch.bfloat16) * 1.6)(Hq * Dh, D) * ((Hq * Dh) ** -0.5)),
        }
    _A_in = _A_setup()

    def _B_setup():
        B, S, D, F = 2, 1, 512, 1792
        return {
            "x": _w((lambda *s: torch.randn(*s, dtype=torch.bfloat16) * 0.4)(B, S, D)),
            "w1": _w((lambda *s: torch.randn(*s, dtype=torch.bfloat16) * 1.6)(D, F) * (D ** -0.5)),
            "w2": _w((lambda *s: torch.randn(*s, dtype=torch.bfloat16) * 1.6)(F, D) * (F ** -0.5)),
        }
    _B_in = _B_setup()

    return {
        "A_x": _A_in["x"],
        "A_k_cache": _A_in["k_cache"],
        "A_v_cache": _A_in["v_cache"],
        "A_wq": _A_in["wq"],
        "A_wo": _A_in["wo"],
        "B_x": _B_in["x"],
        "B_w1": _B_in["w1"],
        "B_w2": _B_in["w2"],
    }

def reference(A_x, A_k_cache, A_v_cache, A_wq, A_wo, B_x, B_w1, B_w2):
    def _A_ref(x, k_cache, v_cache, wq, wo):
        B, Sq, D = x.shape
        Hq, Dh = 32, 128
        Hkv = k_cache.shape[1]
        rep = Hq // Hkv
        q = (lambda x, *d: x.permute(*d))((x @ wq).reshape(B, Sq, Hq, Dh), 0, 2, 1, 3)
        k = (lambda x, n, axis: torch.repeat_interleave(x, n, dim=axis))(k_cache, rep, axis=1)
        v = (lambda x, n, axis: torch.repeat_interleave(x, n, dim=axis))(v_cache, rep, axis=1)
        scores = torch.einsum("bhqd,bhkd->bhqk", q, k) * (Dh ** -0.5)
        p = _softmax(scores, axis=-1)
        o = torch.einsum("bhqk,bhkd->bhqd", p, v)
        return (lambda x, *d: x.permute(*d))(o, 0, 2, 1, 3).reshape(B, Sq, Hq * Dh) @ wo

    def _B_ref(x, w1, w2):
        h = torch.einsum("bsd,df->bsf", x, w1)
        h = _silu(h)
        out = torch.einsum("bsf,fd->bsd", h, w2)
        return out

    x = A_x
    xn = _layernorm(x, axis=-1)
    s = _A_ref(xn, A_k_cache, A_v_cache, A_wq, A_wo)
    g = _silu(_B_ref(xn, B_w1, B_w2))
    return x + s * g
```

```cpp
#include <hip/hip_runtime.h>
#include <hip/hip_bf16.h>
#include <cstdint>

typedef unsigned short u16;
using bf16x2 = __attribute__((ext_vector_type(2))) __bf16;
using u32x4  = __attribute__((ext_vector_type(4))) unsigned;
using f32x4  = __attribute__((ext_vector_type(4))) float;

constexpr int B = 2, D = 512, HQ = 32, HKV = 8, SK = 32768, DH = 128, F = 1792, G = 4;
constexpr int NSPLIT = 32, WAVES = 4, PAGE = 16;
constexpr int KEYS = SK / NSPLIT;

__device__ __forceinline__ float bf2f(u16 v) { return __uint_as_float((unsigned)v << 16); }
__device__ __forceinline__ u16 f2bf(float f) { return __builtin_bit_cast(u16, (__bf16)f); }
__device__ __forceinline__ float rbf(float f) { return bf2f(f2bf(f)); }
__device__ __forceinline__ float dot2(unsigned k, unsigned q, float acc) {
  return __builtin_amdgcn_fdot2_f32_bf16(__builtin_bit_cast(bf16x2, k), __builtin_bit_cast(bf16x2, q), acc, false);
}
template <int CTRL> __device__ __forceinline__ float dpp(float x) {
  return __builtin_bit_cast(float, __builtin_amdgcn_mov_dpp(__builtin_bit_cast(int, x), CTRL, 0xf, 0xf, true));
}
constexpr int XOR1 = 0xB1, XOR2 = 0x4E, XOR7 = 0x141, XOR8 = 0x128, ROR4 = 0x124;
__device__ __forceinline__ float xrow16_max(float x) {
  auto s = __builtin_amdgcn_permlane16_swap(__float_as_uint(x), __float_as_uint(x), false, false);
  x = fmaxf(__uint_as_float(s[0]), __uint_as_float(s[1]));
  auto t = __builtin_amdgcn_permlane32_swap(__float_as_uint(x), __float_as_uint(x), false, false);
  return fmaxf(__uint_as_float(t[0]), __uint_as_float(t[1]));
}
__device__ __forceinline__ float xrow16_sum(float x) {
  auto s = __builtin_amdgcn_permlane16_swap(__float_as_uint(x), __float_as_uint(x), false, false);
  x = __uint_as_float(s[0]) + __uint_as_float(s[1]);
  auto t = __builtin_amdgcn_permlane32_swap(__float_as_uint(x), __float_as_uint(x), false, false);
  return __uint_as_float(t[0]) + __uint_as_float(t[1]);
}

__device__ void layernorm_to_lds(const u16* x, float* xn, float* red) {
  const int tid = threadIdx.x;
  for (int b = 0; b < B; ++b) {
    float v0 = bf2f(x[b * D + tid]), v1 = bf2f(x[b * D + 256 + tid]);
    float s = v0 + v1;
    for (int o = 32; o; o >>= 1) s += __shfl_xor(s, o);
    if ((tid & 63) == 0) red[tid >> 6] = s;
    __syncthreads();
    float mean = (red[0] + red[1] + red[2] + red[3]) * (1.f / D);
    __syncthreads();
    float d0 = v0 - mean, d1 = v1 - mean;
    float q = d0 * d0 + d1 * d1;
    for (int o = 32; o; o >>= 1) q += __shfl_xor(q, o);
    if ((tid & 63) == 0) red[tid >> 6] = q;
    __syncthreads();
    float var = (red[0] + red[1] + red[2] + red[3]) * (1.f / D);
    __syncthreads();
    float mb = rbf(mean);
    float den = rbf(sqrtf(rbf(rbf(var) + 1e-5f)));
    xn[b * D + tid] = rbf(rbf(v0 - mb) / den);
    xn[b * D + 256 + tid] = rbf(rbf(v1 - mb) / den);
  }
  __syncthreads();
}

__global__ void __launch_bounds__(256) k_pro(const u16* x, const u16* wq, const u16* w1, u16* q, float* h) {
  __shared__ float xn[B * D]; __shared__ float red[4]; __shared__ float part[4][2][64];
  layernorm_to_lds(x, xn, red);
  const int lane = threadIdx.x & 63, w = threadIdx.x >> 6;
  const int cb = blockIdx.x;
  const bool isq = cb < 64;
  const u16* W = isq ? wq + cb * 64 : w1 + (cb - 64) * 64;
  const int ld = isq ? 4096 : F;
  float a0 = 0, a1 = 0;
#pragma unroll 16
  for (int k = w * 128; k < w * 128 + 128; ++k) { float wv = bf2f(W[(size_t)k * ld + lane]); a0 += xn[k] * wv; a1 += xn[D + k] * wv; }
  part[w][0][lane] = a0; part[w][1][lane] = a1;
  __syncthreads();
  if (w < 2) {
    float s = part[0][w][lane] + part[1][w][lane] + part[2][w][lane] + part[3][w][lane];
    if (isq) q[w * 4096 + cb * 64 + lane] = f2bf(s);
    else { float v = rbf(s); float sv = rbf(v / (1.f + __expf(-v))); h[w * F + (cb - 64) * 64 + lane] = sv; }
  }
}

constexpr int MRG_REC = 4 + DH;
__global__ void __launch_bounds__(256) k_attn(const u16* q, const u16* kc, const u16* vc, float* pml, float* po) {
  __shared__ __attribute__((aligned(16))) float lds[256 + WAVES * 4 * MRG_REC];
  const int s = blockIdx.x, kvh = blockIdx.y, b = blockIdx.z;
  const int tid = threadIdx.x, lane = tid & 63, wave = __builtin_amdgcn_readfirstlane(tid >> 6), r = lane >> 4, c = lane & 15;
  constexpr float C2 = 0.088388347648318440f * 1.4426950408889634f;
  u32x4 qpk[4];
#pragma unroll
  for (int g = 0; g < 4; ++g) qpk[g] = *reinterpret_cast<const u32x4*>(q + b * 4096 + (kvh * 4 + g) * DH + 8 * c);
  const size_t base = ((size_t)(b * HKV + kvh) * SK + (size_t)s * KEYS) * DH + (size_t)r * DH + 8 * c;
  constexpr int NWP = KEYS / PAGE / WAVES;
  u32x4 Kn[4], Vn[4];
#define ISSUE(i_) do { const size_t o_ = base + (size_t)(wave + 4 * (i_)) * PAGE * DH; \
    _Pragma("unroll") for (int t_ = 0; t_ < 4; ++t_) { Kn[t_] = __builtin_nontemporal_load(reinterpret_cast<const u32x4*>(kc + o_ + t_ * 4 * DH)); Vn[t_] = __builtin_nontemporal_load(reinterpret_cast<const u32x4*>(vc + o_ + t_ * 4 * DH)); } } while (0)
  float m = -INFINITY, l = 0.f, acc[32];
#pragma unroll
  for (int k = 0; k < 32; ++k) acc[k] = 0.f;
  ISSUE(0);
  for (int i = 0; i < NWP; ++i) {
    u32x4 Kc[4], Vc[4];
#pragma unroll
    for (int t = 0; t < 4; ++t) { Kc[t] = Kn[t]; Vc[t] = Vn[t]; }
    if (i + 1 < NWP) ISSUE(i + 1);
    float x[16];
#pragma unroll
    for (int t = 0; t < 4; ++t)
#pragma unroll
      for (int g = 0; g < 4; ++g) { float d = 0.f;
#pragma unroll
        for (int k = 0; k < 4; ++k) d = dot2(Kc[t][k], qpk[g][k], d);
        x[t * 4 + g] = d; }
#define TR_STEP(HALF, CTRL, BIT) _Pragma("unroll") for (int h = 0; h < (HALF); ++h) { \
      const float lo_ = x[h], hi_ = x[h + (HALF)]; const float t1_ = lo_ + dpp<CTRL>(lo_), t2_ = hi_ + dpp<CTRL>(hi_); \
      x[h] = (c & (BIT)) ? t2_ : t1_; }
    TR_STEP(8, XOR8, 8) TR_STEP(4, XOR7, 4) TR_STEP(2, XOR2, 2) TR_STEP(1, XOR1, 1)
    const float sc = x[0] * C2;
    float pm = sc; pm = fmaxf(pm, dpp<ROR4>(pm)); pm = fmaxf(pm, dpp<XOR8>(pm)); pm = xrow16_max(pm);
    const float mn = fmaxf(m, pm);
    const float alpha = __builtin_amdgcn_exp2f(m - mn), p = __builtin_amdgcn_exp2f(sc - mn);
    l = fmaf(l, alpha, p); m = mn;
#pragma unroll
    for (int q4 = 0; q4 < 4; ++q4) {
      const float al = q4 == 0 ? dpp<0x00>(alpha) : q4 == 1 ? dpp<0x55>(alpha) : q4 == 2 ? dpp<0xAA>(alpha) : dpp<0xFF>(alpha);
#pragma unroll
      for (int k = 0; k < 8; ++k) acc[q4 * 8 + k] *= al; }
    float* prow = lds + (wave * 4 + r) * 16;
    prow[c] = p;
    asm volatile("s_waitcnt lgkmcnt(0)" ::: "memory");
    f32x4 pr[4];
#pragma unroll
    for (int u = 0; u < 4; ++u) pr[u] = *reinterpret_cast<const f32x4*>(prow + 4 * u);
#pragma unroll
    for (int t = 0; t < 4; ++t) {
      float vf[8];
#pragma unroll
      for (int k = 0; k < 4; ++k) { vf[2 * k] = __builtin_bit_cast(float, Vc[t][k] << 16); vf[2 * k + 1] = __builtin_bit_cast(float, Vc[t][k] & 0xffff0000u); }
#pragma unroll
      for (int g = 0; g < 4; ++g) { const float pg_ = pr[t][g];
#pragma unroll
        for (int k = 0; k < 8; ++k) acc[g * 8 + k] = fmaf(pg_, vf[k], acc[g * 8 + k]); } }
  }
#pragma unroll
  for (int k = 0; k < 32; ++k) acc[k] = xrow16_sum(acc[k]);
  { float t = l; t += dpp<ROR4>(t); t += dpp<XOR8>(t); l = xrow16_sum(t); }
  float* mrg = lds + 256;
  if (r == 0) {
#pragma unroll
    for (int g = 0; g < 4; ++g) {
      float* rec = mrg + (wave * 4 + g) * MRG_REC;
#pragma unroll
      for (int k = 0; k < 8; k += 4) *reinterpret_cast<f32x4*>(rec + 4 + 8 * c + k) = f32x4{acc[g * 8 + k], acc[g * 8 + k + 1], acc[g * 8 + k + 2], acc[g * 8 + k + 3]};
      if (c == g) { rec[0] = m; rec[1] = l; } } }
  __syncthreads();
  {
    const int g = wave, d0 = 2 * lane;
    const size_t row = (size_t)b * HQ + kvh * 4 + g;
    float M = -INFINITY;
#pragma unroll
    for (int w = 0; w < WAVES; ++w) M = fmaxf(M, mrg[(w * 4 + g) * MRG_REC]);
    float L = 0.f, o0 = 0.f, o1 = 0.f;
#pragma unroll
    for (int w = 0; w < WAVES; ++w) { const float* rec = mrg + (w * 4 + g) * MRG_REC; const float lw = rec[1];
      const float wt = __builtin_amdgcn_exp2f(rec[0] - M);
      L = fmaf(wt, lw, L); o0 = fmaf(wt, rec[4 + d0], o0); o1 = fmaf(wt, rec[4 + d0 + 1], o1); }
    const size_t slot = row * NSPLIT + s;
    *reinterpret_cast<float2*>(po + slot * DH + d0) = make_float2(o0, o1);
    if (lane == 0) *reinterpret_cast<float2*>(pml + 2 * slot) = make_float2(M, L);
  }
}

__global__ void __launch_bounds__(64) k_comb(const float* pml_, const float* po_, float* o) {
  const size_t row = blockIdx.x;
  const float* pml = pml_ + row * NSPLIT * 2; const float* po = po_ + row * NSPLIT * DH;
  const int d0 = 2 * threadIdx.x;
  float M = -INFINITY;
  for (int s = 0; s < NSPLIT; ++s) M = fmaxf(M, pml[2 * s]);
  float L = 0.f, o0 = 0.f, o1 = 0.f;
  for (int s = 0; s < NSPLIT; ++s) {
    const float ls = pml[2 * s + 1], w = __builtin_amdgcn_exp2f(pml[2 * s] - M);
    const float2 a = *reinterpret_cast<const float2*>(po + (size_t)s * DH + d0);
    L = fmaf(w, ls, L); o0 = fmaf(w, a.x, o0); o1 = fmaf(w, a.y, o1);
  }
  const float inv = 1.f / L;
  o[row * DH + d0] = rbf(o0 * inv); o[row * DH + d0 + 1] = rbf(o1 * inv);
}

__global__ void __launch_bounds__(256) k_out(const u16* x, const float* o, const float* h, const u16* wo, const u16* w2, u16* out) {
  __shared__ float red[4][32];
  const int tid = threadIdx.x, c0 = blockIdx.x * 8;
  float s[2][8], gg[2][8];
#pragma unroll
  for (int j = 0; j < 8; ++j) { s[0][j] = s[1][j] = gg[0][j] = gg[1][j] = 0.f; }
#pragma unroll 4
  for (int k = tid; k < 4096; k += 256) {
    u32x4 w = *reinterpret_cast<const u32x4*>(wo + (size_t)k * D + c0);
    float a = o[k], bb = o[4096 + k];
#pragma unroll
    for (int j = 0; j < 4; ++j) { float lo = __uint_as_float(w[j] << 16), hi = __uint_as_float(w[j] & 0xffff0000u);
      s[0][2 * j] += a * lo; s[0][2 * j + 1] += a * hi; s[1][2 * j] += bb * lo; s[1][2 * j + 1] += bb * hi; }
  }
  for (int k = tid; k < F; k += 256) {
    u32x4 w = *reinterpret_cast<const u32x4*>(w2 + (size_t)k * D + c0);
    float a = h[k], bb = h[F + k];
#pragma unroll
    for (int j = 0; j < 4; ++j) { float lo = __uint_as_float(w[j] << 16), hi = __uint_as_float(w[j] & 0xffff0000u);
      gg[0][2 * j] += a * lo; gg[0][2 * j + 1] += a * hi; gg[1][2 * j] += bb * lo; gg[1][2 * j + 1] += bb * hi; }
  }
  float v[32];
#pragma unroll
  for (int j = 0; j < 8; ++j) { v[j] = s[0][j]; v[8 + j] = s[1][j]; v[16 + j] = gg[0][j]; v[24 + j] = gg[1][j]; }
#pragma unroll
  for (int j = 0; j < 32; ++j) { float t = v[j]; for (int of = 32; of; of >>= 1) t += __shfl_xor(t, of); if ((tid & 63) == 0) red[tid >> 6][j] = t; }
  __syncthreads();
  if (tid < 16) {
    float sv = rbf(red[0][tid] + red[1][tid] + red[2][tid] + red[3][tid]);
    float gv = rbf(red[0][16 + tid] + red[1][16 + tid] + red[2][16 + tid] + red[3][16 + tid]);
    float g = rbf(gv / (1.f + __expf(-gv)));
    int b = tid >> 3, col = c0 + (tid & 7);
    out[b * D + col] = f2bf(bf2f(x[b * D + col]) + rbf(sv * g));
  }
}

extern "C" void kernel_launch(void* const* d_in, const int* in_sizes, int n_in, void* d_out, int out_size, void* d_ws, size_t ws_size, hipStream_t stream) {
  const u16* x = (const u16*)d_in[0]; const u16* kc = (const u16*)d_in[1]; const u16* vc = (const u16*)d_in[2];
  const u16* wq = (const u16*)d_in[3]; const u16* wo = (const u16*)d_in[4]; const u16* w1 = (const u16*)d_in[6]; const u16* w2 = (const u16*)d_in[7];
  char* ws = (char*)d_ws;
  u16* q = (u16*)ws; float* h = (float*)(ws + 32768); float* o = (float*)(ws + 65536);
  float* pml = (float*)(ws + 131072); float* po = (float*)(ws + 262144);
  k_pro<<<64 + 28, 256, 0, stream>>>(x, wq, w1, q, h);
  k_attn<<<dim3(NSPLIT, HKV, B), 256, 0, stream>>>(q, kc, vc, pml, po);
  k_comb<<<B * HQ, 64, 0, stream>>>(pml, po, o);
  k_out<<<64, 256, 0, stream>>>(x, o, h, wo, w2, (u16*)d_out);
}
```

```cpp
#include <hip/hip_runtime.h>
#include <hip/hip_bf16.h>

#define STAMPS 0
#define DEPTH 4
#define HEAD_DEPTH 2
#define W_NT 1

namespace {

typedef unsigned short u16;
typedef short s16x4 __attribute__((ext_vector_type(4)));
typedef __bf16 bf16x8 __attribute__((ext_vector_type(8)));
typedef __bf16 bf16x2 __attribute__((ext_vector_type(2)));
typedef float f32x2 __attribute__((ext_vector_type(2)));
typedef float f32x4 __attribute__((ext_vector_type(4)));
typedef unsigned u32x2 __attribute__((ext_vector_type(2)));
typedef unsigned u32x4 __attribute__((ext_vector_type(4)));
typedef __amdgpu_buffer_rsrc_t rsrc_t;
#define LAS __attribute__((address_space(3)))
#define GAS __attribute__((address_space(1)))

constexpr int NWG = 256, NTHR = 320;
constexpr int DM = 512, HQD = 4096, SK = 32768, DH = 128, FF = 1792;
constexpr int NSLOT = 4, NSTEP = 32;
constexpr int KV_BYTES = 2 * 8 * SK * DH * 2;
constexpr int NFFN = FF / 16;
constexpr float SCALE = 0.08838834764831845f, L2E = 1.4426950408889634f;
constexpr float M_INIT = -3.0e38f;
constexpr unsigned SPIN_CAP = 1u << 18;

constexpr int WS_EPOCH = 0;
constexpr int WS_Q = 4096;
constexpr int WS_ML = WS_Q + HQD * 8;
constexpr int WS_P1 = 65536;
constexpr int WS_P2 = WS_P1 + NWG * 4096;
constexpr int WS_FG = WS_P2 + 64 * 8192;
constexpr int WS_END = WS_FG + NFFN * 8192;
constexpr int WS_STAMP = 8 << 20;

constexpr int L_RING = 0;
constexpr int L_STG = 131072;
constexpr int L_XN = L_STG + 16384;
constexpr int L_QI = L_XN + 2048;
constexpr int L_OS = L_QI + 1024;
constexpr int L_HS = L_OS + 256;
constexpr int L_ML = L_HS + 64;
constexpr int L_TAG = L_ML + 128;
constexpr int L_END = L_TAG + 16;

__device__ __forceinline__ float rbf(float f) { return (float)(__bf16)f; }
__device__ __forceinline__ unsigned pk2(float lo, float hi) {
  bf16x2 v = {(__bf16)lo, (__bf16)hi};
  return __builtin_bit_cast(unsigned, v);
}
__device__ __forceinline__ float bflo(unsigned u) { return __uint_as_float(u << 16); }
__device__ __forceinline__ float bfhi(unsigned u) { return __uint_as_float(u & 0xffff0000u); }
template <int CTRL> __device__ __forceinline__ float dpp(float x) {
  return __builtin_bit_cast(float, __builtin_amdgcn_mov_dpp(__builtin_bit_cast(int, x), CTRL, 0xf, 0xf, true));
}
constexpr int XOR1 = 0xB1, XOR2 = 0x4E, ROR4 = 0x124, ROR8 = 0x128;
__device__ __forceinline__ float swap16_sum(float a, float b) {
  auto s = __builtin_amdgcn_permlane16_swap(__float_as_uint(a), __float_as_uint(b), false, false);
  return __uint_as_float(s[0]) + __uint_as_float(s[1]);
}
__device__ __forceinline__ float swap32_sum(float a, float b) {
  auto s = __builtin_amdgcn_permlane32_swap(__float_as_uint(a), __float_as_uint(b), false, false);
  return __uint_as_float(s[0]) + __uint_as_float(s[1]);
}
__device__ __forceinline__ float rows_sum(float x) { x = swap16_sum(x, x); return swap32_sum(x, x); }
__device__ __forceinline__ float rows_max(float x) {
  auto s = __builtin_amdgcn_permlane16_swap(__float_as_uint(x), __float_as_uint(x), false, false);
  x = fmaxf(__uint_as_float(s[0]), __uint_as_float(s[1]));
  auto t = __builtin_amdgcn_permlane32_swap(__float_as_uint(x), __float_as_uint(x), false, false);
  return fmaxf(__uint_as_float(t[0]), __uint_as_float(t[1]));
}
__device__ __forceinline__ float quads_sum(float x) { x += dpp<ROR4>(x); x += dpp<ROR8>(x); return rows_sum(x); }
__device__ __forceinline__ float quads_max(float x) { x = fmaxf(x, dpp<ROR4>(x)); x = fmaxf(x, dpp<ROR8>(x)); return rows_max(x); }
__device__ __forceinline__ float wave_sum(float x) { x += dpp<XOR1>(x); x += dpp<XOR2>(x); return quads_sum(x); }
__device__ __forceinline__ float ex2(float x) { return __builtin_amdgcn_exp2f(x); }
__device__ __forceinline__ float silu(float v) { return v * __builtin_amdgcn_rcpf(1.f + ex2(-v * L2E)); }

template <int N> __device__ __forceinline__ void wait_vm() {
  __builtin_amdgcn_sched_barrier(0);
  asm volatile("s_waitcnt vmcnt(%0)" ::"i"(N) : "memory");
}
__device__ __forceinline__ void wait_lds() { asm volatile("s_waitcnt lgkmcnt(0)" ::: "memory"); }
__device__ __forceinline__ void wg_barrier() {
  asm volatile("s_waitcnt lgkmcnt(0)\n\ts_barrier" ::: "memory");
}
__device__ __forceinline__ rsrc_t make_rsrc(const void* p, int bytes) {
  return __builtin_amdgcn_make_buffer_rsrc((void*)p, (short)0, bytes, 0x00020000);
}
__device__ __forceinline__ u32x4 ld_dev(rsrc_t r, unsigned voff, unsigned soff) { return __builtin_amdgcn_raw_buffer_load_b128(r, voff, soff, 16); }
__device__ __forceinline__ void st_dev(rsrc_t r, u32x4 v, unsigned voff, unsigned soff) { __builtin_amdgcn_raw_buffer_store_b128(v, r, voff, soff, 16); }
__device__ __forceinline__ s16x4 tr_read(unsigned addr) { return __builtin_amdgcn_ds_read_tr16_b64_v4i16((LAS s16x4*)(size_t)addr); }
template <typename T> __device__ __forceinline__ T lds_ld(unsigned addr) { return *(const LAS T*)(size_t)addr; }
template <typename T> __device__ __forceinline__ void lds_st(unsigned addr, T v) { *(LAS T*)(size_t)addr = v; }

#define W_POLICY " nt"
__device__ __forceinline__ void dma_piece(rsrc_t r, unsigned voff, unsigned soff, unsigned dst) {
  asm volatile("s_mov_b32 m0, %0\n\ts_nop 0\n\tbuffer_load_dwordx4 %1, %2, %3 offen" W_POLICY " lds" ::"s"(dst), "v"(voff), "s"(r), "s"(soff) : "memory");
}
__device__ __forceinline__ void dma_tile(rsrc_t rk, rsrc_t rv, const unsigned (&vk)[4], unsigned vv, unsigned soff, unsigned dst) {
  asm volatile(
      "s_mov_b32 m0, %0\n\ts_nop 0\n\t"
      "buffer_load_dwordx4 %1, %6, %8 offen nt lds\n\t"
      "buffer_load_dwordx4 %2, %6, %8 offen offset:1024 nt lds\n\t"
      "buffer_load_dwordx4 %3, %6, %8 offen offset:2048 nt lds\n\t"
      "buffer_load_dwordx4 %4, %6, %8 offen offset:3072 nt lds\n\t"
      "s_add_u32 m0, %0, 4096\n\ts_nop 0\n\t"
      "buffer_load_dwordx4 %5, %7, %8 offen nt lds\n\t"
      "buffer_load_dwordx4 %5, %7, %8 offen offset:1024 nt lds\n\t"
      "buffer_load_dwordx4 %5, %7, %8 offen offset:2048 nt lds\n\t"
      "buffer_load_dwordx4 %5, %7, %8 offen offset:3072 nt lds"
      ::"s"(dst), "v"(vk[0]), "v"(vk[1]), "v"(vk[2]), "v"(vk[3]), "v"(vv), "s"(rk), "s"(rv), "s"(soff)
      : "memory", "scc");
}

__device__ __forceinline__ f32x4 proj16(unsigned lds0, int lane) {
  const unsigned aa = lds0 + L_XN + (lane & 1) * 1024 + (lane >> 4) * 16;
  const unsigned ba = lds0 + L_STG + ((lane >> 4) * 8 + ((lane & 15) >> 2)) * 32 + (lane & 3) * 8;
  f32x4 acc = {0.f, 0.f, 0.f, 0.f};
#pragma unroll
  for (int s = 0; s < 16; ++s) {
    const bf16x8 a = lds_ld<bf16x8>(aa + s * 64);
    const s16x4 b0 = tr_read(ba + s * 1024), b1 = tr_read(ba + s * 1024 + 128);
    const bf16x8 b = __builtin_bit_cast(bf16x8, __builtin_shufflevector(b0, b1, 0, 1, 2, 3, 4, 5, 6, 7));
    acc = __builtin_amdgcn_mfma_f32_16x16x32_bf16(a, b, acc, 0, 0, 0);
  }
  return acc;
}

struct Tile { bf16x8 k[4]; s16x4 v[4][2]; };
struct State { float m, l; f32x4 o[2]; };

__device__ __forceinline__ void read_tile(Tile& t, const unsigned (&ka)[4], const unsigned (&va)[2], int off) {
#pragma unroll
  for (int s = 0; s < 4; ++s) t.k[s] = lds_ld<bf16x8>(ka[s] + off);
#pragma unroll
  for (int a = 0; a < 4; ++a)
#pragma unroll
    for (int h = 0; h < 2; ++h) t.v[a][h] = tr_read(va[h] + off + 4096 + a * 1024);
  asm volatile("s_waitcnt lgkmcnt(0)"
               : "+v"(t.k[0]), "+v"(t.k[1]), "+v"(t.k[2]), "+v"(t.k[3]), "+v"(t.v[0][0]), "+v"(t.v[0][1]), "+v"(t.v[1][0]), "+v"(t.v[1][1]),
                 "+v"(t.v[2][0]), "+v"(t.v[2][1]), "+v"(t.v[3][0]), "+v"(t.v[3][1])
               :
               : "memory");
}

__device__ __forceinline__ void compute_tile(const Tile& t, const bf16x8 (&qf)[4], State& st) {
  f32x4 sc = {0.f, 0.f, 0.f, 0.f};
#pragma unroll
  for (int s = 0; s < 4; ++s) sc = __builtin_amdgcn_mfma_f32_16x16x32_bf16(t.k[s], qf[s], sc, 0, 0, 0);
  float x[4];
#pragma unroll
  for (int r = 0; r < 4; ++r) x[r] = rbf(rbf(sc[r]) * SCALE);
  const float mn = fmaxf(st.m, rows_max(fmaxf(fmaxf(x[0], x[1]), fmaxf(x[2], x[3]))));
  const float alpha = ex2((st.m - mn) * L2E);
  st.m = mn;
  float p[4];
#pragma unroll
  for (int r = 0; r < 4; ++r) p[r] = ex2((x[r] - mn) * L2E);
  st.l = fmaf(st.l, alpha, (p[0] + p[1]) + (p[2] + p[3]));
  const u32x2 pp = {pk2(p[0], p[1]), pk2(p[2], p[3])};
  const s16x4 pa = __builtin_bit_cast(s16x4, pp);
  const float a0 = dpp<0x00>(alpha), a1 = dpp<0x55>(alpha), a2 = dpp<0xAA>(alpha), a3 = dpp<0xFF>(alpha);
#pragma unroll
  for (int h = 0; h < 2; ++h) { st.o[h][0] *= a0; st.o[h][1] *= a1; st.o[h][2] *= a2; st.o[h][3] *= a3; }
#pragma unroll
  for (int h = 0; h < 2; ++h) st.o[h] = __builtin_amdgcn_mfma_f32_4x4x4bf16_1k(pa, t.v[0][h], st.o[h], 4, 0, 0);
#pragma unroll
  for (int h = 0; h < 2; ++h) st.o[h] = __builtin_amdgcn_mfma_f32_4x4x4bf16_1k(pa, t.v[1][h], st.o[h], 4, 4, 0);
#pragma unroll
  for (int h = 0; h < 2; ++h) st.o[h] = __builtin_amdgcn_mfma_f32_4x4x4bf16_1k(pa, t.v[2][h], st.o[h], 4, 8, 0);
#pragma unroll
  for (int h = 0; h < 2; ++h) st.o[h] = __builtin_amdgcn_mfma_f32_4x4x4bf16_1k(pa, t.v[3][h], st.o[h], 4, 12, 0);
}

template <int A> __device__ __forceinline__ f32x4 mfma4(s16x4 a, s16x4 b, f32x4 c) { return __builtin_amdgcn_mfma_f32_4x4x4bf16_1k(a, b, c, 4, A, 0); }

#define STAMP(i) do {} while (0)
#define STAMP_FLUSH(base, n) do {} while (0)

__global__ void __launch_bounds__(NTHR, 1)
fused_decode_kernel(const u16* __restrict__ x, const u16* __restrict__ kc, const u16* __restrict__ vc, const u16* __restrict__ wq,
                    const u16* __restrict__ wo, const u16* __restrict__ w1, const u16* __restrict__ w2, u16* __restrict__ out, char* ws) {
  extern __shared__ __attribute__((aligned(16))) unsigned char lds[];
  const unsigned lds0 = (unsigned)(size_t)(LAS unsigned char*)lds;
  const int lane = threadIdx.x & 63, wave = __builtin_amdgcn_readfirstlane(threadIdx.x >> 6), wg = blockIdx.x;
  const int xg = wg & 7, xi = wg >> 3;
  const rsrc_t rws = make_rsrc(ws, WS_END);
  STAMP(0);

  if (wave < 4) {
    const rsrc_t rk = make_rsrc(kc, KV_BYTES), rv = make_rsrc(vc, KV_BYTES);
    const int g = lane >> 4, c = lane & 15;
    unsigned vk[4];
#pragma unroll
    for (int u = 0; u < 4; ++u) vk[u] = g * 256 + ((c ^ (4 * u + g)) << 4);
    const unsigned vv = g * 256 + ((c ^ (g << 2)) << 4);
    const unsigned ring = lds0 + L_RING + wave * (NSLOT * 8192);
    const unsigned tile0 = (unsigned)wg * (2048 * 256) + wave * 4096;

#pragma unroll
    for (int t = 0; t < NSLOT; ++t) {
      if (t >= HEAD_DEPTH) wait_vm<(HEAD_DEPTH - 1) * 8>();
      dma_tile(rk, rv, vk, vv, tile0 + t * 16384, ring + t * 8192);
    }
    unsigned ka[4], va[2];
#pragma unroll
    for (int s = 0; s < 4; ++s) ka[s] = ring + c * 256 + (((4 * s + g) ^ c) << 4);
#pragma unroll
    for (int h = 0; h < 2; ++h) va[h] = ring + (c >> 2) * 256 + (((8 * h + 2 * g + ((c & 3) >> 1)) ^ ((c >> 2) << 2)) << 4) + (c & 1) * 8;
    STAMP(1);
    wg_barrier();
    STAMP(2);
    bf16x8 qf[4];
#pragma unroll
    for (int s = 0; s < 4; ++s) qf[s] = lds_ld<bf16x8>(lds0 + L_QI + (c & 3) * 256 + (4 * s + g) * 16);
    const unsigned tag = lds_ld<unsigned>(lds0 + L_TAG);

    State st;
    st.m = M_INIT; st.l = 0.f;
    st.o[0] = f32x4{0.f, 0.f, 0.f, 0.f}; st.o[1] = f32x4{0.f, 0.f, 0.f, 0.f};
    Tile tl;
    for (int t0 = 0; t0 < NSTEP - NSLOT; t0 += NSLOT) {
#pragma unroll
      for (int j = 0; j < NSLOT; ++j) {
        wait_vm<(DEPTH - 1) * 8>();
        read_tile(tl, ka, va, j * 8192);
        dma_tile(rk, rv, vk, vv, tile0 + (t0 + j + NSLOT) * 16384, ring + j * 8192);
        compute_tile(tl, qf, st);
      }
    }
    STAMP(3);
    wait_vm<(DEPTH < 4 ? DEPTH - 1 : 3) * 8>(); read_tile(tl, ka, va, 0);     compute_tile(tl, qf, st);
    wait_vm<(DEPTH < 3 ? DEPTH - 1 : 2) * 8>(); read_tile(tl, ka, va, 8192);  compute_tile(tl, qf, st);
    wait_vm<(DEPTH < 2 ? DEPTH - 1 : 1) * 8>(); read_tile(tl, ka, va, 16384); compute_tile(tl, qf, st);
    wait_vm<0>();
    STAMP(4);
    read_tile(tl, ka, va, 24576); compute_tile(tl, qf, st);

    const float lsum = rows_sum(st.l);
#pragma unroll
    for (int h = 0; h < 2; ++h)
#pragma unroll
      for (int r = 0; r < 4; ++r) lds_st<float>(ring + (r * 128 + h * 64 + lane) * 4, st.o[h][r]);
    if (lane < 4) lds_st<f32x2>(lds0 + L_ML + (wave * 4 + lane) * 8, f32x2{st.m, lsum});
    STAMP(5);
    wg_barrier();
    STAMP(6);
    {
      f32x2 ml[4], ov[4];
#pragma unroll
      for (int w = 0; w < 4; ++w) {
        ml[w] = lds_ld<f32x2>(lds0 + L_ML + (w * 4 + wave) * 8);
        ov[w] = lds_ld<f32x2>(lds0 + L_RING + w * (NSLOT * 8192) + (wave * 128 + 2 * lane) * 4);
      }
      const float M = fmaxf(fmaxf(ml[0][0], ml[1][0]), fmaxf(ml[2][0], ml[3][0]));
      float L = 0.f, o0 = 0.f, o1 = 0.f;
#pragma unroll
      for (int w = 0; w < 4; ++w) {
        const float wt = ex2((ml[w][0] - M) * L2E);
        L = fmaf(wt, ml[w][1], L); o0 = fmaf(wt, ov[w][0], o0); o1 = fmaf(wt, ov[w][1], o1);
      }
      st_dev(rws, u32x4{__float_as_uint(o0), tag, __float_as_uint(o1), tag}, lane * 16, WS_P1 + wg * 4096 + wave * 1024);
      if (lane == 0) st_dev(rws, u32x4{__float_as_uint(M), tag, __float_as_uint(L), tag}, 0, WS_ML + wg * 64 + wave * 16);
    }
    const int ks = xg * 8 + (xi >> 2), cs = xi & 3;
    s16x4 wf[16];
    if (wave < 2) {
      const unsigned wa = lds0 + L_STG + (c >> 2) * 256 + (((8 * wave + 2 * g + ((c & 3) >> 1)) ^ ((c >> 2) << 2)) << 4) + (c & 1) * 8;
#pragma unroll
      for (int a = 0; a < 16; ++a) wf[a] = tr_read(wa + a * 1024);
    }
    STAMP(7);
    {
      const int b = wave >> 1, half = wave & 1, sp = lane >> 2, p = lane & 3;
      const int src0 = (b * 8 + xg) * 16, hl = (ks >> 1) & 3;
      const unsigned vo = sp * 4096 + p * 16, so = WS_P1 + src0 * 4096 + hl * 1024 + ((ks & 1) * 64 + half * 32) * 8;
      const unsigned vm = sp * 64, sm = WS_ML + src0 * 64 + hl * 16;
      u32x4 gv[4], gm;
      for (unsigned spin = 0; spin < SPIN_CAP; ++spin) {
        asm volatile("" ::: "memory");
        gm = ld_dev(rws, vm, sm);
#pragma unroll
        for (int i = 0; i < 4; ++i) gv[i] = ld_dev(rws, vo + i * 64, so);
        bool ok = gm[1] == tag && gm[3] == tag;
#pragma unroll
        for (int i = 0; i < 4; ++i) ok = ok && gv[i][1] == tag && gv[i][3] == tag;
        if (__all(ok)) break;
      }
      STAMP(8);
      const float ms = __uint_as_float(gm[0]), M = quads_max(ms);
      const float wt = ex2((ms - M) * L2E);
      const float inv = __builtin_amdgcn_rcpf(quads_sum(wt * __uint_as_float(gm[2])));
      float e[2];
#pragma unroll
      for (int j = 0; j < 2; ++j) {
        const float u0 = swap32_sum(wt * __uint_as_float(gv[0][2 * j]), wt * __uint_as_float(gv[2][2 * j]));
        const float u1 = swap32_sum(wt * __uint_as_float(gv[1][2 * j]), wt * __uint_as_float(gv[3][2 * j]));
        float v = swap16_sum(u0, u1);
        v += dpp<ROR4>(v); v += dpp<ROR8>(v);
        e[j] = v * inv;
      }
      if ((c >> 2) == 0) lds_st<unsigned>(lds0 + L_OS + b * 128 + (half * 16 + g * 4 + p) * 4, pk2(e[0], e[1]));
    }
    wg_barrier();
    STAMP(9);
    if (wave < 2) {
      const s16x4 oa = lds_ld<s16x4>(lds0 + L_OS + (lane & 1) * 128 + (lane >> 2) * 8);
      f32x4 c0 = {0.f, 0.f, 0.f, 0.f}, c1 = {0.f, 0.f, 0.f, 0.f};
      c0 = mfma4<0>(oa, wf[0], c0);   c1 = mfma4<1>(oa, wf[1], c1);   c0 = mfma4<2>(oa, wf[2], c0);   c1 = mfma4<3>(oa, wf[3], c1);
      c0 = mfma4<4>(oa, wf[4], c0);   c1 = mfma4<5>(oa, wf[5], c1);   c0 = mfma4<6>(oa, wf[6], c0);   c1 = mfma4<7>(oa, wf[7], c1);
      c0 = mfma4<8>(oa, wf[8], c0);   c1 = mfma4<9>(oa, wf[9], c1);   c0 = mfma4<10>(oa, wf[10], c0); c1 = mfma4<11>(oa, wf[11], c1);
      c0 = mfma4<12>(oa, wf[12], c0); c1 = mfma4<13>(oa, wf[13], c1); c0 = mfma4<14>(oa, wf[14], c0); c1 = mfma4<15>(oa, wf[15], c1);
      st_dev(rws, u32x4{__float_as_uint(c0[0] + c1[0]), tag, __float_as_uint(c0[1] + c1[1]), tag}, lane * 16, WS_P2 + ks * 8192 + (cs * 128 + wave * 64) * 16);
    }
    STAMP(10);
    STAMP_FLUSH(wave * 12, 12);
  } else {
    const int cg = xg * 32 + xi;
    const rsrc_t rx = make_rsrc(x, 2 * DM * 2), rq = make_rsrc(wq, DM * HQD * 2);
    unsigned ev, xo0, xo1;
    u32x4 x0, x1;
    asm volatile(
        "s_nop 4\n\t"
        "buffer_load_dwordx4 %0, %5, %6, 0 offen\n\t"
        "buffer_load_dwordx4 %1, %5, %6, 0 offen offset:1024\n\t"
        "buffer_load_dword %2, off, %7, 0 sc1\n\t"
        "buffer_load_ushort %3, %8, %6, 0 offen\n\t"
        "buffer_load_ushort %4, %8, %6, 0 offen offset:1024"
        : "=&v"(x0), "=&v"(x1), "=&v"(ev), "=&v"(xo0), "=&v"(xo1)
        : "v"(lane * 16), "s"(rx), "s"(rws), "v"((cg * 2 + (lane & 1)) * 2)
        : "memory");
    {
      const unsigned vo = (lane >> 1) * (HQD * 2) + (lane & 1) * 16;
#pragma unroll
      for (int i = 0; i < 16; ++i) dma_piece(rq, vo, cg * 32 + i * 32 * (HQD * 2), lds0 + L_STG + i * 1024);
    }
    asm volatile("s_waitcnt vmcnt(16)" : "+v"(x0), "+v"(x1), "+v"(ev), "+v"(xo0), "+v"(xo1)::"memory");
    STAMP(1);
    const unsigned tag = __builtin_amdgcn_readfirstlane(ev) + 1u;
    {
      float a[2][8];
#pragma unroll
      for (int j = 0; j < 4; ++j) { a[0][2 * j] = bflo(x0[j]); a[0][2 * j + 1] = bfhi(x0[j]); a[1][2 * j] = bflo(x1[j]); a[1][2 * j + 1] = bfhi(x1[j]); }
#pragma unroll
      for (int b = 0; b < 2; ++b) {
        const float mean = wave_sum(((a[b][0] + a[b][1]) + (a[b][2] + a[b][3])) + ((a[b][4] + a[b][5]) + (a[b][6] + a[b][7]))) * (1.f / DM);
        float q = 0.f;
#pragma unroll
        for (int j = 0; j < 8; ++j) q = fmaf(a[b][j] - mean, a[b][j] - mean, q);
        const float var = wave_sum(q) * (1.f / DM);
        const float mb = rbf(mean), inv = __builtin_amdgcn_rcpf(rbf(__builtin_amdgcn_sqrtf(rbf(rbf(var) + 1e-5f))));
        u32x4 xn;
#pragma unroll
        for (int j = 0; j < 4; ++j) xn[j] = pk2(rbf(a[b][2 * j] - mb) * inv, rbf(a[b][2 * j + 1] - mb) * inv);
        lds_st<u32x4>(lds0 + L_XN + b * 1024 + lane * 16, xn);
      }
    }
    if (lane == 0) lds_st<unsigned>(lds0 + L_TAG, tag);
    asm volatile("s_waitcnt vmcnt(0) lgkmcnt(0)" ::: "memory");
    STAMP(2);
    {
      const f32x4 q = proj16(lds0, lane);
      if (lane < 16) __builtin_amdgcn_raw_buffer_store_b64(u32x2{pk2(q[0], q[1]), tag}, rws, lane * 8, WS_Q + cg * 128, 16);
    }
    STAMP(3);
    {
      const int bh = wg >> 4, b = bh >> 3, kvh = bh & 7;
      u32x4 gq[4];
      for (unsigned spin = 0; spin < SPIN_CAP; ++spin) {
        asm volatile("" ::: "memory");
#pragma unroll
        for (int i = 0; i < 4; ++i) gq[i] = ld_dev(rws, lane * 16 + i * 1024, WS_Q + kvh * 4096);
        bool ok = true;
#pragma unroll
        for (int i = 0; i < 4; ++i) ok = ok && gq[i][1] == tag && gq[i][3] == tag;
        if (__all(ok)) break;
      }
#pragma unroll
      for (int i = 0; i < 4; ++i)
        lds_st<unsigned>(lds0 + L_QI + i * 256 + lane * 4, b ? (gq[i][0] >> 16) | (gq[i][2] & 0xffff0000u) : (gq[i][0] & 0xffffu) | (gq[i][2] << 16));
    }
    STAMP(4);
    wg_barrier();

    if (xi < 14) {
      const int fi = xg * 14 + xi;
      const rsrc_t r1 = make_rsrc(w1, DM * FF * 2), r2 = make_rsrc(w2, FF * DM * 2);
      const unsigned vo = (lane >> 1) * (FF * 2) + (lane & 1) * 16;
#pragma unroll
      for (int i = 0; i < 16; ++i) dma_piece(r1, vo, fi * 32 + i * 32 * (FF * 2), lds0 + L_STG + i * 1024);
      wait_vm<0>();
      const f32x4 hv = proj16(lds0, lane);
      if (lane < 16) {
        lds_st<__bf16>(lds0 + L_HS + lane * 2, (__bf16)silu(rbf(hv[0])));
        lds_st<__bf16>(lds0 + L_HS + 32 + lane * 2, (__bf16)silu(rbf(hv[1])));
      }
      wait_lds();
#pragma unroll
      for (int i = 0; i < 16; ++i) dma_piece(r2, lane * 16, (fi * 16 + i) * (DM * 2), lds0 + L_STG + i * 1024);
      wait_vm<0>();
      const s16x4 ha = lds_ld<s16x4>(lds0 + L_HS + (lane & 1) * 32 + ((lane >> 2) & 3) * 8);
      const unsigned wa = lds0 + L_STG + ((lane & 15) >> 2) * 1024 + (lane >> 4) * 32 + (lane & 3) * 8;
#pragma unroll
      for (int cb = 0; cb < 8; ++cb) {
        f32x4 acc = {0.f, 0.f, 0.f, 0.f};
        acc = mfma4<0>(ha, tr_read(wa + cb * 128), acc);
        acc = mfma4<1>(ha, tr_read(wa + cb * 128 + 4096), acc);
        acc = mfma4<2>(ha, tr_read(wa + cb * 128 + 8192), acc);
        acc = mfma4<3>(ha, tr_read(wa + cb * 128 + 12288), acc);
        st_dev(rws, u32x4{__float_as_uint(acc[0]), tag, __float_as_uint(acc[1]), tag}, lane * 16, WS_FG + fi * 8192 + cb * 1024);
      }
      wait_lds();
    }
    STAMP(5);
    {
      const int ks = xg * 8 + (xi >> 2), cs = xi & 3;
      const rsrc_t ro = make_rsrc(wo, HQD * DM * 2);
      const unsigned vo = (lane >> 4) * (DM * 2) + (((lane & 15) ^ ((lane >> 4) << 2)) << 4);
#pragma unroll
      for (int i = 0; i < 16; ++i) dma_piece(ro, vo, (ks * 64 + 4 * i) * (DM * 2) + cs * 256, lds0 + L_STG + i * 1024);
      wait_vm<0>();
    }
    STAMP(6);
    float g0, g1;
    {
      u32x4 gf[4];
      const int pr = lane >> 1;
      const unsigned vo = pr * 8192 + (lane & 1) * 16, vo3 = (pr < 16 ? pr : 0) * 8192 + (lane & 1) * 16;
      for (unsigned spin = 0; spin < SPIN_CAP; ++spin) {
        asm volatile("" ::: "memory");
#pragma unroll
        for (int i = 0; i < 3; ++i) gf[i] = ld_dev(rws, vo, WS_FG + i * 32 * 8192 + cg * 32);
        gf[3] = ld_dev(rws, vo3, WS_FG + 96 * 8192 + cg * 32);
        bool ok = true;
#pragma unroll
        for (int i = 0; i < 4; ++i) ok = ok && gf[i][1] == tag && gf[i][3] == tag;
        if (__all(ok)) break;
        __builtin_amdgcn_s_sleep(32);
      }
      float s0 = (__uint_as_float(gf[0][0]) + __uint_as_float(gf[1][0])) + (__uint_as_float(gf[2][0]) + (pr < 16 ? __uint_as_float(gf[3][0]) : 0.f));
      float s1 = (__uint_as_float(gf[0][2]) + __uint_as_float(gf[1][2])) + (__uint_as_float(gf[2][2]) + (pr < 16 ? __uint_as_float(gf[3][2]) : 0.f));
      s0 += dpp<XOR2>(s0); s1 += dpp<XOR2>(s1);
      g0 = rbf(silu(rbf(quads_sum(s0))));
      g1 = rbf(silu(rbf(quads_sum(s1))));
    }
    STAMP(7);
    wg_barrier();
    wg_barrier();
    STAMP(8);
    {
      u32x4 gs[2];
      const unsigned vo = (lane >> 1) * 8192 + (lane & 1) * 16;
      for (unsigned spin = 0; spin < SPIN_CAP; ++spin) {
        asm volatile("" ::: "memory");
#pragma unroll
        for (int i = 0; i < 2; ++i) gs[i] = ld_dev(rws, vo, WS_P2 + i * 32 * 8192 + cg * 32);
        if (__all(gs[0][1] == tag && gs[0][3] == tag && gs[1][1] == tag && gs[1][3] == tag)) break;
      }
      STAMP(9);
      float s0 = __uint_as_float(gs[0][0]) + __uint_as_float(gs[1][0]), s1 = __uint_as_float(gs[0][2]) + __uint_as_float(gs[1][2]);
      s0 += dpp<XOR2>(s0); s1 += dpp<XOR2>(s1);
      s0 = rbf(quads_sum(s0)); s1 = rbf(quads_sum(s1));
      if (lane < 2) {
        out[cg * 2 + lane] = __builtin_bit_cast(u16, (__bf16)(bflo(xo0) + rbf(s0 * g0)));
        out[DM + cg * 2 + lane] = __builtin_bit_cast(u16, (__bf16)(bflo(xo1) + rbf(s1 * g1)));
      }
      if (wg == 0 && lane == 0) __builtin_amdgcn_raw_buffer_store_b32(tag, rws, 0, WS_EPOCH, 16);
    }
    STAMP(10);
    STAMP_FLUSH(48, 12);
  }
}

}

extern "C" void kernel_launch(void* const* d_in, const int* in_sizes, int n_in, void* d_out, int out_size, void* d_ws, size_t ws_size,
                              hipStream_t stream) {
  (void)hipFuncSetAttribute((const void*)fused_decode_kernel, hipFuncAttributeMaxDynamicSharedMemorySize, L_END);
  fused_decode_kernel<<<NWG, NTHR, L_END, stream>>>((const u16*)d_in[0], (const u16*)d_in[1], (const u16*)d_in[2], (const u16*)d_in[3],
                                                    (const u16*)d_in[4], (const u16*)d_in[6], (const u16*)d_in[7], (u16*)d_out, (char*)d_ws);
}
```

```cpp
#include <hip/hip_runtime.h>
#include <hip/hip_bf16.h>
#include <type_traits>

#define STAMPS 0
#define WIN 4
#define HEAD_WIN 4
#define HPRIO 3
#define QSLEEP 8
#define SPEC_TH 3
#define TSTAMP 0
#define PACE 1
#define CPA 7
#define CPB 11
#define PACE_OFF 100
#define WIN2 WIN
#define LD_MID 16
#define ST_MID 16
#define LD_END 16
#define ST_END 16
#define NO_FFN 0

namespace {

typedef unsigned short u16;
typedef short s16x4 __attribute__((ext_vector_type(4)));
typedef __bf16 bf16x8 __attribute__((ext_vector_type(8)));
typedef __bf16 bf16x2 __attribute__((ext_vector_type(2)));
typedef float f32x2 __attribute__((ext_vector_type(2)));
typedef float f32x4 __attribute__((ext_vector_type(4)));
typedef unsigned u32x2 __attribute__((ext_vector_type(2)));
typedef unsigned u32x4 __attribute__((ext_vector_type(4)));
typedef __amdgpu_buffer_rsrc_t rsrc_t;
#define LAS __attribute__((address_space(3)))
#define GAS __attribute__((address_space(1)))

constexpr int NWG = 256, NTHR = 320;
constexpr int DM = 512, HQD = 4096, SK = 32768, DH = 128, FF = 1792;
constexpr int NSLOT = 3, NSTEP = 32, NPRE = 5;
constexpr int KV_BYTES = 2 * 8 * SK * DH * 2;
constexpr float SCALE = 0.08838834764831845f, L2E = 1.4426950408889634f;
constexpr float M_INIT = -3.0e38f;
constexpr unsigned SPIN_CAP = 1u << 18;

constexpr int WS_EPOCH = 0;
constexpr int WS_Q = 4096;
constexpr int WS_H = WS_Q + HQD * 8;
constexpr int WS_ML = WS_H + FF * 8;
constexpr int WS_PC = WS_ML + NWG * 64;
constexpr int WS_QP = 131072;
constexpr int WS_HP = WS_QP + 4 * HQD * 16;
constexpr int WS_FG = WS_HP + 4 * FF * 16;
constexpr int WS_P2 = WS_FG + 14 * DM * 16;
constexpr int WS_P1 = WS_P2 + 64 * DM * 16;
constexpr int WS_END = WS_P1 + NWG * 4096;
constexpr int WS_STAMP = 8 << 20;
static_assert(WS_PC + 512 <= WS_QP, "workspace map");

constexpr int L_RING = 0;
constexpr int L_WQ = 4 * NSLOT * 8192;
constexpr int L_WF = L_WQ + 16384;
constexpr int L_WO = L_WF + 16384;
constexpr int L_XN = L_WO + 16384;
constexpr int L_QI = L_XN + 2048;
constexpr int L_OS = L_QI + 1024;
constexpr int L_HS = L_OS + 256;
constexpr int L_ML = L_HS + 512;
constexpr int L_TAG = L_ML + 128;
constexpr int L_TL = L_TAG + 16;
constexpr int L_TS = L_TL + 16;
constexpr int L_END = L_TS + 32;

__device__ __forceinline__ float rbf(float f) { return (float)(__bf16)f; }
__device__ __forceinline__ unsigned pk2(float lo, float hi) {
  bf16x2 v = {(__bf16)lo, (__bf16)hi};
  return __builtin_bit_cast(unsigned, v);
}
__device__ __forceinline__ float bflo(unsigned u) { return __uint_as_float(u << 16); }
__device__ __forceinline__ float bfhi(unsigned u) { return __uint_as_float(u & 0xffff0000u); }
template <int CTRL> __device__ __forceinline__ float dpp(float x) {
  return __builtin_bit_cast(float, __builtin_amdgcn_mov_dpp(__builtin_bit_cast(int, x), CTRL, 0xf, 0xf, true));
}
constexpr int XOR1 = 0xB1, XOR2 = 0x4E, ROR4 = 0x124, ROR8 = 0x128;
__device__ __forceinline__ float swap16_sum(float a, float b) {
  auto s = __builtin_amdgcn_permlane16_swap(__float_as_uint(a), __float_as_uint(b), false, false);
  return __uint_as_float(s[0]) + __uint_as_float(s[1]);
}
__device__ __forceinline__ float swap32_sum(float a, float b) {
  auto s = __builtin_amdgcn_permlane32_swap(__float_as_uint(a), __float_as_uint(b), false, false);
  return __uint_as_float(s[0]) + __uint_as_float(s[1]);
}
__device__ __forceinline__ float rows_sum(float x) { x = swap16_sum(x, x); return swap32_sum(x, x); }
__device__ __forceinline__ float rows_max(float x) {
  auto s = __builtin_amdgcn_permlane16_swap(__float_as_uint(x), __float_as_uint(x), false, false);
  x = fmaxf(__uint_as_float(s[0]), __uint_as_float(s[1]));
  auto t = __builtin_amdgcn_permlane32_swap(__float_as_uint(x), __float_as_uint(x), false, false);
  return fmaxf(__uint_as_float(t[0]), __uint_as_float(t[1]));
}
__device__ __forceinline__ float quads_sum(float x) { x += dpp<ROR4>(x); x += dpp<ROR8>(x); return rows_sum(x); }
__device__ __forceinline__ float quads_max(float x) { x = fmaxf(x, dpp<ROR4>(x)); x = fmaxf(x, dpp<ROR8>(x)); return rows_max(x); }
__device__ __forceinline__ float wave_sum(float x) { x += dpp<XOR1>(x); x += dpp<XOR2>(x); return quads_sum(x); }
__device__ __forceinline__ float ex2(float x) { return __builtin_amdgcn_exp2f(x); }
__device__ __forceinline__ float silu(float v) { return v * __builtin_amdgcn_rcpf(1.f + ex2(-v * L2E)); }

template <int N> __device__ __forceinline__ void wait_vm() {
  __builtin_amdgcn_sched_barrier(0);
  asm volatile("s_waitcnt vmcnt(%0)" ::"i"(N) : "memory");
}
__device__ __forceinline__ void wait_lds() { asm volatile("s_waitcnt lgkmcnt(0)" ::: "memory"); }
__device__ __forceinline__ void wg_barrier() {
  asm volatile("s_waitcnt lgkmcnt(0)\n\ts_barrier" ::: "memory");
}
__device__ __forceinline__ unsigned now32() { return (unsigned)__builtin_amdgcn_s_memrealtime(); }
__device__ __forceinline__ rsrc_t make_rsrc(const void* p, int bytes) {
  return __builtin_amdgcn_make_buffer_rsrc((void*)p, (short)0, bytes, 0x00020000);
}
template <int AUX = LD_MID> __device__ __forceinline__ u32x4 ld_dev(rsrc_t r, unsigned voff, unsigned soff) { return __builtin_amdgcn_raw_buffer_load_b128(r, voff, soff, AUX); }
template <int AUX = ST_MID> __device__ __forceinline__ void st_dev(rsrc_t r, u32x4 v, unsigned voff, unsigned soff) { __builtin_amdgcn_raw_buffer_store_b128(v, r, voff, soff, AUX); }
__device__ __forceinline__ s16x4 tr_read(unsigned addr) { return __builtin_amdgcn_ds_read_tr16_b64_v4i16((LAS s16x4*)(size_t)addr); }
template <typename T> __device__ __forceinline__ T lds_ld(unsigned addr) { return *(const LAS T*)(size_t)addr; }
template <typename T> __device__ __forceinline__ void lds_st(unsigned addr, T v) { *(LAS T*)(size_t)addr = v; }

__device__ __forceinline__ void dma_piece(rsrc_t r, unsigned voff, unsigned soff, unsigned dst) {
  asm volatile("s_mov_b32 m0, %0\n\ts_nop 0\n\tbuffer_load_dwordx4 %1, %2, %3 offen nt lds" ::"s"(dst), "v"(voff), "s"(r), "s"(soff) : "memory");
}
__device__ __forceinline__ void dma_half(rsrc_t r, unsigned v0, unsigned v1, unsigned v2, unsigned v3, unsigned soff, unsigned dst) {
  asm volatile(
      "s_mov_b32 m0, %0\n\ts_nop 0\n\t"
      "buffer_load_dwordx4 %1, %5, %6 offen nt lds\n\t"
      "buffer_load_dwordx4 %2, %5, %6 offen offset:1024 nt lds\n\t"
      "buffer_load_dwordx4 %3, %5, %6 offen offset:2048 nt lds\n\t"
      "buffer_load_dwordx4 %4, %5, %6 offen offset:3072 nt lds"
      ::"s"(dst), "v"(v0), "v"(v1), "v"(v2), "v"(v3), "s"(r), "s"(soff)
      : "memory");
}
template <int A> __device__ __forceinline__ f32x4 mfma4(s16x4 a, s16x4 b, f32x4 c) { return __builtin_amdgcn_mfma_f32_4x4x4bf16_1k(a, b, c, 4, A, 0); }
template <int A> struct Proj {
  static __device__ __forceinline__ void run(const s16x4 (&av)[2], unsigned ba, f32x4 (&c)[4]) {
    c[A & 3] = mfma4<A & 15>(av[A >> 4], tr_read(ba + A * 512), c[A & 3]);
    if constexpr (A + 1 < 32) Proj<A + 1>::run(av, ba, c);
  }
};
__device__ __forceinline__ f32x2 proj64(unsigned a_img, int a_stride, unsigned w_img, int lane) {
  const int q = (lane & 15) >> 2, p = lane & 3;
  const unsigned aa = a_img + (lane & 1) * a_stride + (lane >> 2) * 8;
  const s16x4 av[2] = {lds_ld<s16x4>(aa), lds_ld<s16x4>(aa + 128)};
  const unsigned ba = w_img + q * 128 + (((2 * (lane >> 4) + (p >> 1)) ^ ((q >> 1) << 2)) << 4) + (p & 1) * 8;
  f32x4 c[4] = {};
  Proj<0>::run(av, ba, c);
  return f32x2{(c[0][0] + c[1][0]) + (c[2][0] + c[3][0]), (c[0][1] + c[1][1]) + (c[2][1] + c[3][1])};
}
__device__ __forceinline__ f32x2 gather_quarters(rsrc_t rws, unsigned base, int quarter_bytes, unsigned tag, int lane) {
  u32x4 g;
  for (unsigned spin = 0; spin < SPIN_CAP; ++spin) {
    asm volatile("" ::: "memory");
    g = ld_dev(rws, (lane >> 4) * quarter_bytes + (lane & 15) * 16, base);
    if (__all((g[1] == tag) & (g[3] == tag))) break;
    __builtin_amdgcn_s_sleep(QSLEEP);
  }
  return f32x2{rows_sum(__uint_as_float(g[0])), rows_sum(__uint_as_float(g[2]))};
}

struct KFrag { bf16x8 k[4]; };
struct VFrag { s16x4 v[4][2]; };
struct State { float m, l; f32x4 o[2]; };

__device__ __forceinline__ void read_k(KFrag& t, const unsigned (&ka)[4], int off) {
#pragma unroll
  for (int s = 0; s < 4; ++s) t.k[s] = lds_ld<bf16x8>(ka[s] + off);
  asm volatile("s_waitcnt lgkmcnt(0)" : "+v"(t.k[0]), "+v"(t.k[1]), "+v"(t.k[2]), "+v"(t.k[3])::"memory");
}
__device__ __forceinline__ void read_v(VFrag& t, const unsigned (&va)[2], int off) {
#pragma unroll
  for (int a = 0; a < 4; ++a)
#pragma unroll
    for (int h = 0; h < 2; ++h) t.v[a][h] = tr_read(va[h] + off + a * 1024);
  asm volatile("s_waitcnt lgkmcnt(0)"
               : "+v"(t.v[0][0]), "+v"(t.v[0][1]), "+v"(t.v[1][0]), "+v"(t.v[1][1]), "+v"(t.v[2][0]), "+v"(t.v[2][1]), "+v"(t.v[3][0]), "+v"(t.v[3][1])
               :
               : "memory");
}
__device__ __forceinline__ s16x4 score_part(const KFrag& t, const bf16x8 (&qf)[4], State& st) {
  f32x4 sc = {0.f, 0.f, 0.f, 0.f};
#pragma unroll
  for (int s = 0; s < 4; ++s) sc = __builtin_amdgcn_mfma_f32_16x16x32_bf16(t.k[s], qf[s], sc, 0, 0, 0);
  float x[4];
#pragma unroll
  for (int r = 0; r < 4; ++r) x[r] = rbf(rbf(sc[r]) * SCALE);
  const float mn = fmaxf(st.m, rows_max(fmaxf(fmaxf(x[0], x[1]), fmaxf(x[2], x[3]))));
  const float alpha = ex2((st.m - mn) * L2E);
  st.m = mn;
  float p[4];
#pragma unroll
  for (int r = 0; r < 4; ++r) p[r] = ex2((x[r] - mn) * L2E);
  st.l = fmaf(st.l, alpha, (p[0] + p[1]) + (p[2] + p[3]));
  const float a0 = dpp<0x00>(alpha), a1 = dpp<0x55>(alpha), a2 = dpp<0xAA>(alpha), a3 = dpp<0xFF>(alpha);
#pragma unroll
  for (int h = 0; h < 2; ++h) { st.o[h][0] *= a0; st.o[h][1] *= a1; st.o[h][2] *= a2; st.o[h][3] *= a3; }
  const u32x2 pp = {pk2(p[0], p[1]), pk2(p[2], p[3])};
  return __builtin_bit_cast(s16x4, pp);
}
__device__ __forceinline__ void value_part(const VFrag& t, s16x4 pa, State& st) {
#pragma unroll
  for (int h = 0; h < 2; ++h) st.o[h] = mfma4<0>(pa, t.v[0][h], st.o[h]);
#pragma unroll
  for (int h = 0; h < 2; ++h) st.o[h] = mfma4<4>(pa, t.v[1][h], st.o[h]);
#pragma unroll
  for (int h = 0; h < 2; ++h) st.o[h] = mfma4<8>(pa, t.v[2][h], st.o[h]);
#pragma unroll
  for (int h = 0; h < 2; ++h) st.o[h] = mfma4<12>(pa, t.v[3][h], st.o[h]);
}

#define STAMP(i) do {} while (0)
#define STAMP_FLUSH(base, n) do {} while (0)

__global__ void __launch_bounds__(NTHR, 1)
fused_decode_kernel(const u16* __restrict__ x, const u16* __restrict__ kc, const u16* __restrict__ vc, const u16* __restrict__ wq,
                    const u16* __restrict__ wo, const u16* __restrict__ w1, const u16* __restrict__ w2, u16* __restrict__ out, char* ws) {
  extern __shared__ __attribute__((aligned(16))) unsigned char lds[];
  const unsigned lds0 = (unsigned)(size_t)(LAS unsigned char*)lds;
  const int lane = threadIdx.x & 63, wave = __builtin_amdgcn_readfirstlane(threadIdx.x >> 6), wg = blockIdx.x;
  const int xg = wg & 7, xi = wg >> 3;
  const rsrc_t rws = make_rsrc(ws, WS_END);
  STAMP(0);

  if (wave < 4) {
    const rsrc_t rk = make_rsrc(kc, KV_BYTES), rv = make_rsrc(vc, KV_BYTES);
    const int g = lane >> 4, c = lane & 15;
    unsigned vk[4];
#pragma unroll
    for (int u = 0; u < 4; ++u) vk[u] = g * 256 + ((c ^ (4 * u + g)) << 4);
    const unsigned vv = g * 256 + ((c ^ (g << 2)) << 4);
    const unsigned ring = lds0 + L_RING + wave * (NSLOT * 8192);
    const unsigned tile0 = (unsigned)wg * (2048 * 256) + wave * 4096;
    unsigned ka[4], va[2];
#pragma unroll
    for (int s = 0; s < 4; ++s) ka[s] = ring + c * 256 + (((4 * s + g) ^ c) << 4);
#pragma unroll
    for (int h = 0; h < 2; ++h) va[h] = ring + 4096 + (c >> 2) * 256 + (((8 * h + 2 * g + ((c & 3) >> 1)) ^ ((c >> 2) << 2)) << 4) + (c & 1) * 8;
    auto issue_k = [&](int t, int slot) { dma_half(rk, vk[0], vk[1], vk[2], vk[3], tile0 + t * 16384, ring + slot * 8192); };
    auto issue_v = [&](int t, int slot) { dma_half(rv, vv, vv, vv, vv, tile0 + t * 16384, ring + slot * 8192 + 4096); };

    wg_barrier();
#pragma unroll
    for (int t = 0; t < NSLOT; ++t) {
      wait_vm<HEAD_WIN>(); issue_k(t, t);
      wait_vm<HEAD_WIN>(); issue_v(t, t);
    }
    unsigned tsv = TSTAMP ? (unsigned)__builtin_amdgcn_s_memrealtime() : 0u;
    KFrag pk[NPRE]; VFrag pv[NPRE];
#pragma unroll
    for (int t = 0; t < NPRE; ++t) {
      wait_vm<HEAD_WIN>(); read_k(pk[t], ka, (t % NSLOT) * 8192); issue_k(t + NSLOT, t % NSLOT);
      wait_vm<HEAD_WIN>();
      if (TSTAMP) { const unsigned tt = (unsigned)__builtin_amdgcn_s_memrealtime(); tsv = lane == t + 2 ? tt : tsv; }
      read_v(pv[t], va, (t % NSLOT) * 8192); issue_v(t + NSLOT, t % NSLOT);
    }
    STAMP(1);
    wg_barrier();
    STAMP(2);
    bf16x8 qf[4];
#pragma unroll
    for (int s = 0; s < 4; ++s) qf[s] = lds_ld<bf16x8>(lds0 + L_QI + (c & 3) * 256 + (4 * s + g) * 16);
    const unsigned tag = lds_ld<unsigned>(lds0 + L_TAG);

    State st;
    st.m = M_INIT; st.l = 0.f;
    st.o[0] = f32x4{0.f, 0.f, 0.f, 0.f}; st.o[1] = f32x4{0.f, 0.f, 0.f, 0.f};
    unsigned prev = now32();
    auto pace = [&](int n) {
      if (PACE) {
        const unsigned tl = __builtin_amdgcn_readfirstlane(*(volatile LAS unsigned*)(size_t)(lds0 + L_TL));
        const int d = (int)(tl - prev);
        if (tl != 0 && d > 0) {
          const unsigned due = prev + (unsigned)__builtin_amdgcn_readfirstlane((int)((float)d * __builtin_amdgcn_rcpf((float)n)));
          while ((int)(now32() - due) < 0) __builtin_amdgcn_s_sleep(1);
        }
        prev = now32();
      }
    };
    auto step = [&](int t, int slot, auto win) {
      KFrag kf; VFrag vf;
      wait_vm<win.value>(); read_k(kf, ka, slot * 8192);
      pace(2 * (NSTEP - NSLOT - t)); issue_k(t + NSLOT, slot);
      const s16x4 pa = score_part(kf, qf, st);
      wait_vm<win.value>();
      if (TSTAMP) { const unsigned tt = (unsigned)__builtin_amdgcn_s_memrealtime(); tsv = lane == t + 2 ? tt : tsv; }
      if (PACE && (t == CPA || t == CPB)) lds_st<unsigned>(lds0 + L_TS + (t == CPB ? 16 : 0) + wave * 4, now32() | 1u);
      read_v(vf, va, slot * 8192);
      pace(2 * (NSTEP - NSLOT - t) - 1); issue_v(t + NSLOT, slot);
      value_part(vf, pa, st);
    };
    using W1 = std::integral_constant<int, WIN>;
    using W2 = std::integral_constant<int, WIN2>;
#pragma unroll
    for (int j = 0; j < 2 * NSLOT; ++j) {
      step(NPRE + j, (NPRE + j) % NSLOT, W1{});
      if (j < NPRE) value_part(pv[j], score_part(pk[j], qf, st), st);
    }
    static_assert(2 * NSLOT >= NPRE && (NSTEP - NPRE) % NSLOT == 0, "tile schedule");
#pragma unroll
    for (int j = 0; j < NSLOT; ++j) step(NPRE + 2 * NSLOT + j, (NPRE + j) % NSLOT, W1{});
    for (int t0 = NPRE + 3 * NSLOT; t0 < NSTEP - NSLOT; t0 += NSLOT) {
#pragma unroll
      for (int j = 0; j < NSLOT; ++j) step(t0 + j, (NPRE + j) % NSLOT, W2{});
    }
    STAMP(3);
#pragma unroll
    for (int j = 0; j < NSLOT; ++j) {
      KFrag kf; VFrag vf;
      if (j == 0) wait_vm<20>(); else if (j == 1) wait_vm<12>(); else wait_vm<4>();
      read_k(kf, ka, ((NPRE + j) % NSLOT) * 8192);
      const s16x4 pa = score_part(kf, qf, st);
      if (j == 0) wait_vm<16>(); else if (j == 1) wait_vm<8>(); else wait_vm<0>();
      if (j == NSLOT - 1) STAMP(4);
      if (TSTAMP && j == NSLOT - 1) { const unsigned tt = (unsigned)__builtin_amdgcn_s_memrealtime(); tsv = lane == 31 ? tt : tsv;
        unsigned xcc; asm volatile("s_getreg_b32 %0, hwreg(HW_REG_XCC_ID, 0, 4)" : "=s"(xcc)); tsv = lane == 32 ? xcc : tsv;
        ((GAS unsigned*)(ws + WS_STAMP + (1 << 20)))[(wg * 4 + wave) * 64 + lane] = tsv; }
      read_v(vf, va, ((NPRE + j) % NSLOT) * 8192);
      value_part(vf, pa, st);
    }

    const float lsum = rows_sum(st.l);
#pragma unroll
    for (int h = 0; h < 2; ++h)
#pragma unroll
      for (int r = 0; r < 4; ++r) lds_st<float>(ring + (r * 128 + h * 64 + lane) * 4, st.o[h][r]);
    if (lane < 4) lds_st<f32x2>(lds0 + L_ML + (wave * 4 + lane) * 8, f32x2{st.m, lsum});
    STAMP(5);
    wg_barrier();
    STAMP(6);
    {
      f32x2 ml[4], ov[4];
#pragma unroll
      for (int w = 0; w < 4; ++w) {
        ml[w] = lds_ld<f32x2>(lds0 + L_ML + (w * 4 + wave) * 8);
        ov[w] = lds_ld<f32x2>(lds0 + L_RING + w * (NSLOT * 8192) + (wave * 128 + 2 * lane) * 4);
      }
      const float M = fmaxf(fmaxf(ml[0][0], ml[1][0]), fmaxf(ml[2][0], ml[3][0]));
      float L = 0.f, o0 = 0.f, o1 = 0.f;
#pragma unroll
      for (int w = 0; w < 4; ++w) {
        const float wt = ex2((ml[w][0] - M) * L2E);
        L = fmaf(wt, ml[w][1], L); o0 = fmaf(wt, ov[w][0], o0); o1 = fmaf(wt, ov[w][1], o1);
      }
      st_dev<ST_END>(rws, u32x4{__float_as_uint(o0), tag, __float_as_uint(o1), tag}, lane * 16, WS_P1 + wg * 4096 + wave * 1024);
      if (lane == 0) st_dev<ST_END>(rws, u32x4{__float_as_uint(M), tag, __float_as_uint(L), tag}, 0, WS_ML + wg * 64 + wave * 16);
    }
    const int ks = xg * 8 + (xi >> 2), cs = xi & 3;
    s16x4 wf[16];
    if (wave < 2) {
      const unsigned wa = lds0 + L_WO + (c >> 2) * 256 + (((8 * wave + 2 * g + ((c & 3) >> 1)) ^ ((c >> 2) << 2)) << 4) + (c & 1) * 8;
#pragma unroll
      for (int a = 0; a < 16; ++a) wf[a] = tr_read(wa + a * 1024);
    }
    STAMP(7);
    {
      const int b = wave >> 1, half = wave & 1, sp = lane >> 2, p = lane & 3;
      const int src0 = (b * 8 + xg) * 16, hl = (ks >> 1) & 3;
      const unsigned vo = sp * 4096 + p * 16, so = WS_P1 + src0 * 4096 + hl * 1024 + ((ks & 1) * 64 + half * 32) * 8;
      const unsigned vm = sp * 64, sm = WS_ML + src0 * 64 + hl * 16;
      u32x4 gv[4], gm;
      for (unsigned spin = 0; spin < SPIN_CAP; ++spin) {
        asm volatile("" ::: "memory");
        gm = ld_dev<LD_END>(rws, vm, sm);
        if (__popcll(__ballot(gm[1] != tag)) <= 4 * SPEC_TH) break;
        __builtin_amdgcn_s_sleep(4);
      }
      for (unsigned spin = 0; spin < SPIN_CAP; ++spin) {
        asm volatile("" ::: "memory");
        gm = ld_dev<LD_END>(rws, vm, sm);
#pragma unroll
        for (int i = 0; i < 4; ++i) gv[i] = ld_dev<LD_END>(rws, vo + i * 64, so);
        bool ok = (gm[1] == tag) & (gm[3] == tag);
#pragma unroll
        for (int i = 0; i < 4; ++i) ok = ok & (gv[i][1] == tag) & (gv[i][3] == tag);
        if (__all(ok)) break;
      }
      STAMP(8);
      const float ms = __uint_as_float(gm[0]), M = quads_max(ms);
      const float wt = ex2((ms - M) * L2E);
      const float inv = __builtin_amdgcn_rcpf(quads_sum(wt * __uint_as_float(gm[2])));
      float e[2];
#pragma unroll
      for (int j = 0; j < 2; ++j) {
        const float u0 = swap32_sum(wt * __uint_as_float(gv[0][2 * j]), wt * __uint_as_float(gv[2][2 * j]));
        const float u1 = swap32_sum(wt * __uint_as_float(gv[1][2 * j]), wt * __uint_as_float(gv[3][2 * j]));
        float v = swap16_sum(u0, u1);
        v += dpp<ROR4>(v); v += dpp<ROR8>(v);
        e[j] = v * inv;
      }
      if ((c >> 2) == 0) lds_st<unsigned>(lds0 + L_OS + b * 128 + (half * 16 + g * 4 + p) * 4, pk2(e[0], e[1]));
    }
    wg_barrier();
    STAMP(9);
    if (wave < 2) {
      const s16x4 oa = lds_ld<s16x4>(lds0 + L_OS + (lane & 1) * 128 + (lane >> 2) * 8);
      f32x4 c0 = {0.f, 0.f, 0.f, 0.f}, c1 = {0.f, 0.f, 0.f, 0.f};
      c0 = mfma4<0>(oa, wf[0], c0);   c1 = mfma4<1>(oa, wf[1], c1);   c0 = mfma4<2>(oa, wf[2], c0);   c1 = mfma4<3>(oa, wf[3], c1);
      c0 = mfma4<4>(oa, wf[4], c0);   c1 = mfma4<5>(oa, wf[5], c1);   c0 = mfma4<6>(oa, wf[6], c0);   c1 = mfma4<7>(oa, wf[7], c1);
      c0 = mfma4<8>(oa, wf[8], c0);   c1 = mfma4<9>(oa, wf[9], c1);   c0 = mfma4<10>(oa, wf[10], c0); c1 = mfma4<11>(oa, wf[11], c1);
      c0 = mfma4<12>(oa, wf[12], c0); c1 = mfma4<13>(oa, wf[13], c1); c0 = mfma4<14>(oa, wf[14], c0); c1 = mfma4<15>(oa, wf[15], c1);
      st_dev<ST_END>(rws, u32x4{__float_as_uint(c0[0] + c1[0]), tag, __float_as_uint(c0[1] + c1[1]), tag}, lane * 16, WS_P2 + ks * 8192 + (cs * 128 + wave * 64) * 16);
    }
    STAMP(10);
    STAMP_FLUSH(wave * 12, 12);
  } else {
    __builtin_amdgcn_s_setprio(HPRIO);
    const int cg = xg * 32 + xi;
    const rsrc_t rx = make_rsrc(x, 2 * DM * 2), rq = make_rsrc(wq, DM * HQD * 2);
    unsigned ev, xo0, xo1;
    u32x4 x0, x1;
    asm volatile(
        "s_nop 4\n\t"
        "buffer_load_dwordx4 %0, %5, %6, 0 offen\n\t"
        "buffer_load_dwordx4 %1, %5, %6, 0 offen offset:1024\n\t"
        "buffer_load_dword %2, off, %7, 0 sc1\n\t"
        "buffer_load_ushort %3, %8, %6, 0 offen\n\t"
        "buffer_load_ushort %4, %8, %6, 0 offen offset:1024"
        : "=&v"(x0), "=&v"(x1), "=&v"(ev), "=&v"(xo0), "=&v"(xo1)
        : "v"(lane * 16), "s"(rx), "s"(rws), "v"((cg * 2 + (lane & 1)) * 2)
        : "memory");
    const int qk = cg >> 6, qc = cg & 63;
    const bool f1 = xi < 14 && !NO_FFN, f2 = xi >= 14 && xi < 28 && !NO_FFN;
    const int fi = xg * 14 + (xi < 14 ? xi : xi - 14);
    const int hk = fi / 28, hc = fi % 28;
    const int gk = fi >> 3, gc = fi & 7;
    {
      const unsigned sw = ((lane & 7) ^ (((lane >> 4) & 1) << 2)) << 4;
      const unsigned vq = (lane >> 3) * (HQD * 2) + sw;
#pragma unroll
      for (int i = 0; i < 16; ++i) dma_piece(rq, vq, (qk * 128 + 8 * i) * (HQD * 2) + qc * 128, lds0 + L_WQ + i * 1024);
      const rsrc_t rf = make_rsrc(f1 ? w1 : w2, f1 || f2 ? DM * FF * 2 : 0);
      const int ld = f1 ? FF * 2 : DM * 2;
      const unsigned vf = (lane >> 3) * ld + sw, sf = f1 ? hk * 128 * (FF * 2) + hc * 128 : gk * 128 * (DM * 2) + gc * 128;
#pragma unroll
      for (int i = 0; i < 16; ++i) dma_piece(rf, vf, sf + 8 * i * ld, lds0 + L_WF + i * 1024);
      const int ks = xg * 8 + (xi >> 2), cs = xi & 3;
      const rsrc_t ro = make_rsrc(wo, HQD * DM * 2);
      const unsigned vo = (lane >> 4) * (DM * 2) + (((lane & 15) ^ ((lane >> 4) << 2)) << 4);
#pragma unroll
      for (int i = 0; i < 16; ++i) dma_piece(ro, vo, (ks * 64 + 4 * i) * (DM * 2) + cs * 256, lds0 + L_WO + i * 1024);
    }
    wg_barrier();
    asm volatile("s_waitcnt vmcnt(48)" : "+v"(x0), "+v"(x1), "+v"(ev), "+v"(xo0), "+v"(xo1)::"memory");
    STAMP(1);
    const unsigned tag = __builtin_amdgcn_readfirstlane(ev) + 1u;
    {
      float a[2][8];
#pragma unroll
      for (int j = 0; j < 4; ++j) { a[0][2 * j] = bflo(x0[j]); a[0][2 * j + 1] = bfhi(x0[j]); a[1][2 * j] = bflo(x1[j]); a[1][2 * j + 1] = bfhi(x1[j]); }
#pragma unroll
      for (int b = 0; b < 2; ++b) {
        const float mean = wave_sum(((a[b][0] + a[b][1]) + (a[b][2] + a[b][3])) + ((a[b][4] + a[b][5]) + (a[b][6] + a[b][7]))) * (1.f / DM);
        float q = 0.f;
#pragma unroll
        for (int j = 0; j < 8; ++j) q = fmaf(a[b][j] - mean, a[b][j] - mean, q);
        const float var = wave_sum(q) * (1.f / DM);
        const float mb = rbf(mean), inv = __builtin_amdgcn_rcpf(rbf(__builtin_amdgcn_sqrtf(rbf(rbf(var) + 1e-5f))));
        u32x4 xn;
#pragma unroll
        for (int j = 0; j < 4; ++j) xn[j] = pk2(rbf(a[b][2 * j] - mb) * inv, rbf(a[b][2 * j + 1] - mb) * inv);
        lds_st<u32x4>(lds0 + L_XN + b * 1024 + lane * 16, xn);
      }
    }
    if (lane == 0) lds_st<unsigned>(lds0 + L_TAG, tag);
    if (lane < 12) lds_st<unsigned>(lds0 + L_TL + lane * 4, 0u);
    asm volatile("s_waitcnt vmcnt(32) lgkmcnt(0)" ::: "memory");
    STAMP(2);
    {
      const f32x2 y = proj64(lds0 + L_XN + qk * 256, 1024, lds0 + L_WQ, lane);
      st_dev(rws, u32x4{__float_as_uint(y[0]), tag, __float_as_uint(y[1]), tag}, lane * 16, WS_QP + (qk * HQD + qc * 64) * 16);
    }
    if (f1) {
      wait_vm<17>();
      const f32x2 y = proj64(lds0 + L_XN + hk * 256, 1024, lds0 + L_WF, lane);
      st_dev(rws, u32x4{__float_as_uint(y[0]), tag, __float_as_uint(y[1]), tag}, lane * 16, WS_HP + (hk * FF + hc * 64) * 16);
    }
    {
      const f32x2 q = gather_quarters(rws, WS_QP + (qc * 64 + qk * 16) * 16, HQD * 16, tag, lane);
      if (lane < 16) __builtin_amdgcn_raw_buffer_store_b64(u32x2{pk2(q[0], q[1]), tag}, rws, lane * 8, WS_Q + (qc * 64 + qk * 16) * 8, ST_MID);
    }
    STAMP(3);
    {
      const int bh = wg >> 4, b = bh >> 3, kvh = bh & 7;
      u32x4 gq[4];
      for (unsigned spin = 0; spin < SPIN_CAP; ++spin) {
        asm volatile("" ::: "memory");
#pragma unroll
        for (int i = 0; i < 4; ++i) gq[i] = ld_dev(rws, lane * 16 + i * 1024, WS_Q + kvh * 4096);
        bool ok = true;
#pragma unroll
        for (int i = 0; i < 4; ++i) ok = ok & (gq[i][1] == tag) & (gq[i][3] == tag);
        if (__all(ok)) break;
        __builtin_amdgcn_s_sleep(QSLEEP);
      }
#pragma unroll
      for (int i = 0; i < 4; ++i)
        lds_st<unsigned>(lds0 + L_QI + i * 256 + lane * 4, b ? (gq[i][0] >> 16) | (gq[i][2] & 0xffff0000u) : (gq[i][0] & 0xffffu) | (gq[i][2] << 16));
    }
    STAMP(4);
    wg_barrier();

    if (f1) {
      const f32x2 h = gather_quarters(rws, WS_HP + (hc * 64 + hk * 16) * 16, FF * 16, tag, lane);
      if (lane < 16) __builtin_amdgcn_raw_buffer_store_b64(u32x2{pk2(silu(rbf(h[0])), silu(rbf(h[1]))), tag}, rws, lane * 8, WS_H + (hc * 64 + hk * 16) * 8, ST_MID);
    } else if (f2) {
      u32x4 gh;
      for (unsigned spin = 0; spin < SPIN_CAP; ++spin) {
        asm volatile("" ::: "memory");
        gh = ld_dev(rws, lane * 16, WS_H + gk * 1024);
        if (__all((gh[1] == tag) & (gh[3] == tag))) break;
        __builtin_amdgcn_s_sleep(32);
      }
      lds_st<unsigned>(lds0 + L_HS + lane * 4, (gh[0] & 0xffffu) | (gh[2] << 16));
      lds_st<unsigned>(lds0 + L_HS + 256 + lane * 4, (gh[0] >> 16) | (gh[2] & 0xffff0000u));
      wait_lds();
      const f32x2 y = proj64(lds0 + L_HS, 256, lds0 + L_WF, lane);
      st_dev(rws, u32x4{__float_as_uint(y[0]), tag, __float_as_uint(y[1]), tag}, lane * 16, WS_FG + (gk * DM + gc * 64) * 16);
    }
    STAMP(5);
    if (PACE) {
      u32x4 ta, tb;
      for (unsigned spin = 0; spin < SPIN_CAP; ++spin) {
        ta = *(volatile LAS u32x4*)(size_t)(lds0 + L_TS); tb = *(volatile LAS u32x4*)(size_t)(lds0 + L_TS + 16);
        if (__all((tb[0] != 0) & (tb[1] != 0) & (tb[2] != 0) & (tb[3] != 0))) break;
        __builtin_amdgcn_s_sleep(8);
      }
      const unsigned base = tb[0];
      if (wg < 32 && lane == 0) {
        const unsigned a = base + (unsigned)(((int)(ta[0] - base) + (int)(ta[1] - base) + (int)(ta[2] - base) + (int)(ta[3] - base)) >> 2);
        const unsigned b = base + (unsigned)(((int)(tb[1] - base) + (int)(tb[2] - base) + (int)(tb[3] - base)) >> 2);
        st_dev(rws, u32x4{a, tag, b, tag}, 0, WS_PC + wg * 16);
      }
      u32x4 gp;
      for (unsigned spin = 0; spin < SPIN_CAP; ++spin) {
        asm volatile("" ::: "memory");
        gp = ld_dev(rws, (lane & 31) * 16, WS_PC);
        if (__all((gp[1] == tag) & (gp[3] == tag))) break;
        __builtin_amdgcn_s_sleep(16);
      }
      const float r = (float)(CPB - CPA) * __builtin_amdgcn_rcpf((float)(int)(gp[2] - gp[0]));
      const float fin = ((float)(64 * (NSTEP - 1 - (CPB + 2))) + wave_sum(r * (float)(int)(gp[2] - base))) * __builtin_amdgcn_rcpf(wave_sum(r));
      if (lane == 0) *(volatile LAS unsigned*)(size_t)(lds0 + L_TL) = (base + (unsigned)((int)fin - PACE_OFF)) | 1u;
    }
    STAMP(11);
    wg_barrier();
    STAMP(6);
    float g0, g1;
    {
      u32x4 gf = {};
      const int pr = lane >> 1;
      const unsigned vo = (pr < 14 ? pr : 0) * (DM * 16) + (lane & 1) * 16;
      for (unsigned spin = 0; spin < (NO_FFN ? 0 : SPIN_CAP); ++spin) {
        asm volatile("" ::: "memory");
        gf = ld_dev<LD_END>(rws, vo, WS_FG + cg * 32);
        if (__all((gf[1] == tag) & (gf[3] == tag))) break;
      }
      float s0 = pr < 14 ? __uint_as_float(gf[0]) : 0.f, s1 = pr < 14 ? __uint_as_float(gf[2]) : 0.f;
      s0 += dpp<XOR2>(s0); s1 += dpp<XOR2>(s1);
      g0 = rbf(silu(rbf(quads_sum(s0))));
      g1 = rbf(silu(rbf(quads_sum(s1))));
    }
    STAMP(7);
    wg_barrier();
    STAMP(8);
    {
      u32x4 gs[2];
      const unsigned vo = (lane >> 1) * 8192 + (lane & 1) * 16;
      for (unsigned spin = 0; spin < SPIN_CAP; ++spin) {
        asm volatile("" ::: "memory");
#pragma unroll
        for (int i = 0; i < 2; ++i) gs[i] = ld_dev<LD_END>(rws, vo, WS_P2 + i * 32 * 8192 + cg * 32);
        if (__all((gs[0][1] == tag) & (gs[0][3] == tag) & (gs[1][1] == tag) & (gs[1][3] == tag))) break;
      }
      STAMP(9);
      float s0 = __uint_as_float(gs[0][0]) + __uint_as_float(gs[1][0]), s1 = __uint_as_float(gs[0][2]) + __uint_as_float(gs[1][2]);
      s0 += dpp<XOR2>(s0); s1 += dpp<XOR2>(s1);
      s0 = rbf(quads_sum(s0)); s1 = rbf(quads_sum(s1));
      if (lane < 2) {
        out[cg * 2 + lane] = __builtin_bit_cast(u16, (__bf16)(bflo(xo0) + rbf(s0 * g0)));
        out[DM + cg * 2 + lane] = __builtin_bit_cast(u16, (__bf16)(bflo(xo1) + rbf(s1 * g1)));
      }
      if (wg == 0 && lane == 0) __builtin_amdgcn_raw_buffer_store_b32(tag, rws, 0, WS_EPOCH, 16);
    }
    STAMP(10);
    STAMP_FLUSH(48, 12);
  }
}

}

extern "C" void kernel_launch(void* const* d_in, const int* in_sizes, int n_in, void* d_out, int out_size, void* d_ws, size_t ws_size,
                              hipStream_t stream) {
  (void)hipFuncSetAttribute((const void*)fused_decode_kernel, hipFuncAttributeMaxDynamicSharedMemorySize, L_END);
  fused_decode_kernel<<<NWG, NTHR, L_END, stream>>>((const u16*)d_in[0], (const u16*)d_in[1], (const u16*)d_in[2], (const u16*)d_in[3],
                                                    (const u16*)d_in[4], (const u16*)d_in[6], (const u16*)d_in[7], (u16*)d_out, (char*)d_ws);
}
```

```cpp
#include <hip/hip_runtime.h>
#include <hip/hip_bf16.h>
#include <type_traits>
#include <cstdio>

#define STAMPS 0
#define WIN 4
#define HEAD_WIN 4
#define HPRIO 3
#define QSLEEP 8
#define SPEC_TH 3
#define TSTAMP 0
#define PACE 0
#define CPA 7
#define CPB 11
#define PACE_OFF 130
#define WIN2 WIN
#define LD_MID 16
#define ST_MID 16
#define LD_END 16
#define ST_END 16
#define NO_FFN 0

namespace {

typedef unsigned short u16;
typedef short s16x4 __attribute__((ext_vector_type(4)));
typedef __bf16 bf16x8 __attribute__((ext_vector_type(8)));
typedef __bf16 bf16x2 __attribute__((ext_vector_type(2)));
typedef float f32x2 __attribute__((ext_vector_type(2)));
typedef float f32x4 __attribute__((ext_vector_type(4)));
typedef unsigned u32x2 __attribute__((ext_vector_type(2)));
typedef unsigned u32x4 __attribute__((ext_vector_type(4)));
typedef __amdgpu_buffer_rsrc_t rsrc_t;
#define LAS __attribute__((address_space(3)))
#define GAS __attribute__((address_space(1)))

constexpr int NWG = 256, NTHR = 320;
constexpr int DM = 512, HQD = 4096, SK = 32768, DH = 128, FF = 1792;
constexpr int NSLOT = 3, NSTEP = 32, NPRE = 5;
constexpr int KV_BYTES = 2 * 8 * SK * DH * 2;
constexpr float SCALE = 0.08838834764831845f, L2E = 1.4426950408889634f;
constexpr float M_INIT = -3.0e38f;
constexpr unsigned SPIN_CAP = 1u << 18;

constexpr int WS_EPOCH = 0;
constexpr int WS_Q = 4096;
constexpr int WS_H = WS_Q + HQD * 8;
constexpr int WS_ML = WS_H + FF * 8;
constexpr int WS_PC = WS_ML + NWG * 64;
constexpr int WS_QP = 131072;
constexpr int WS_HP = WS_QP + 4 * HQD * 16;
constexpr int WS_FG = WS_HP + 4 * FF * 16;
constexpr int WS_P2 = WS_FG + 14 * DM * 16;
constexpr int WS_P1 = WS_P2 + 64 * DM * 16;
constexpr int WS_END = WS_P1 + NWG * 4096;
constexpr int WS_STAMP = 8 << 20;
static_assert(WS_PC + 512 <= WS_QP, "workspace map");

constexpr int L_RING = 0;
constexpr int L_WQ = 4 * NSLOT * 8192;
constexpr int L_WF = L_WQ + 16384;
constexpr int L_WO = L_WF + 16384;
constexpr int L_XN = L_WO + 16384;
constexpr int L_QI = L_XN + 2048;
constexpr int L_OS = L_QI + 1024;
constexpr int L_HS = L_OS + 256;
constexpr int L_ML = L_HS + 512;
constexpr int L_TAG = L_ML + 128;
constexpr int L_TL = L_TAG + 16;
constexpr int L_TS = L_TL + 16;
constexpr int L_END = L_TS + 32;

__device__ __forceinline__ float rbf(float f) { return (float)(__bf16)f; }
__device__ __forceinline__ unsigned pk2(float lo, float hi) {
  bf16x2 v = {(__bf16)lo, (__bf16)hi};
  return __builtin_bit_cast(unsigned, v);
}
__device__ __forceinline__ float bflo(unsigned u) { return __uint_as_float(u << 16); }
__device__ __forceinline__ float bfhi(unsigned u) { return __uint_as_float(u & 0xffff0000u); }
template <int CTRL> __device__ __forceinline__ float dpp(float x) {
  return __builtin_bit_cast(float, __builtin_amdgcn_mov_dpp(__builtin_bit_cast(int, x), CTRL, 0xf, 0xf, true));
}
constexpr int XOR1 = 0xB1, XOR2 = 0x4E, ROR4 = 0x124, ROR8 = 0x128;
__device__ __forceinline__ float swap16_sum(float a, float b) {
  auto s = __builtin_amdgcn_permlane16_swap(__float_as_uint(a), __float_as_uint(b), false, false);
  return __uint_as_float(s[0]) + __uint_as_float(s[1]);
}
__device__ __forceinline__ float swap32_sum(float a, float b) {
  auto s = __builtin_amdgcn_permlane32_swap(__float_as_uint(a), __float_as_uint(b), false, false);
  return __uint_as_float(s[0]) + __uint_as_float(s[1]);
}
__device__ __forceinline__ float rows_sum(float x) { x = swap16_sum(x, x); return swap32_sum(x, x); }
__device__ __forceinline__ float rows_max(float x) {
  auto s = __builtin_amdgcn_permlane16_swap(__float_as_uint(x), __float_as_uint(x), false, false);
  x = fmaxf(__uint_as_float(s[0]), __uint_as_float(s[1]));
  auto t = __builtin_amdgcn_permlane32_swap(__float_as_uint(x), __float_as_uint(x), false, false);
  return fmaxf(__uint_as_float(t[0]), __uint_as_float(t[1]));
}
__device__ __forceinline__ float quads_sum(float x) { x += dpp<ROR4>(x); x += dpp<ROR8>(x); return rows_sum(x); }
__device__ __forceinline__ float quads_max(float x) { x = fmaxf(x, dpp<ROR4>(x)); x = fmaxf(x, dpp<ROR8>(x)); return rows_max(x); }
__device__ __forceinline__ float wave_sum(float x) { x += dpp<XOR1>(x); x += dpp<XOR2>(x); return quads_sum(x); }
__device__ __forceinline__ float ex2(float x) { return __builtin_amdgcn_exp2f(x); }
__device__ __forceinline__ float silu(float v) { return v * __builtin_amdgcn_rcpf(1.f + ex2(-v * L2E)); }

template <int N> __device__ __forceinline__ void wait_vm() {
  __builtin_amdgcn_sched_barrier(0);
  asm volatile("s_waitcnt vmcnt(%0)" ::"i"(N) : "memory");
}
__device__ __forceinline__ void wait_lds() { asm volatile("s_waitcnt lgkmcnt(0)" ::: "memory"); }
__device__ __forceinline__ void wg_barrier() {
  asm volatile("s_waitcnt lgkmcnt(0)\n\ts_barrier" ::: "memory");
}
__device__ __forceinline__ unsigned now32() { return (unsigned)__builtin_amdgcn_s_memrealtime(); }
__device__ __forceinline__ rsrc_t make_rsrc(const void* p, int bytes) {
  return __builtin_amdgcn_make_buffer_rsrc((void*)p, (short)0, bytes, 0x00020000);
}
template <int AUX = LD_MID> __device__ __forceinline__ u32x4 ld_dev(rsrc_t r, unsigned voff, unsigned soff) { return __builtin_amdgcn_raw_buffer_load_b128(r, voff, soff, AUX); }
template <int AUX = ST_MID> __device__ __forceinline__ void st_dev(rsrc_t r, u32x4 v, unsigned voff, unsigned soff) { __builtin_amdgcn_raw_buffer_store_b128(v, r, voff, soff, AUX); }
__device__ __forceinline__ s16x4 tr_read(unsigned addr) { return __builtin_amdgcn_ds_read_tr16_b64_v4i16((LAS s16x4*)(size_t)addr); }
template <typename T> __device__ __forceinline__ T lds_ld(unsigned addr) { return *(const LAS T*)(size_t)addr; }
template <typename T> __device__ __forceinline__ void lds_st(unsigned addr, T v) { *(LAS T*)(size_t)addr = v; }

__device__ __forceinline__ void dma_piece(rsrc_t r, unsigned voff, unsigned soff, unsigned dst) {
  asm volatile("s_mov_b32 m0, %0\n\ts_nop 0\n\tbuffer_load_dwordx4 %1, %2, %3 offen nt lds" ::"s"(dst), "v"(voff), "s"(r), "s"(soff) : "memory");
}
__device__ __forceinline__ void dma_half(rsrc_t r, unsigned v0, unsigned v1, unsigned v2, unsigned v3, unsigned soff, unsigned dst) {
  asm volatile(
      "s_mov_b32 m0, %0\n\ts_nop 0\n\t"
      "buffer_load_dwordx4 %1, %5, %6 offen nt lds\n\t"
      "buffer_load_dwordx4 %2, %5, %6 offen offset:1024 nt lds\n\t"
      "buffer_load_dwordx4 %3, %5, %6 offen offset:2048 nt lds\n\t"
      "buffer_load_dwordx4 %4, %5, %6 offen offset:3072 nt lds"
      ::"s"(dst), "v"(v0), "v"(v1), "v"(v2), "v"(v3), "s"(r), "s"(soff)
      : "memory");
}
template <int A> __device__ __forceinline__ f32x4 mfma4(s16x4 a, s16x4 b, f32x4 c) { return __builtin_amdgcn_mfma_f32_4x4x4bf16_1k(a, b, c, 4, A, 0); }
template <int A> struct Proj {
  static __device__ __forceinline__ void run(const s16x4 (&av)[2], unsigned ba, f32x4 (&c)[4]) {
    c[A & 3] = mfma4<A & 15>(av[A >> 4], tr_read(ba + A * 512), c[A & 3]);
    if constexpr (A + 1 < 32) Proj<A + 1>::run(av, ba, c);
  }
};
__device__ __forceinline__ f32x2 proj64(unsigned a_img, int a_stride, unsigned w_img, int lane) {
  const int q = (lane & 15) >> 2, p = lane & 3;
  const unsigned aa = a_img + (lane & 1) * a_stride + (lane >> 2) * 8;
  const s16x4 av[2] = {lds_ld<s16x4>(aa), lds_ld<s16x4>(aa + 128)};
  const unsigned ba = w_img + q * 128 + (((2 * (lane >> 4) + (p >> 1)) ^ ((q >> 1) << 2)) << 4) + (p & 1) * 8;
  f32x4 c[4] = {};
  Proj<0>::run(av, ba, c);
  return f32x2{(c[0][0] + c[1][0]) + (c[2][0] + c[3][0]), (c[0][1] + c[1][1]) + (c[2][1] + c[3][1])};
}
__device__ __forceinline__ f32x2 gather_quarters(rsrc_t rws, unsigned base, int quarter_bytes, unsigned tag, int lane) {
  u32x4 g;
  for (unsigned spin = 0; spin < SPIN_CAP; ++spin) {
    asm volatile("" ::: "memory");
    g = ld_dev(rws, (lane >> 4) * quarter_bytes + (lane & 15) * 16, base);
    if (__all((g[1] == tag) & (g[3] == tag))) break;
    __builtin_amdgcn_s_sleep(QSLEEP);
  }
  return f32x2{rows_sum(__uint_as_float(g[0])), rows_sum(__uint_as_float(g[2]))};
}

struct KFrag { bf16x8 k[4]; };
struct VFrag { s16x4 v[4][2]; };
struct State { float m, l; f32x4 o[2]; };

__device__ __forceinline__ void read_k(KFrag& t, const unsigned (&ka)[4], int off) {
#pragma unroll
  for (int s = 0; s < 4; ++s) t.k[s] = lds_ld<bf16x8>(ka[s] + off);
  asm volatile("s_waitcnt lgkmcnt(0)" : "+v"(t.k[0]), "+v"(t.k[1]), "+v"(t.k[2]), "+v"(t.k[3])::"memory");
}
__device__ __forceinline__ void read_v(VFrag& t, const unsigned (&va)[2], int off) {
#pragma unroll
  for (int a = 0; a < 4; ++a)
#pragma unroll
    for (int h = 0; h < 2; ++h) t.v[a][h] = tr_read(va[h] + off + a * 1024);
  asm volatile("s_waitcnt lgkmcnt(0)"
               : "+v"(t.v[0][0]), "+v"(t.v[0][1]), "+v"(t.v[1][0]), "+v"(t.v[1][1]), "+v"(t.v[2][0]), "+v"(t.v[2][1]), "+v"(t.v[3][0]), "+v"(t.v[3][1])
               :
               : "memory");
}
__device__ __forceinline__ s16x4 score_part(const KFrag& t, const bf16x8 (&qf)[4], State& st) {
  f32x4 sc = {0.f, 0.f, 0.f, 0.f};
#pragma unroll
  for (int s = 0; s < 4; ++s) sc = __builtin_amdgcn_mfma_f32_16x16x32_bf16(t.k[s], qf[s], sc, 0, 0, 0);
  float x[4];
#pragma unroll
  for (int r = 0; r < 4; ++r) x[r] = rbf(rbf(sc[r]) * SCALE);
  const float mn = fmaxf(st.m, rows_max(fmaxf(fmaxf(x[0], x[1]), fmaxf(x[2], x[3]))));
  const float alpha = ex2((st.m - mn) * L2E);
  st.m = mn;
  float p[4];
#pragma unroll
  for (int r = 0; r < 4; ++r) p[r] = ex2((x[r] - mn) * L2E);
  st.l = fmaf(st.l, alpha, (p[0] + p[1]) + (p[2] + p[3]));
  const float a0 = dpp<0x00>(alpha), a1 = dpp<0x55>(alpha), a2 = dpp<0xAA>(alpha), a3 = dpp<0xFF>(alpha);
#pragma unroll
  for (int h = 0; h < 2; ++h) { st.o[h][0] *= a0; st.o[h][1] *= a1; st.o[h][2] *= a2; st.o[h][3] *= a3; }
  const u32x2 pp = {pk2(p[0], p[1]), pk2(p[2], p[3])};
  return __builtin_bit_cast(s16x4, pp);
}
__device__ __forceinline__ void value_part(const VFrag& t, s16x4 pa, State& st) {
#pragma unroll
  for (int h = 0; h < 2; ++h) st.o[h] = mfma4<0>(pa, t.v[0][h], st.o[h]);
#pragma unroll
  for (int h = 0; h < 2; ++h) st.o[h] = mfma4<4>(pa, t.v[1][h], st.o[h]);
#pragma unroll
  for (int h = 0; h < 2; ++h) st.o[h] = mfma4<8>(pa, t.v[2][h], st.o[h]);
#pragma unroll
  for (int h = 0; h < 2; ++h) st.o[h] = mfma4<12>(pa, t.v[3][h], st.o[h]);
}

#define STAMP(i) do {} while (0)
#define STAMP_FLUSH(base, n) do {} while (0)

__global__ void __launch_bounds__(NTHR, 1)
fused_decode_kernel(const u16* __restrict__ x, const u16* __restrict__ kc, const u16* __restrict__ vc, const u16* __restrict__ wq,
                    const u16* __restrict__ wo, const u16* __restrict__ w1, const u16* __restrict__ w2, u16* __restrict__ out, char* ws) {
  extern __shared__ __attribute__((aligned(16))) unsigned char lds[];
  const unsigned lds0 = (unsigned)(size_t)(LAS unsigned char*)lds;
  const int lane = threadIdx.x & 63, wave = __builtin_amdgcn_readfirstlane(threadIdx.x >> 6), wg = blockIdx.x;
  const int xg = wg & 7, xi = wg >> 3;
  const rsrc_t rws = make_rsrc(ws, WS_END);
  STAMP(0);

  if (wave < 4) {
    const rsrc_t rk = make_rsrc(kc, KV_BYTES), rv = make_rsrc(vc, KV_BYTES);
    const int g = lane >> 4, c = lane & 15;
    unsigned vk[4];
#pragma unroll
    for (int u = 0; u < 4; ++u) vk[u] = g * 256 + ((c ^ (4 * u + g)) << 4);
    const unsigned vv = g * 256 + ((c ^ (g << 2)) << 4);
    const unsigned ring = lds0 + L_RING + wave * (NSLOT * 8192);
    const unsigned tile0 = (unsigned)wg * (2048 * 256) + wave * 4096;
    unsigned ka[4], va[2];
#pragma unroll
    for (int s = 0; s < 4; ++s) ka[s] = ring + c * 256 + (((4 * s + g) ^ c) << 4);
#pragma unroll
    for (int h = 0; h < 2; ++h) va[h] = ring + 4096 + (c >> 2) * 256 + (((8 * h + 2 * g + ((c & 3) >> 1)) ^ ((c >> 2) << 2)) << 4) + (c & 1) * 8;
    auto issue_k = [&](int t, int slot) { dma_half(rk, vk[0], vk[1], vk[2], vk[3], tile0 + t * 16384, ring + slot * 8192); };
    auto issue_v = [&](int t, int slot) { dma_half(rv, vv, vv, vv, vv, tile0 + t * 16384, ring + slot * 8192 + 4096); };

    wg_barrier();
#pragma unroll
    for (int t = 0; t < NSLOT; ++t) {
      wait_vm<HEAD_WIN>(); issue_k(t, t);
      wait_vm<HEAD_WIN>(); issue_v(t, t);
    }
    unsigned tsv = TSTAMP ? (unsigned)__builtin_amdgcn_s_memrealtime() : 0u;
    KFrag pk[NPRE]; VFrag pv[NPRE];
#pragma unroll
    for (int t = 0; t < NPRE; ++t) {
      wait_vm<HEAD_WIN>(); read_k(pk[t], ka, (t % NSLOT) * 8192); issue_k(t + NSLOT, t % NSLOT);
      wait_vm<HEAD_WIN>();
      if (TSTAMP) { const unsigned tt = (unsigned)__builtin_amdgcn_s_memrealtime(); tsv = lane == t + 2 ? tt : tsv; }
      read_v(pv[t], va, (t % NSLOT) * 8192); issue_v(t + NSLOT, t % NSLOT);
    }
    STAMP(1);
    wg_barrier();
    STAMP(2);
    bf16x8 qf[4];
#pragma unroll
    for (int s = 0; s < 4; ++s) qf[s] = lds_ld<bf16x8>(lds0 + L_QI + (c & 3) * 256 + (4 * s + g) * 16);
    const unsigned tag = lds_ld<unsigned>(lds0 + L_TAG);

    State st;
    st.m = M_INIT; st.l = 0.f;
    st.o[0] = f32x4{0.f, 0.f, 0.f, 0.f}; st.o[1] = f32x4{0.f, 0.f, 0.f, 0.f};
    unsigned prev = now32();
    auto pace = [&](int n) {
      if (PACE) {
        const unsigned tl = __builtin_amdgcn_readfirstlane(*(volatile LAS unsigned*)(size_t)(lds0 + L_TL));
        const int d = (int)(tl - prev);
        if (tl != 0 && d > 0) {
          const unsigned due = prev + (unsigned)__builtin_amdgcn_readfirstlane((int)((float)d * __builtin_amdgcn_rcpf((float)n)));
          while ((int)(now32() - due) < 0) __builtin_amdgcn_s_sleep(1);
        }
        prev = now32();
      }
    };
    auto step = [&](int t, int slot, auto win) {
      KFrag kf; VFrag vf;
      wait_vm<win.value>(); read_k(kf, ka, slot * 8192);
      pace(2 * (NSTEP - NSLOT - t)); issue_k(t + NSLOT, slot);
      const s16x4 pa = score_part(kf, qf, st);
      wait_vm<win.value>();
      if (TSTAMP) { const unsigned tt = (unsigned)__builtin_amdgcn_s_memrealtime(); tsv = lane == t + 2 ? tt : tsv; }
      if (PACE && (t == CPA || t == CPB)) lds_st<unsigned>(lds0 + L_TS + (t == CPB ? 16 : 0) + wave * 4, now32() | 1u);
      read_v(vf, va, slot * 8192);
      pace(2 * (NSTEP - NSLOT - t) - 1); issue_v(t + NSLOT, slot);
      value_part(vf, pa, st);
    };
    using W1 = std::integral_constant<int, WIN>;
    using W2 = std::integral_constant<int, WIN2>;
#pragma unroll
    for (int j = 0; j < 2 * NSLOT; ++j) {
      step(NPRE + j, (NPRE + j) % NSLOT, W1{});
      if (j < NPRE) value_part(pv[j], score_part(pk[j], qf, st), st);
    }
    static_assert(2 * NSLOT >= NPRE && (NSTEP - NPRE) % NSLOT == 0, "tile schedule");
#pragma unroll
    for (int j = 0; j < NSLOT; ++j) step(NPRE + 2 * NSLOT + j, (NPRE + j) % NSLOT, W1{});
    for (int t0 = NPRE + 3 * NSLOT; t0 < NSTEP - NSLOT; t0 += NSLOT) {
#pragma unroll
      for (int j = 0; j < NSLOT; ++j) step(t0 + j, (NPRE + j) % NSLOT, W2{});
    }
    STAMP(3);
#pragma unroll
    for (int j = 0; j < NSLOT; ++j) {
      KFrag kf; VFrag vf;
      if (j == 0) wait_vm<20>(); else if (j == 1) wait_vm<12>(); else wait_vm<4>();
      read_k(kf, ka, ((NPRE + j) % NSLOT) * 8192);
      const s16x4 pa = score_part(kf, qf, st);
      if (j == 0) wait_vm<16>(); else if (j == 1) wait_vm<8>(); else wait_vm<0>();
      if (j == NSLOT - 1) STAMP(4);
      if (TSTAMP && j == NSLOT - 1) { const unsigned tt = (unsigned)__builtin_amdgcn_s_memrealtime(); tsv = lane == 31 ? tt : tsv;
        unsigned xcc; asm volatile("s_getreg_b32 %0, hwreg(HW_REG_XCC_ID, 0, 4)" : "=s"(xcc)); tsv = lane == 32 ? xcc : tsv;
        ((GAS unsigned*)(ws + WS_STAMP + (1 << 20)))[(wg * 4 + wave) * 64 + lane] = tsv; }
      read_v(vf, va, ((NPRE + j) % NSLOT) * 8192);
      value_part(vf, pa, st);
    }

    const float lsum = rows_sum(st.l);
#pragma unroll
    for (int h = 0; h < 2; ++h)
#pragma unroll
      for (int r = 0; r < 4; ++r) lds_st<float>(ring + (r * 128 + h * 64 + lane) * 4, st.o[h][r]);
    if (lane < 4) lds_st<f32x2>(lds0 + L_ML + (wave * 4 + lane) * 8, f32x2{st.m, lsum});
    STAMP(5);
    wg_barrier();
    STAMP(6);
    {
      f32x2 ml[4], ov[4];
#pragma unroll
      for (int w = 0; w < 4; ++w) {
        ml[w] = lds_ld<f32x2>(lds0 + L_ML + (w * 4 + wave) * 8);
        ov[w] = lds_ld<f32x2>(lds0 + L_RING + w * (NSLOT * 8192) + (wave * 128 + 2 * lane) * 4);
      }
      const float M = fmaxf(fmaxf(ml[0][0], ml[1][0]), fmaxf(ml[2][0], ml[3][0]));
      float L = 0.f, o0 = 0.f, o1 = 0.f;
#pragma unroll
      for (int w = 0; w < 4; ++w) {
        const float wt = ex2((ml[w][0] - M) * L2E);
        L = fmaf(wt, ml[w][1], L); o0 = fmaf(wt, ov[w][0], o0); o1 = fmaf(wt, ov[w][1], o1);
      }
      st_dev<ST_END>(rws, u32x4{__float_as_uint(o0), tag, __float_as_uint(o1), tag}, lane * 16, WS_P1 + wg * 4096 + wave * 1024);
      if (lane == 0) st_dev<ST_END>(rws, u32x4{__float_as_uint(M), tag, __float_as_uint(L), tag}, 0, WS_ML + wg * 64 + wave * 16);
    }
    const int ks = xg * 8 + (xi >> 2), cs = xi & 3;
    s16x4 wf[16];
    if (wave < 2) {
      const unsigned wa = lds0 + L_WO + (c >> 2) * 256 + (((8 * wave + 2 * g + ((c & 3) >> 1)) ^ ((c >> 2) << 2)) << 4) + (c & 1) * 8;
#pragma unroll
      for (int a = 0; a < 16; ++a) wf[a] = tr_read(wa + a * 1024);
    }
    STAMP(7);
    {
      const int b = wave >> 1, half = wave & 1, sp = lane >> 2, p = lane & 3;
      const int src0 = (b * 8 + xg) * 16, hl = (ks >> 1) & 3;
      const unsigned vo = sp * 4096 + p * 16, so = WS_P1 + src0 * 4096 + hl * 1024 + ((ks & 1) * 64 + half * 32) * 8;
      const unsigned vm = sp * 64, sm = WS_ML + src0 * 64 + hl * 16;
      u32x4 gv[4], gm;
      for (unsigned spin = 0; spin < SPIN_CAP; ++spin) {
        asm volatile("" ::: "memory");
        gm = ld_dev<LD_END>(rws, vm, sm);
        if (__popcll(__ballot(gm[1] != tag)) <= 4 * SPEC_TH) break;
        __builtin_amdgcn_s_sleep(4);
      }
      for (unsigned spin = 0; spin < SPIN_CAP; ++spin) {
        asm volatile("" ::: "memory");
        gm = ld_dev<LD_END>(rws, vm, sm);
#pragma unroll
        for (int i = 0; i < 4; ++i) gv[i] = ld_dev<LD_END>(rws, vo + i * 64, so);
        bool ok = (gm[1] == tag) & (gm[3] == tag);
#pragma unroll
        for (int i = 0; i < 4; ++i) ok = ok & (gv[i][1] == tag) & (gv[i][3] == tag);
        if (__all(ok)) break;
      }
      STAMP(8);
      const float ms = __uint_as_float(gm[0]), M = quads_max(ms);
      const float wt = ex2((ms - M) * L2E);
      const float inv = __builtin_amdgcn_rcpf(quads_sum(wt * __uint_as_float(gm[2])));
      float e[2];
#pragma unroll
      for (int j = 0; j < 2; ++j) {
        const float u0 = swap32_sum(wt * __uint_as_float(gv[0][2 * j]), wt * __uint_as_float(gv[2][2 * j]));
        const float u1 = swap32_sum(wt * __uint_as_float(gv[1][2 * j]), wt * __uint_as_float(gv[3][2 * j]));
        float v = swap16_sum(u0, u1);
        v += dpp<ROR4>(v); v += dpp<ROR8>(v);
        e[j] = v * inv;
      }
      if ((c >> 2) == 0) lds_st<unsigned>(lds0 + L_OS + b * 128 + (half * 16 + g * 4 + p) * 4, pk2(e[0], e[1]));
    }
    wg_barrier();
    STAMP(9);
    if (wave < 2) {
      const s16x4 oa = lds_ld<s16x4>(lds0 + L_OS + (lane & 1) * 128 + (lane >> 2) * 8);
      f32x4 c0 = {0.f, 0.f, 0.f, 0.f}, c1 = {0.f, 0.f, 0.f, 0.f};
      c0 = mfma4<0>(oa, wf[0], c0);   c1 = mfma4<1>(oa, wf[1], c1);   c0 = mfma4<2>(oa, wf[2], c0);   c1 = mfma4<3>(oa, wf[3], c1);
      c0 = mfma4<4>(oa, wf[4], c0);   c1 = mfma4<5>(oa, wf[5], c1);   c0 = mfma4<6>(oa, wf[6], c0);   c1 = mfma4<7>(oa, wf[7], c1);
      c0 = mfma4<8>(oa, wf[8], c0);   c1 = mfma4<9>(oa, wf[9], c1);   c0 = mfma4<10>(oa, wf[10], c0); c1 = mfma4<11>(oa, wf[11], c1);
      c0 = mfma4<12>(oa, wf[12], c0); c1 = mfma4<13>(oa, wf[13], c1); c0 = mfma4<14>(oa, wf[14], c0); c1 = mfma4<15>(oa, wf[15], c1);
      st_dev<ST_END>(rws, u32x4{__float_as_uint(c0[0] + c1[0]), tag, __float_as_uint(c0[1] + c1[1]), tag}, lane * 16, WS_P2 + ks * 8192 + (cs * 128 + wave * 64) * 16);
    }
    STAMP(10);
    STAMP_FLUSH(wave * 12, 12);
  } else {
    __builtin_amdgcn_s_setprio(HPRIO);
    const int cg = xg * 32 + xi;
    const rsrc_t rx = make_rsrc(x, 2 * DM * 2), rq = make_rsrc(wq, DM * HQD * 2);
    unsigned ev, xo0, xo1;
    u32x4 x0, x1;
    asm volatile(
        "s_nop 4\n\t"
        "buffer_load_dwordx4 %0, %5, %6, 0 offen\n\t"
        "buffer_load_dwordx4 %1, %5, %6, 0 offen offset:1024\n\t"
        "buffer_load_dword %2, off, %7, 0 sc1\n\t"
        "buffer_load_ushort %3, %8, %6, 0 offen\n\t"
        "buffer_load_ushort %4, %8, %6, 0 offen offset:1024"
        : "=&v"(x0), "=&v"(x1), "=&v"(ev), "=&v"(xo0), "=&v"(xo1)
        : "v"(lane * 16), "s"(rx), "s"(rws), "v"((cg * 2 + (lane & 1)) * 2)
        : "memory");
    const int qk = cg >> 6, qc = cg & 63;
    const bool f1 = xi < 14 && !NO_FFN, f2 = xi >= 14 && xi < 28 && !NO_FFN;
    const int fi = xg * 14 + (xi < 14 ? xi : xi - 14);
    const int hk = fi / 28, hc = fi % 28;
    const int gk = fi >> 3, gc = fi & 7;
    {
      const unsigned sw = ((lane & 7) ^ (((lane >> 4) & 1) << 2)) << 4;
      const unsigned vq = (lane >> 3) * (HQD * 2) + sw;
#pragma unroll
      for (int i = 0; i < 16; ++i) dma_piece(rq, vq, (qk * 128 + 8 * i) * (HQD * 2) + qc * 128, lds0 + L_WQ + i * 1024);
      const rsrc_t rf = make_rsrc(f1 ? w1 : w2, f1 || f2 ? DM * FF * 2 : 0);
      const int ld = f1 ? FF * 2 : DM * 2;
      const unsigned vf = (lane >> 3) * ld + sw, sf = f1 ? hk * 128 * (FF * 2) + hc * 128 : gk * 128 * (DM * 2) + gc * 128;
#pragma unroll
      for (int i = 0; i < 16; ++i) dma_piece(rf, vf, sf + 8 * i * ld, lds0 + L_WF + i * 1024);
      const int ks = xg * 8 + (xi >> 2), cs = xi & 3;
      const rsrc_t ro = make_rsrc(wo, HQD * DM * 2);
      const unsigned vo = (lane >> 4) * (DM * 2) + (((lane & 15) ^ ((lane >> 4) << 2)) << 4);
#pragma unroll
      for (int i = 0; i < 16; ++i) dma_piece(ro, vo, (ks * 64 + 4 * i) * (DM * 2) + cs * 256, lds0 + L_WO + i * 1024);
    }
    wg_barrier();
    asm volatile("s_waitcnt vmcnt(48)" : "+v"(x0), "+v"(x1), "+v"(ev), "+v"(xo0), "+v"(xo1)::"memory");
    STAMP(1);
    const unsigned tag = __builtin_amdgcn_readfirstlane(ev) + 1u;
    {
      float a[2][8];
#pragma unroll
      for (int j = 0; j < 4; ++j) { a[0][2 * j] = bflo(x0[j]); a[0][2 * j + 1] = bfhi(x0[j]); a[1][2 * j] = bflo(x1[j]); a[1][2 * j + 1] = bfhi(x1[j]); }
#pragma unroll
      for (int b = 0; b < 2; ++b) {
        const float mean = wave_sum(((a[b][0] + a[b][1]) + (a[b][2] + a[b][3])) + ((a[b][4] + a[b][5]) + (a[b][6] + a[b][7]))) * (1.f / DM);
        float q = 0.f;
#pragma unroll
        for (int j = 0; j < 8; ++j) q = fmaf(a[b][j] - mean, a[b][j] - mean, q);
        const float var = wave_sum(q) * (1.f / DM);
        const float mb = rbf(mean), inv = __builtin_amdgcn_rcpf(rbf(__builtin_amdgcn_sqrtf(rbf(rbf(var) + 1e-5f))));
        u32x4 xn;
#pragma unroll
        for (int j = 0; j < 4; ++j) xn[j] = pk2(rbf(a[b][2 * j] - mb) * inv, rbf(a[b][2 * j + 1] - mb) * inv);
        lds_st<u32x4>(lds0 + L_XN + b * 1024 + lane * 16, xn);
      }
    }
    if (lane == 0) lds_st<unsigned>(lds0 + L_TAG, tag);
    if (lane < 12) lds_st<unsigned>(lds0 + L_TL + lane * 4, 0u);
    asm volatile("s_waitcnt vmcnt(32) lgkmcnt(0)" ::: "memory");
    STAMP(2);
    {
      const f32x2 y = proj64(lds0 + L_XN + qk * 256, 1024, lds0 + L_WQ, lane);
      st_dev(rws, u32x4{__float_as_uint(y[0]), tag, __float_as_uint(y[1]), tag}, lane * 16, WS_QP + (qk * HQD + qc * 64) * 16);
    }
    if (f1) {
      wait_vm<17>();
      const f32x2 y = proj64(lds0 + L_XN + hk * 256, 1024, lds0 + L_WF, lane);
      st_dev(rws, u32x4{__float_as_uint(y[0]), tag, __float_as_uint(y[1]), tag}, lane * 16, WS_HP + (hk * FF + hc * 64) * 16);
    }
    {
      const f32x2 q = gather_quarters(rws, WS_QP + (qc * 64 + qk * 16) * 16, HQD * 16, tag, lane);
      if (lane < 16) __builtin_amdgcn_raw_buffer_store_b64(u32x2{pk2(q[0], q[1]), tag}, rws, lane * 8, WS_Q + (qc * 64 + qk * 16) * 8, ST_MID);
    }
    STAMP(3);
    {
      const int bh = wg >> 4, b = bh >> 3, kvh = bh & 7;
      u32x4 gq[4];
      for (unsigned spin = 0; spin < SPIN_CAP; ++spin) {
        asm volatile("" ::: "memory");
#pragma unroll
        for (int i = 0; i < 4; ++i) gq[i] = ld_dev(rws, lane * 16 + i * 1024, WS_Q + kvh * 4096);
        bool ok = true;
#pragma unroll
        for (int i = 0; i < 4; ++i) ok = ok & (gq[i][1] == tag) & (gq[i][3] == tag);
        if (__all(ok)) break;
        __builtin_amdgcn_s_sleep(QSLEEP);
      }
#pragma unroll
      for (int i = 0; i < 4; ++i)
        lds_st<unsigned>(lds0 + L_QI + i * 256 + lane * 4, b ? (gq[i][0] >> 16) | (gq[i][2] & 0xffff0000u) : (gq[i][0] & 0xffffu) | (gq[i][2] << 16));
    }
    STAMP(4);
    wg_barrier();

    if (f1) {
      const f32x2 h = gather_quarters(rws, WS_HP + (hc * 64 + hk * 16) * 16, FF * 16, tag, lane);
      if (lane < 16) __builtin_amdgcn_raw_buffer_store_b64(u32x2{pk2(silu(rbf(h[0])), silu(rbf(h[1]))), tag}, rws, lane * 8, WS_H + (hc * 64 + hk * 16) * 8, ST_MID);
    } else if (f2) {
      u32x4 gh;
      for (unsigned spin = 0; spin < SPIN_CAP; ++spin) {
        asm volatile("" ::: "memory");
        gh = ld_dev(rws, lane * 16, WS_H + gk * 1024);
        if (__all((gh[1] == tag) & (gh[3] == tag))) break;
        __builtin_amdgcn_s_sleep(32);
      }
      lds_st<unsigned>(lds0 + L_HS + lane * 4, (gh[0] & 0xffffu) | (gh[2] << 16));
      lds_st<unsigned>(lds0 + L_HS + 256 + lane * 4, (gh[0] >> 16) | (gh[2] & 0xffff0000u));
      wait_lds();
      const f32x2 y = proj64(lds0 + L_HS, 256, lds0 + L_WF, lane);
      st_dev(rws, u32x4{__float_as_uint(y[0]), tag, __float_as_uint(y[1]), tag}, lane * 16, WS_FG + (gk * DM + gc * 64) * 16);
    }
    STAMP(5);
    if (PACE) {
      u32x4 ta, tb;
      for (unsigned spin = 0; spin < SPIN_CAP; ++spin) {
        ta = *(volatile LAS u32x4*)(size_t)(lds0 + L_TS); tb = *(volatile LAS u32x4*)(size_t)(lds0 + L_TS + 16);
        if (__all((tb[0] != 0) & (tb[1] != 0) & (tb[2] != 0) & (tb[3] != 0))) break;
        __builtin_amdgcn_s_sleep(8);
      }
      const unsigned base = tb[0];
      if (wg < 32 && lane == 0) {
        const unsigned a = base + (unsigned)(((int)(ta[0] - base) + (int)(ta[1] - base) + (int)(ta[2] - base) + (int)(ta[3] - base)) >> 2);
        const unsigned b = base + (unsigned)(((int)(tb[1] - base) + (int)(tb[2] - base) + (int)(tb[3] - base)) >> 2);
        st_dev(rws, u32x4{a, tag, b, tag}, 0, WS_PC + wg * 16);
      }
      u32x4 gp;
      for (unsigned spin = 0; spin < SPIN_CAP; ++spin) {
        asm volatile("" ::: "memory");
        gp = ld_dev(rws, (lane & 31) * 16, WS_PC);
        if (__all((gp[1] == tag) & (gp[3] == tag))) break;
        __builtin_amdgcn_s_sleep(16);
      }
      const float r = (float)(CPB - CPA) * __builtin_amdgcn_rcpf((float)(int)(gp[2] - gp[0]));
      const float fin = ((float)(64 * (NSTEP - 1 - (CPB + 2))) + wave_sum(r * (float)(int)(gp[2] - base))) * __builtin_amdgcn_rcpf(wave_sum(r));
      if (lane == 0) *(volatile LAS unsigned*)(size_t)(lds0 + L_TL) = (base + (unsigned)((int)fin - PACE_OFF)) | 1u;
    }
    STAMP(11);
    wg_barrier();
    STAMP(6);
    float g0, g1;
    {
      u32x4 gf = {};
      const int pr = lane >> 1;
      const unsigned vo = (pr < 14 ? pr : 0) * (DM * 16) + (lane & 1) * 16;
      for (unsigned spin = 0; spin < (NO_FFN ? 0 : SPIN_CAP); ++spin) {
        asm volatile("" ::: "memory");
        gf = ld_dev<LD_END>(rws, vo, WS_FG + cg * 32);
        if (__all((gf[1] == tag) & (gf[3] == tag))) break;
      }
      float s0 = pr < 14 ? __uint_as_float(gf[0]) : 0.f, s1 = pr < 14 ? __uint_as_float(gf[2]) : 0.f;
      s0 += dpp<XOR2>(s0); s1 += dpp<XOR2>(s1);
      g0 = rbf(silu(rbf(quads_sum(s0))));
      g1 = rbf(silu(rbf(quads_sum(s1))));
    }
    STAMP(7);
    wg_barrier();
    STAMP(8);
    {
      u32x4 gs[2];
      const unsigned vo = (lane >> 1) * 8192 + (lane & 1) * 16;
      for (unsigned spin = 0; spin < SPIN_CAP; ++spin) {
        asm volatile("" ::: "memory");
#pragma unroll
        for (int i = 0; i < 2; ++i) gs[i] = ld_dev<LD_END>(rws, vo, WS_P2 + i * 32 * 8192 + cg * 32);
        if (__all((gs[0][1] == tag) & (gs[0][3] == tag) & (gs[1][1] == tag) & (gs[1][3] == tag))) break;
      }
      STAMP(9);
      float s0 = __uint_as_float(gs[0][0]) + __uint_as_float(gs[1][0]), s1 = __uint_as_float(gs[0][2]) + __uint_as_float(gs[1][2]);
      s0 += dpp<XOR2>(s0); s1 += dpp<XOR2>(s1);
      s0 = rbf(quads_sum(s0)); s1 = rbf(quads_sum(s1));
      if (lane < 2) {
        out[cg * 2 + lane] = __builtin_bit_cast(u16, (__bf16)(bflo(xo0) + rbf(s0 * g0)));
        out[DM + cg * 2 + lane] = __builtin_bit_cast(u16, (__bf16)(bflo(xo1) + rbf(s1 * g1)));
      }
      if (wg == 0 && lane == 0) __builtin_amdgcn_raw_buffer_store_b32(tag, rws, 0, WS_EPOCH, 16);
    }
    STAMP(10);
    STAMP_FLUSH(48, 12);
  }
}

}

extern "C" void kernel_launch(void* const* d_in, const int* in_sizes, int n_in, void* d_out, int out_size, void* d_ws, size_t ws_size,
                              hipStream_t stream) {
  for (int i = 0; i < n_in; ++i) fprintf(stderr, "in%d %p n=%d\n", i, d_in[i], in_sizes[i]);
  fprintf(stderr, "out %p ws %p %zu\n", d_out, d_ws, ws_size);
  (void)hipFuncSetAttribute((const void*)fused_decode_kernel, hipFuncAttributeMaxDynamicSharedMemorySize, L_END);
  fused_decode_kernel<<<NWG, NTHR, L_END, stream>>>((const u16*)d_in[0], (const u16*)d_in[1], (const u16*)d_in[2], (const u16*)d_in[3],
                                                    (const u16*)d_in[4], (const u16*)d_in[6], (const u16*)d_in[7], (u16*)d_out, (char*)d_ws);
}
```

```cpp
#include <hip/hip_runtime.h>
#include <hip/hip_bf16.h>
#include <type_traits>

#define STAMPS 0
#define WIN 4
#define HEAD_WIN 4
#define HPRIO 3
#define QSLEEP 8
#define SPEC_TH 3
#define TSTAMP 0
#define PACE 0
#define CPA 7
#define CPB 11
#define PACE_OFF 130
#define WIN2 WIN
#define LD_MID 16
#define ST_MID 16
#define LD_END 16
#define ST_END 16
#define MEAS 0
#define MWG 5
#define NO_FFN 0

namespace {

typedef unsigned short u16;
typedef short s16x4 __attribute__((ext_vector_type(4)));
typedef __bf16 bf16x8 __attribute__((ext_vector_type(8)));
typedef __bf16 bf16x2 __attribute__((ext_vector_type(2)));
typedef float f32x2 __attribute__((ext_vector_type(2)));
typedef float f32x4 __attribute__((ext_vector_type(4)));
typedef unsigned u32x2 __attribute__((ext_vector_type(2)));
typedef unsigned u32x4 __attribute__((ext_vector_type(4)));
typedef __amdgpu_buffer_rsrc_t rsrc_t;
#define LAS __attribute__((address_space(3)))
#define GAS __attribute__((address_space(1)))

constexpr int NWG = 256, NTHR = 320;
constexpr int DM = 512, HQD = 4096, SK = 32768, DH = 128, FF = 1792;
constexpr int NSLOT = 3, NSTEP = 32, NPRE = 5;
constexpr int KV_BYTES = 2 * 8 * SK * DH * 2;
constexpr float SCALE = 0.08838834764831845f, L2E = 1.4426950408889634f;
constexpr float M_INIT = -3.0e38f;
constexpr unsigned SPIN_CAP = 1u << 18;

constexpr int WS_EPOCH = 0;
constexpr int WS_Q = 4096;
constexpr int WS_H = WS_Q + HQD * 8;
constexpr int WS_ML = WS_H + FF * 8;
constexpr int WS_PC = WS_ML + NWG * 64;
constexpr int WS_QP = 131072;
constexpr int WS_HP = WS_QP + 4 * HQD * 16;
constexpr int WS_FG = WS_HP + 4 * FF * 16;
constexpr int WS_P2 = WS_FG + 14 * DM * 16;
constexpr int WS_P1 = WS_P2 + 64 * DM * 16;
constexpr int WS_END = WS_P1 + NWG * 4096;
constexpr int WS_STAMP = 8 << 20;
static_assert(WS_PC + 512 <= WS_QP, "workspace map");

constexpr int L_RING = 0;
constexpr int L_WQ = 4 * NSLOT * 8192;
constexpr int L_WF = L_WQ + 16384;
constexpr int L_WO = L_WF + 16384;
constexpr int L_XN = L_WO + 16384;
constexpr int L_QI = L_XN + 2048;
constexpr int L_OS = L_QI + 1024;
constexpr int L_HS = L_OS + 256;
constexpr int L_ML = L_HS + 512;
constexpr int L_TAG = L_ML + 128;
constexpr int L_TL = L_TAG + 16;
constexpr int L_TS = L_TL + 16;
constexpr int L_END = L_TS + 32;

__device__ __forceinline__ float rbf(float f) { return (float)(__bf16)f; }
__device__ __forceinline__ unsigned pk2(float lo, float hi) {
  bf16x2 v = {(__bf16)lo, (__bf16)hi};
  return __builtin_bit_cast(unsigned, v);
}
__device__ __forceinline__ float bflo(unsigned u) { return __uint_as_float(u << 16); }
__device__ __forceinline__ float bfhi(unsigned u) { return __uint_as_float(u & 0xffff0000u); }
template <int CTRL> __device__ __forceinline__ float dpp(float x) {
  return __builtin_bit_cast(float, __builtin_amdgcn_mov_dpp(__builtin_bit_cast(int, x), CTRL, 0xf, 0xf, true));
}
constexpr int XOR1 = 0xB1, XOR2 = 0x4E, ROR4 = 0x124, ROR8 = 0x128;
__device__ __forceinline__ float swap16_sum(float a, float b) {
  auto s = __builtin_amdgcn_permlane16_swap(__float_as_uint(a), __float_as_uint(b), false, false);
  return __uint_as_float(s[0]) + __uint_as_float(s[1]);
}
__device__ __forceinline__ float swap32_sum(float a, float b) {
  auto s = __builtin_amdgcn_permlane32_swap(__float_as_uint(a), __float_as_uint(b), false, false);
  return __uint_as_float(s[0]) + __uint_as_float(s[1]);
}
__device__ __forceinline__ float rows_sum(float x) { x = swap16_sum(x, x); return swap32_sum(x, x); }
__device__ __forceinline__ float rows_max(float x) {
  auto s = __builtin_amdgcn_permlane16_swap(__float_as_uint(x), __float_as_uint(x), false, false);
  x = fmaxf(__uint_as_float(s[0]), __uint_as_float(s[1]));
  auto t = __builtin_amdgcn_permlane32_swap(__float_as_uint(x), __float_as_uint(x), false, false);
  return fmaxf(__uint_as_float(t[0]), __uint_as_float(t[1]));
}
__device__ __forceinline__ float quads_sum(float x) { x += dpp<ROR4>(x); x += dpp<ROR8>(x); return rows_sum(x); }
__device__ __forceinline__ float quads_max(float x) { x = fmaxf(x, dpp<ROR4>(x)); x = fmaxf(x, dpp<ROR8>(x)); return rows_max(x); }
__device__ __forceinline__ float wave_sum(float x) { x += dpp<XOR1>(x); x += dpp<XOR2>(x); return quads_sum(x); }
__device__ __forceinline__ float ex2(float x) { return __builtin_amdgcn_exp2f(x); }
__device__ __forceinline__ float silu(float v) { return v * __builtin_amdgcn_rcpf(1.f + ex2(-v * L2E)); }

template <int N> __device__ __forceinline__ void wait_vm() {
  __builtin_amdgcn_sched_barrier(0);
  asm volatile("s_waitcnt vmcnt(%0)" ::"i"(N) : "memory");
}
__device__ __forceinline__ void wait_lds() { asm volatile("s_waitcnt lgkmcnt(0)" ::: "memory"); }
__device__ __forceinline__ void wg_barrier() {
  asm volatile("s_waitcnt lgkmcnt(0)\n\ts_barrier" ::: "memory");
}
__device__ __forceinline__ unsigned now32() { return (unsigned)__builtin_amdgcn_s_memrealtime(); }
__device__ __forceinline__ rsrc_t make_rsrc(const void* p, int bytes) {
  return __builtin_amdgcn_make_buffer_rsrc((void*)p, (short)0, bytes, 0x00020000);
}
template <int AUX = LD_MID> __device__ __forceinline__ u32x4 ld_dev(rsrc_t r, unsigned voff, unsigned soff) { return __builtin_amdgcn_raw_buffer_load_b128(r, voff, soff, AUX); }
template <int AUX = ST_MID> __device__ __forceinline__ void st_dev(rsrc_t r, u32x4 v, unsigned voff, unsigned soff) { __builtin_amdgcn_raw_buffer_store_b128(v, r, voff, soff, AUX); }
__device__ __forceinline__ s16x4 tr_read(unsigned addr) { return __builtin_amdgcn_ds_read_tr16_b64_v4i16((LAS s16x4*)(size_t)addr); }
template <typename T> __device__ __forceinline__ T lds_ld(unsigned addr) { return *(const LAS T*)(size_t)addr; }
template <typename T> __device__ __forceinline__ void lds_st(unsigned addr, T v) { *(LAS T*)(size_t)addr = v; }

__device__ __forceinline__ void dma_piece(rsrc_t r, unsigned voff, unsigned soff, unsigned dst) {
  asm volatile("s_mov_b32 m0, %0\n\ts_nop 0\n\tbuffer_load_dwordx4 %1, %2, %3 offen nt lds" ::"s"(dst), "v"(voff), "s"(r), "s"(soff) : "memory");
}
__device__ __forceinline__ void dma_half(rsrc_t r, unsigned v0, unsigned v1, unsigned v2, unsigned v3, unsigned soff, unsigned dst) {
  asm volatile(
      "s_mov_b32 m0, %0\n\ts_nop 0\n\t"
      "buffer_load_dwordx4 %1, %5, %6 offen nt lds\n\t"
      "buffer_load_dwordx4 %2, %5, %6 offen offset:1024 nt lds\n\t"
      "buffer_load_dwordx4 %3, %5, %6 offen offset:2048 nt lds\n\t"
      "buffer_load_dwordx4 %4, %5, %6 offen offset:3072 nt lds"
      ::"s"(dst), "v"(v0), "v"(v1), "v"(v2), "v"(v3), "s"(r), "s"(soff)
      : "memory");
}
template <int A> __device__ __forceinline__ f32x4 mfma4(s16x4 a, s16x4 b, f32x4 c) { return __builtin_amdgcn_mfma_f32_4x4x4bf16_1k(a, b, c, 4, A, 0); }
template <int A> struct Proj {
  static __device__ __forceinline__ void run(const s16x4 (&av)[2], unsigned ba, f32x4 (&c)[4]) {
    c[A & 3] = mfma4<A & 15>(av[A >> 4], tr_read(ba + A * 512), c[A & 3]);
    if constexpr (A + 1 < 32) Proj<A + 1>::run(av, ba, c);
  }
};
__device__ __forceinline__ f32x2 proj64(unsigned a_img, int a_stride, unsigned w_img, int lane) {
  const int q = (lane & 15) >> 2, p = lane & 3;
  const unsigned aa = a_img + (lane & 1) * a_stride + (lane >> 2) * 8;
  const s16x4 av[2] = {lds_ld<s16x4>(aa), lds_ld<s16x4>(aa + 128)};
  const unsigned ba = w_img + q * 128 + (((2 * (lane >> 4) + (p >> 1)) ^ ((q >> 1) << 2)) << 4) + (p & 1) * 8;
  f32x4 c[4] = {};
  Proj<0>::run(av, ba, c);
  return f32x2{(c[0][0] + c[1][0]) + (c[2][0] + c[3][0]), (c[0][1] + c[1][1]) + (c[2][1] + c[3][1])};
}
__device__ __forceinline__ f32x2 gather_quarters(rsrc_t rws, unsigned base, int quarter_bytes, unsigned tag, int lane) {
  u32x4 g;
  for (unsigned spin = 0; spin < SPIN_CAP; ++spin) {
    asm volatile("" ::: "memory");
    g = ld_dev(rws, (lane >> 4) * quarter_bytes + (lane & 15) * 16, base);
    if (__all((g[1] == tag) & (g[3] == tag))) break;
    __builtin_amdgcn_s_sleep(QSLEEP);
  }
  return f32x2{rows_sum(__uint_as_float(g[0])), rows_sum(__uint_as_float(g[2]))};
}

struct KFrag { bf16x8 k[4]; };
struct VFrag { s16x4 v[4][2]; };
struct State { float m, l; f32x4 o[2]; };

__device__ __forceinline__ void read_k(KFrag& t, const unsigned (&ka)[4], int off) {
#pragma unroll
  for (int s = 0; s < 4; ++s) t.k[s] = lds_ld<bf16x8>(ka[s] + off);
  asm volatile("s_waitcnt lgkmcnt(0)" : "+v"(t.k[0]), "+v"(t.k[1]), "+v"(t.k[2]), "+v"(t.k[3])::"memory");
}
__device__ __forceinline__ void read_v(VFrag& t, const unsigned (&va)[2], int off) {
#pragma unroll
  for (int a = 0; a < 4; ++a)
#pragma unroll
    for (int h = 0; h < 2; ++h) t.v[a][h] = tr_read(va[h] + off + a * 1024);
  asm volatile("s_waitcnt lgkmcnt(0)"
               : "+v"(t.v[0][0]), "+v"(t.v[0][1]), "+v"(t.v[1][0]), "+v"(t.v[1][1]), "+v"(t.v[2][0]), "+v"(t.v[2][1]), "+v"(t.v[3][0]), "+v"(t.v[3][1])
               :
               : "memory");
}
__device__ __forceinline__ s16x4 score_part(const KFrag& t, const bf16x8 (&qf)[4], State& st) {
  f32x4 sc = {0.f, 0.f, 0.f, 0.f};
#pragma unroll
  for (int s = 0; s < 4; ++s) sc = __builtin_amdgcn_mfma_f32_16x16x32_bf16(t.k[s], qf[s], sc, 0, 0, 0);
  float x[4];
#pragma unroll
  for (int r = 0; r < 4; ++r) x[r] = rbf(rbf(sc[r]) * SCALE);
  const float mn = fmaxf(st.m, rows_max(fmaxf(fmaxf(x[0], x[1]), fmaxf(x[2], x[3]))));
  const float alpha = ex2((st.m - mn) * L2E);
  st.m = mn;
  float p[4];
#pragma unroll
  for (int r = 0; r < 4; ++r) p[r] = ex2((x[r] - mn) * L2E);
  st.l = fmaf(st.l, alpha, (p[0] + p[1]) + (p[2] + p[3]));
  const float a0 = dpp<0x00>(alpha), a1 = dpp<0x55>(alpha), a2 = dpp<0xAA>(alpha), a3 = dpp<0xFF>(alpha);
#pragma unroll
  for (int h = 0; h < 2; ++h) { st.o[h][0] *= a0; st.o[h][1] *= a1; st.o[h][2] *= a2; st.o[h][3] *= a3; }
  const u32x2 pp = {pk2(p[0], p[1]), pk2(p[2], p[3])};
  return __builtin_bit_cast(s16x4, pp);
}
__device__ __forceinline__ void value_part(const VFrag& t, s16x4 pa, State& st) {
#pragma unroll
  for (int h = 0; h < 2; ++h) st.o[h] = mfma4<0>(pa, t.v[0][h], st.o[h]);
#pragma unroll
  for (int h = 0; h < 2; ++h) st.o[h] = mfma4<4>(pa, t.v[1][h], st.o[h]);
#pragma unroll
  for (int h = 0; h < 2; ++h) st.o[h] = mfma4<8>(pa, t.v[2][h], st.o[h]);
#pragma unroll
  for (int h = 0; h < 2; ++h) st.o[h] = mfma4<12>(pa, t.v[3][h], st.o[h]);
}

#define STAMP(i) do {} while (0)
#define STAMP_FLUSH(base, n) do {} while (0)

__global__ void __launch_bounds__(NTHR, 1)
fused_decode_kernel(const u16* __restrict__ x, const u16* __restrict__ kc, const u16* __restrict__ vc, const u16* __restrict__ wq,
                    const u16* __restrict__ wo, const u16* __restrict__ w1, const u16* __restrict__ w2, u16* __restrict__ out, char* ws) {
  extern __shared__ __attribute__((aligned(16))) unsigned char lds[];
  const unsigned lds0 = (unsigned)(size_t)(LAS unsigned char*)lds;
  const int lane = threadIdx.x & 63, wave = __builtin_amdgcn_readfirstlane(threadIdx.x >> 6), wg = blockIdx.x;
  const int xg = wg & 7, xi = wg >> 3;
  const rsrc_t rws = make_rsrc(ws, WS_END);
  STAMP(0);

  if (wave < 4) {
    const rsrc_t rk = make_rsrc(kc, KV_BYTES), rv = make_rsrc(vc, KV_BYTES);
    const int g = lane >> 4, c = lane & 15;
    unsigned vk[4];
#pragma unroll
    for (int u = 0; u < 4; ++u) vk[u] = g * 256 + ((c ^ (4 * u + g)) << 4);
    const unsigned vv = g * 256 + ((c ^ (g << 2)) << 4);
    const unsigned ring = lds0 + L_RING + wave * (NSLOT * 8192);
    const unsigned tile0 = (unsigned)wg * (2048 * 256) + wave * 4096;
    unsigned ka[4], va[2];
#pragma unroll
    for (int s = 0; s < 4; ++s) ka[s] = ring + c * 256 + (((4 * s + g) ^ c) << 4);
#pragma unroll
    for (int h = 0; h < 2; ++h) va[h] = ring + 4096 + (c >> 2) * 256 + (((8 * h + 2 * g + ((c & 3) >> 1)) ^ ((c >> 2) << 2)) << 4) + (c & 1) * 8;
    auto issue_k = [&](int t, int slot) { dma_half(rk, vk[0], vk[1], vk[2], vk[3], tile0 + t * 16384, ring + slot * 8192); };
    auto issue_v = [&](int t, int slot) { dma_half(rv, vv, vv, vv, vv, tile0 + t * 16384, ring + slot * 8192 + 4096); };

    wg_barrier();
#pragma unroll
    for (int t = 0; t < NSLOT; ++t) {
      wait_vm<HEAD_WIN>(); issue_k(t, t);
      wait_vm<HEAD_WIN>(); issue_v(t, t);
    }
    unsigned tsv = TSTAMP ? (unsigned)__builtin_amdgcn_s_memrealtime() : 0u;
    KFrag pk[NPRE]; VFrag pv[NPRE];
#pragma unroll
    for (int t = 0; t < NPRE; ++t) {
      wait_vm<HEAD_WIN>(); read_k(pk[t], ka, (t % NSLOT) * 8192); issue_k(t + NSLOT, t % NSLOT);
      wait_vm<HEAD_WIN>();
      if (TSTAMP) { const unsigned tt = (unsigned)__builtin_amdgcn_s_memrealtime(); tsv = lane == t + 2 ? tt : tsv; }
      read_v(pv[t], va, (t % NSLOT) * 8192); issue_v(t + NSLOT, t % NSLOT);
    }
    STAMP(1);
    wg_barrier();
    STAMP(2);
    bf16x8 qf[4];
#pragma unroll
    for (int s = 0; s < 4; ++s) qf[s] = lds_ld<bf16x8>(lds0 + L_QI + (c & 3) * 256 + (4 * s + g) * 16);
    const unsigned tag = lds_ld<unsigned>(lds0 + L_TAG);

    State st;
    st.m = M_INIT; st.l = 0.f;
    st.o[0] = f32x4{0.f, 0.f, 0.f, 0.f}; st.o[1] = f32x4{0.f, 0.f, 0.f, 0.f};
    unsigned prev = now32();
    auto pace = [&](int n) {
      if (PACE) {
        const unsigned tl = __builtin_amdgcn_readfirstlane(*(volatile LAS unsigned*)(size_t)(lds0 + L_TL));
        const int d = (int)(tl - prev);
        if (tl != 0 && d > 0) {
          const unsigned due = prev + (unsigned)__builtin_amdgcn_readfirstlane((int)((float)d * __builtin_amdgcn_rcpf((float)n)));
          while ((int)(now32() - due) < 0) __builtin_amdgcn_s_sleep(1);
        }
        prev = now32();
      }
    };
    auto step = [&](int t, int slot, auto win) {
      KFrag kf; VFrag vf;
      wait_vm<win.value>(); read_k(kf, ka, slot * 8192);
      pace(2 * (NSTEP - NSLOT - t)); issue_k(t + NSLOT, slot);
      const s16x4 pa = score_part(kf, qf, st);
      wait_vm<win.value>();
      if (TSTAMP) { const unsigned tt = (unsigned)__builtin_amdgcn_s_memrealtime(); tsv = lane == t + 2 ? tt : tsv; }
      if (PACE && (t == CPA || t == CPB)) lds_st<unsigned>(lds0 + L_TS + (t == CPB ? 16 : 0) + wave * 4, now32() | 1u);
      read_v(vf, va, slot * 8192);
      pace(2 * (NSTEP - NSLOT - t) - 1); issue_v(t + NSLOT, slot);
      value_part(vf, pa, st);
    };
    using W1 = std::integral_constant<int, WIN>;
    using W2 = std::integral_constant<int, WIN2>;
#pragma unroll
    for (int j = 0; j < 2 * NSLOT; ++j) {
      step(NPRE + j, (NPRE + j) % NSLOT, W1{});
      if (j < NPRE) value_part(pv[j], score_part(pk[j], qf, st), st);
    }
    static_assert(2 * NSLOT >= NPRE && (NSTEP - NPRE) % NSLOT == 0, "tile schedule");
#pragma unroll
    for (int j = 0; j < NSLOT; ++j) step(NPRE + 2 * NSLOT + j, (NPRE + j) % NSLOT, W1{});
    for (int t0 = NPRE + 3 * NSLOT; t0 < NSTEP - NSLOT; t0 += NSLOT) {
#pragma unroll
      for (int j = 0; j < NSLOT; ++j) step(t0 + j, (NPRE + j) % NSLOT, W2{});
    }
    STAMP(3);
#pragma unroll
    for (int j = 0; j < NSLOT; ++j) {
      KFrag kf; VFrag vf;
      if (j == 0) wait_vm<20>(); else if (j == 1) wait_vm<12>(); else wait_vm<4>();
      read_k(kf, ka, ((NPRE + j) % NSLOT) * 8192);
      const s16x4 pa = score_part(kf, qf, st);
      if (j == 0) wait_vm<16>(); else if (j == 1) wait_vm<8>(); else wait_vm<0>();
      if (j == NSLOT - 1) STAMP(4);
      if (TSTAMP && j == NSLOT - 1) { const unsigned tt = (unsigned)__builtin_amdgcn_s_memrealtime(); tsv = lane == 31 ? tt : tsv;
        unsigned xcc; asm volatile("s_getreg_b32 %0, hwreg(HW_REG_XCC_ID, 0, 4)" : "=s"(xcc)); tsv = lane == 32 ? xcc : tsv;
        ((GAS unsigned*)(ws + WS_STAMP + (1 << 20)))[(wg * 4 + wave) * 64 + lane] = tsv; }
      read_v(vf, va, ((NPRE + j) % NSLOT) * 8192);
      value_part(vf, pa, st);
    }

    const float lsum = rows_sum(st.l);
#pragma unroll
    for (int h = 0; h < 2; ++h)
#pragma unroll
      for (int r = 0; r < 4; ++r) lds_st<float>(ring + (r * 128 + h * 64 + lane) * 4, st.o[h][r]);
    if (lane < 4) lds_st<f32x2>(lds0 + L_ML + (wave * 4 + lane) * 8, f32x2{st.m, lsum});
    STAMP(5);
    wg_barrier();
    STAMP(6);
    {
      f32x2 ml[4], ov[4];
#pragma unroll
      for (int w = 0; w < 4; ++w) {
        ml[w] = lds_ld<f32x2>(lds0 + L_ML + (w * 4 + wave) * 8);
        ov[w] = lds_ld<f32x2>(lds0 + L_RING + w * (NSLOT * 8192) + (wave * 128 + 2 * lane) * 4);
      }
      const float M = fmaxf(fmaxf(ml[0][0], ml[1][0]), fmaxf(ml[2][0], ml[3][0]));
      float L = 0.f, o0 = 0.f, o1 = 0.f;
#pragma unroll
      for (int w = 0; w < 4; ++w) {
        const float wt = ex2((ml[w][0] - M) * L2E);
        L = fmaf(wt, ml[w][1], L); o0 = fmaf(wt, ov[w][0], o0); o1 = fmaf(wt, ov[w][1], o1);
      }
      st_dev<ST_END>(rws, u32x4{__float_as_uint(o0), tag, __float_as_uint(o1), tag}, lane * 16, WS_P1 + wg * 4096 + wave * 1024);
      if (lane == 0) st_dev<ST_END>(rws, u32x4{__float_as_uint(M), tag, __float_as_uint(L), tag}, 0, WS_ML + wg * 64 + wave * 16);
    }
    const int ks = xg * 8 + (xi >> 2), cs = xi & 3;
    s16x4 wf[16];
    if (wave < 2) {
      const unsigned wa = lds0 + L_WO + (c >> 2) * 256 + (((8 * wave + 2 * g + ((c & 3) >> 1)) ^ ((c >> 2) << 2)) << 4) + (c & 1) * 8;
#pragma unroll
      for (int a = 0; a < 16; ++a) wf[a] = tr_read(wa + a * 1024);
    }
    STAMP(7);
    {
      const int b = wave >> 1, half = wave & 1, sp = lane >> 2, p = lane & 3;
      const int src0 = (b * 8 + xg) * 16, hl = (ks >> 1) & 3;
      const unsigned vo = sp * 4096 + p * 16, so = WS_P1 + src0 * 4096 + hl * 1024 + ((ks & 1) * 64 + half * 32) * 8;
      const unsigned vm = sp * 64, sm = WS_ML + src0 * 64 + hl * 16;
      u32x4 gv[4], gm;
      for (unsigned spin = 0; spin < SPIN_CAP; ++spin) {
        asm volatile("" ::: "memory");
        gm = ld_dev<LD_END>(rws, vm, sm);
        if (__popcll(__ballot(gm[1] != tag)) <= 4 * SPEC_TH) break;
        __builtin_amdgcn_s_sleep(4);
      }
      for (unsigned spin = 0; spin < SPIN_CAP; ++spin) {
        asm volatile("" ::: "memory");
        gm = ld_dev<LD_END>(rws, vm, sm);
#pragma unroll
        for (int i = 0; i < 4; ++i) gv[i] = ld_dev<LD_END>(rws, vo + i * 64, so);
        bool ok = (gm[1] == tag) & (gm[3] == tag);
#pragma unroll
        for (int i = 0; i < 4; ++i) ok = ok & (gv[i][1] == tag) & (gv[i][3] == tag);
        if (__all(ok)) break;
      }
      STAMP(8);
      const float ms = __uint_as_float(gm[0]), M = quads_max(ms);
      const float wt = ex2((ms - M) * L2E);
      const float inv = __builtin_amdgcn_rcpf(quads_sum(wt * __uint_as_float(gm[2])));
      float e[2];
#pragma unroll
      for (int j = 0; j < 2; ++j) {
        const float u0 = swap32_sum(wt * __uint_as_float(gv[0][2 * j]), wt * __uint_as_float(gv[2][2 * j]));
        const float u1 = swap32_sum(wt * __uint_as_float(gv[1][2 * j]), wt * __uint_as_float(gv[3][2 * j]));
        float v = swap16_sum(u0, u1);
        v += dpp<ROR4>(v); v += dpp<ROR8>(v);
        e[j] = v * inv;
      }
      if ((c >> 2) == 0) lds_st<unsigned>(lds0 + L_OS + b * 128 + (half * 16 + g * 4 + p) * 4, pk2(e[0], e[1]));
    }
    wg_barrier();
    STAMP(9);
    if (wave < 2) {
      const s16x4 oa = lds_ld<s16x4>(lds0 + L_OS + (lane & 1) * 128 + (lane >> 2) * 8);
      f32x4 c0 = {0.f, 0.f, 0.f, 0.f}, c1 = {0.f, 0.f, 0.f, 0.f};
      c0 = mfma4<0>(oa, wf[0], c0);   c1 = mfma4<1>(oa, wf[1], c1);   c0 = mfma4<2>(oa, wf[2], c0);   c1 = mfma4<3>(oa, wf[3], c1);
      c0 = mfma4<4>(oa, wf[4], c0);   c1 = mfma4<5>(oa, wf[5], c1);   c0 = mfma4<6>(oa, wf[6], c0);   c1 = mfma4<7>(oa, wf[7], c1);
      c0 = mfma4<8>(oa, wf[8], c0);   c1 = mfma4<9>(oa, wf[9], c1);   c0 = mfma4<10>(oa, wf[10], c0); c1 = mfma4<11>(oa, wf[11], c1);
      c0 = mfma4<12>(oa, wf[12], c0); c1 = mfma4<13>(oa, wf[13], c1); c0 = mfma4<14>(oa, wf[14], c0); c1 = mfma4<15>(oa, wf[15], c1);
      st_dev<ST_END>(rws, u32x4{__float_as_uint(c0[0] + c1[0]), tag, __float_as_uint(c0[1] + c1[1]), tag}, lane * 16, WS_P2 + ks * 8192 + (cs * 128 + wave * 64) * 16);
    }
    STAMP(10);
    STAMP_FLUSH(wave * 12, 12);
  } else {
    __builtin_amdgcn_s_setprio(HPRIO);
    const unsigned mt0 = now32();
    unsigned mt1 = mt0;
    const int cg = xg * 32 + xi;
    const rsrc_t rx = make_rsrc(x, 2 * DM * 2), rq = make_rsrc(wq, DM * HQD * 2);
    unsigned ev, xo0, xo1;
    u32x4 x0, x1;
    asm volatile(
        "s_nop 4\n\t"
        "buffer_load_dwordx4 %0, %5, %6, 0 offen\n\t"
        "buffer_load_dwordx4 %1, %5, %6, 0 offen offset:1024\n\t"
        "buffer_load_dword %2, off, %7, 0 sc1\n\t"
        "buffer_load_ushort %3, %8, %6, 0 offen\n\t"
        "buffer_load_ushort %4, %8, %6, 0 offen offset:1024"
        : "=&v"(x0), "=&v"(x1), "=&v"(ev), "=&v"(xo0), "=&v"(xo1)
        : "v"(lane * 16), "s"(rx), "s"(rws), "v"((cg * 2 + (lane & 1)) * 2)
        : "memory");
    const int qk = cg >> 6, qc = cg & 63;
    const bool f1 = xi < 14 && !NO_FFN, f2 = xi >= 14 && xi < 28 && !NO_FFN;
    const int fi = xg * 14 + (xi < 14 ? xi : xi - 14);
    const int hk = fi / 28, hc = fi % 28;
    const int gk = fi >> 3, gc = fi & 7;
    {
      const unsigned sw = ((lane & 7) ^ (((lane >> 4) & 1) << 2)) << 4;
      const unsigned vq = (lane >> 3) * (HQD * 2) + sw;
#pragma unroll
      for (int i = 0; i < 16; ++i) dma_piece(rq, vq, (qk * 128 + 8 * i) * (HQD * 2) + qc * 128, lds0 + L_WQ + i * 1024);
      const rsrc_t rf = make_rsrc(f1 ? w1 : w2, f1 || f2 ? DM * FF * 2 : 0);
      const int ld = f1 ? FF * 2 : DM * 2;
      const unsigned vf = (lane >> 3) * ld + sw, sf = f1 ? hk * 128 * (FF * 2) + hc * 128 : gk * 128 * (DM * 2) + gc * 128;
#pragma unroll
      for (int i = 0; i < 16; ++i) dma_piece(rf, vf, sf + 8 * i * ld, lds0 + L_WF + i * 1024);
      const int ks = xg * 8 + (xi >> 2), cs = xi & 3;
      const rsrc_t ro = make_rsrc(wo, HQD * DM * 2);
      const unsigned vo = (lane >> 4) * (DM * 2) + (((lane & 15) ^ ((lane >> 4) << 2)) << 4);
#pragma unroll
      for (int i = 0; i < 16; ++i) dma_piece(ro, vo, (ks * 64 + 4 * i) * (DM * 2) + cs * 256, lds0 + L_WO + i * 1024);
    }
    wg_barrier();
    asm volatile("s_waitcnt vmcnt(48)" : "+v"(x0), "+v"(x1), "+v"(ev), "+v"(xo0), "+v"(xo1)::"memory");
    if (MEAS == 2) mt1 = now32();
    const unsigned tag = __builtin_amdgcn_readfirstlane(ev) + 1u;
    {
      float a[2][8];
#pragma unroll
      for (int j = 0; j < 4; ++j) { a[0][2 * j] = bflo(x0[j]); a[0][2 * j + 1] = bfhi(x0[j]); a[1][2 * j] = bflo(x1[j]); a[1][2 * j + 1] = bfhi(x1[j]); }
#pragma unroll
      for (int b = 0; b < 2; ++b) {
        const float mean = wave_sum(((a[b][0] + a[b][1]) + (a[b][2] + a[b][3])) + ((a[b][4] + a[b][5]) + (a[b][6] + a[b][7]))) * (1.f / DM);
        float q = 0.f;
#pragma unroll
        for (int j = 0; j < 8; ++j) q = fmaf(a[b][j] - mean, a[b][j] - mean, q);
        const float var = wave_sum(q) * (1.f / DM);
        const float mb = rbf(mean), inv = __builtin_amdgcn_rcpf(rbf(__builtin_amdgcn_sqrtf(rbf(rbf(var) + 1e-5f))));
        u32x4 xn;
#pragma unroll
        for (int j = 0; j < 4; ++j) xn[j] = pk2(rbf(a[b][2 * j] - mb) * inv, rbf(a[b][2 * j + 1] - mb) * inv);
        lds_st<u32x4>(lds0 + L_XN + b * 1024 + lane * 16, xn);
      }
    }
    if (lane == 0) lds_st<unsigned>(lds0 + L_TAG, tag);
    if (lane < 12) lds_st<unsigned>(lds0 + L_TL + lane * 4, 0u);
    asm volatile("s_waitcnt vmcnt(32) lgkmcnt(0)" ::: "memory");
    STAMP(2);
    {
      const f32x2 y = proj64(lds0 + L_XN + qk * 256, 1024, lds0 + L_WQ, lane);
      st_dev(rws, u32x4{__float_as_uint(y[0]), tag, __float_as_uint(y[1]), tag}, lane * 16, WS_QP + (qk * HQD + qc * 64) * 16);
    }
    if (f1) {
      wait_vm<17>();
      const f32x2 y = proj64(lds0 + L_XN + hk * 256, 1024, lds0 + L_WF, lane);
      st_dev(rws, u32x4{__float_as_uint(y[0]), tag, __float_as_uint(y[1]), tag}, lane * 16, WS_HP + (hk * FF + hc * 64) * 16);
    }
    {
      const f32x2 q = gather_quarters(rws, WS_QP + (qc * 64 + qk * 16) * 16, HQD * 16, tag, lane);
      if (lane < 16) __builtin_amdgcn_raw_buffer_store_b64(u32x2{pk2(q[0], q[1]), tag}, rws, lane * 8, WS_Q + (qc * 64 + qk * 16) * 8, ST_MID);
    }
    STAMP(3);
    {
      const int bh = wg >> 4, b = bh >> 3, kvh = bh & 7;
      u32x4 gq[4];
      for (unsigned spin = 0; spin < SPIN_CAP; ++spin) {
        asm volatile("" ::: "memory");
#pragma unroll
        for (int i = 0; i < 4; ++i) gq[i] = ld_dev(rws, lane * 16 + i * 1024, WS_Q + kvh * 4096);
        bool ok = true;
#pragma unroll
        for (int i = 0; i < 4; ++i) ok = ok & (gq[i][1] == tag) & (gq[i][3] == tag);
        if (__all(ok)) break;
        __builtin_amdgcn_s_sleep(QSLEEP);
      }
#pragma unroll
      for (int i = 0; i < 4; ++i)
        lds_st<unsigned>(lds0 + L_QI + i * 256 + lane * 4, b ? (gq[i][0] >> 16) | (gq[i][2] & 0xffff0000u) : (gq[i][0] & 0xffffu) | (gq[i][2] << 16));
    }
    if (MEAS == 3) mt1 = now32();
    wg_barrier();

    if (f1) {
      const f32x2 h = gather_quarters(rws, WS_HP + (hc * 64 + hk * 16) * 16, FF * 16, tag, lane);
      if (lane < 16) __builtin_amdgcn_raw_buffer_store_b64(u32x2{pk2(silu(rbf(h[0])), silu(rbf(h[1]))), tag}, rws, lane * 8, WS_H + (hc * 64 + hk * 16) * 8, ST_MID);
    } else if (f2) {
      u32x4 gh;
      for (unsigned spin = 0; spin < SPIN_CAP; ++spin) {
        asm volatile("" ::: "memory");
        gh = ld_dev(rws, lane * 16, WS_H + gk * 1024);
        if (__all((gh[1] == tag) & (gh[3] == tag))) break;
        __builtin_amdgcn_s_sleep(32);
      }
      lds_st<unsigned>(lds0 + L_HS + lane * 4, (gh[0] & 0xffffu) | (gh[2] << 16));
      lds_st<unsigned>(lds0 + L_HS + 256 + lane * 4, (gh[0] >> 16) | (gh[2] & 0xffff0000u));
      wait_lds();
      const f32x2 y = proj64(lds0 + L_HS, 256, lds0 + L_WF, lane);
      st_dev(rws, u32x4{__float_as_uint(y[0]), tag, __float_as_uint(y[1]), tag}, lane * 16, WS_FG + (gk * DM + gc * 64) * 16);
    }
    STAMP(5);
    if (PACE) {
      u32x4 ta, tb;
      for (unsigned spin = 0; spin < SPIN_CAP; ++spin) {
        ta = *(volatile LAS u32x4*)(size_t)(lds0 + L_TS); tb = *(volatile LAS u32x4*)(size_t)(lds0 + L_TS + 16);
        if (__all((tb[0] != 0) & (tb[1] != 0) & (tb[2] != 0) & (tb[3] != 0))) break;
        __builtin_amdgcn_s_sleep(8);
      }
      const unsigned base = tb[0];
      if (wg < 32 && lane == 0) {
        const unsigned a = base + (unsigned)(((int)(ta[0] - base) + (int)(ta[1] - base) + (int)(ta[2] - base) + (int)(ta[3] - base)) >> 2);
        const unsigned b = base + (unsigned)(((int)(tb[1] - base) + (int)(tb[2] - base) + (int)(tb[3] - base)) >> 2);
        st_dev(rws, u32x4{a, tag, b, tag}, 0, WS_PC + wg * 16);
      }
      u32x4 gp;
      for (unsigned spin = 0; spin < SPIN_CAP; ++spin) {
        asm volatile("" ::: "memory");
        gp = ld_dev(rws, (lane & 31) * 16, WS_PC);
        if (__all((gp[1] == tag) & (gp[3] == tag))) break;
        __builtin_amdgcn_s_sleep(16);
      }
      const float r = (float)(CPB - CPA) * __builtin_amdgcn_rcpf((float)(int)(gp[2] - gp[0]));
      const float fin = ((float)(64 * (NSTEP - 1 - (CPB + 2))) + wave_sum(r * (float)(int)(gp[2] - base))) * __builtin_amdgcn_rcpf(wave_sum(r));
      if (lane == 0) *(volatile LAS unsigned*)(size_t)(lds0 + L_TL) = (base + (unsigned)((int)fin - PACE_OFF)) | 1u;
    }
    STAMP(11);
    wg_barrier();
    if (MEAS == 4) mt1 = now32();
    float g0, g1;
    {
      u32x4 gf = {};
      const int pr = lane >> 1;
      const unsigned vo = (pr < 14 ? pr : 0) * (DM * 16) + (lane & 1) * 16;
      for (unsigned spin = 0; spin < (NO_FFN ? 0 : SPIN_CAP); ++spin) {
        asm volatile("" ::: "memory");
        gf = ld_dev<LD_END>(rws, vo, WS_FG + cg * 32);
        if (__all((gf[1] == tag) & (gf[3] == tag))) break;
      }
      float s0 = pr < 14 ? __uint_as_float(gf[0]) : 0.f, s1 = pr < 14 ? __uint_as_float(gf[2]) : 0.f;
      s0 += dpp<XOR2>(s0); s1 += dpp<XOR2>(s1);
      g0 = rbf(silu(rbf(quads_sum(s0))));
      g1 = rbf(silu(rbf(quads_sum(s1))));
    }
    STAMP(7);
    wg_barrier();
    STAMP(8);
    {
      u32x4 gs[2];
      const unsigned vo = (lane >> 1) * 8192 + (lane & 1) * 16;
      for (unsigned spin = 0; spin < SPIN_CAP; ++spin) {
        asm volatile("" ::: "memory");
#pragma unroll
        for (int i = 0; i < 2; ++i) gs[i] = ld_dev<LD_END>(rws, vo, WS_P2 + i * 32 * 8192 + cg * 32);
        if (__all((gs[0][1] == tag) & (gs[0][3] == tag) & (gs[1][1] == tag) & (gs[1][3] == tag))) break;
      }
      STAMP(9);
      float s0 = __uint_as_float(gs[0][0]) + __uint_as_float(gs[1][0]), s1 = __uint_as_float(gs[0][2]) + __uint_as_float(gs[1][2]);
      s0 += dpp<XOR2>(s0); s1 += dpp<XOR2>(s1);
      s0 = rbf(quads_sum(s0)); s1 = rbf(quads_sum(s1));
      if (lane < 2) {
        out[cg * 2 + lane] = __builtin_bit_cast(u16, (__bf16)(bflo(xo0) + rbf(s0 * g0)));
        out[DM + cg * 2 + lane] = __builtin_bit_cast(u16, (__bf16)(bflo(xo1) + rbf(s1 * g1)));
      }
      if (wg == 0 && lane == 0) __builtin_amdgcn_raw_buffer_store_b32(tag, rws, 0, WS_EPOCH, 16);
    }
    if (MEAS && wg == MWG) {
      const unsigned te = now32();
      if (MEAS == 1) mt1 = te;
      const unsigned due = te + 10u * (mt1 - mt0);
      while ((int)(now32() - due) < 0) __builtin_amdgcn_s_sleep(2);
    }
  }
}

}

extern "C" void kernel_launch(void* const* d_in, const int* in_sizes, int n_in, void* d_out, int out_size, void* d_ws, size_t ws_size,
                              hipStream_t stream) {
  (void)hipFuncSetAttribute((const void*)fused_decode_kernel, hipFuncAttributeMaxDynamicSharedMemorySize, L_END);
  fused_decode_kernel<<<NWG, NTHR, L_END, stream>>>((const u16*)d_in[0], (const u16*)d_in[1], (const u16*)d_in[2], (const u16*)d_in[3],
                                                    (const u16*)d_in[4], (const u16*)d_in[6], (const u16*)d_in[7], (u16*)d_out, (char*)d_ws);
}
```

```cpp
#include <hip/hip_runtime.h>
#include <hip/hip_bf16.h>
#include <type_traits>

#define STAMPS 0
#define WIN 4
#define HEAD_WIN 4
#define HPRIO 3
#define QSLEEP 8
#define SPEC_TH 3
#define TSTAMP 0
#define PACE 1
#define CPA 7
#define TS2 1800
#define TSD 150
#define RBOOST 1.03f
#define NSAMP 8
#define QA_AT 0
#define QB_AT 0
#define PACE_OFF 150
#define WIN2 12
#define LD_MID 16
#define ST_MID 16
#define LD_END 16
#define ST_END 16
#define NO_FFN 0

namespace {

typedef unsigned short u16;
typedef short s16x4 __attribute__((ext_vector_type(4)));
typedef __bf16 bf16x8 __attribute__((ext_vector_type(8)));
typedef __bf16 bf16x2 __attribute__((ext_vector_type(2)));
typedef float f32x2 __attribute__((ext_vector_type(2)));
typedef float f32x4 __attribute__((ext_vector_type(4)));
typedef unsigned u32x2 __attribute__((ext_vector_type(2)));
typedef unsigned u32x4 __attribute__((ext_vector_type(4)));
typedef __amdgpu_buffer_rsrc_t rsrc_t;
#define LAS __attribute__((address_space(3)))
#define GAS __attribute__((address_space(1)))

constexpr int NWG = 256, NTHR = 320;
constexpr int DM = 512, HQD = 4096, SK = 32768, DH = 128, FF = 1792;
constexpr int NSLOT = 3, NSTEP = 32, NPRE = 5;
constexpr int KV_BYTES = 2 * 8 * SK * DH * 2;
constexpr float SCALE = 0.08838834764831845f, L2E = 1.4426950408889634f;
constexpr float M_INIT = -3.0e38f;
constexpr unsigned SPIN_CAP = 1u << 18;

constexpr int WS_EPOCH = 0;
constexpr int WS_Q = 4096;
constexpr int WS_H = WS_Q + HQD * 8;
constexpr int WS_ML = WS_H + FF * 8;
constexpr int WS_PC = 98304;
constexpr int WS_QP = 131072;
constexpr int WS_HP = WS_QP + 4 * HQD * 16;
constexpr int WS_FG = WS_HP + 4 * FF * 16;
constexpr int WS_P2 = WS_FG + 14 * DM * 16;
constexpr int WS_P1 = WS_P2 + 64 * DM * 16;
constexpr int WS_END = WS_P1 + NWG * 4096;
constexpr int WS_STAMP = 8 << 20;
static_assert(WS_ML + NWG * 64 <= WS_PC && WS_PC + NWG * 128 <= WS_QP, "workspace map");

constexpr int L_RING = 0;
constexpr int L_WQ = 4 * NSLOT * 8192;
constexpr int L_WF = L_WQ + 16384;
constexpr int L_WO = L_WF + 16384;
constexpr int L_XN = L_WO + 16384;
constexpr int L_QI = L_XN + 2048;
constexpr int L_OS = L_QI + 1024;
constexpr int L_HS = L_OS + 256;
constexpr int L_ML = L_HS + 512;
constexpr int L_TAG = L_ML + 128;
constexpr int L_QR = L_TAG + 4;
constexpr int L_TL = L_TAG + 16;
constexpr int L_TS = L_TL + 16;
constexpr int L_TP = L_TS + 16;
constexpr int L_END = L_TP + 32;

__device__ __forceinline__ float rbf(float f) { return (float)(__bf16)f; }
__device__ __forceinline__ unsigned pk2(float lo, float hi) {
  bf16x2 v = {(__bf16)lo, (__bf16)hi};
  return __builtin_bit_cast(unsigned, v);
}
__device__ __forceinline__ float bflo(unsigned u) { return __uint_as_float(u << 16); }
__device__ __forceinline__ float bfhi(unsigned u) { return __uint_as_float(u & 0xffff0000u); }
template <int CTRL> __device__ __forceinline__ float dpp(float x) {
  return __builtin_bit_cast(float, __builtin_amdgcn_mov_dpp(__builtin_bit_cast(int, x), CTRL, 0xf, 0xf, true));
}
constexpr int XOR1 = 0xB1, XOR2 = 0x4E, ROR4 = 0x124, ROR8 = 0x128;
__device__ __forceinline__ float swap16_sum(float a, float b) {
  auto s = __builtin_amdgcn_permlane16_swap(__float_as_uint(a), __float_as_uint(b), false, false);
  return __uint_as_float(s[0]) + __uint_as_float(s[1]);
}
__device__ __forceinline__ float swap32_sum(float a, float b) {
  auto s = __builtin_amdgcn_permlane32_swap(__float_as_uint(a), __float_as_uint(b), false, false);
  return __uint_as_float(s[0]) + __uint_as_float(s[1]);
}
__device__ __forceinline__ float rows_sum(float x) { x = swap16_sum(x, x); return swap32_sum(x, x); }
__device__ __forceinline__ float rows_max(float x) {
  auto s = __builtin_amdgcn_permlane16_swap(__float_as_uint(x), __float_as_uint(x), false, false);
  x = fmaxf(__uint_as_float(s[0]), __uint_as_float(s[1]));
  auto t = __builtin_amdgcn_permlane32_swap(__float_as_uint(x), __float_as_uint(x), false, false);
  return fmaxf(__uint_as_float(t[0]), __uint_as_float(t[1]));
}
__device__ __forceinline__ float quads_sum(float x) { x += dpp<ROR4>(x); x += dpp<ROR8>(x); return rows_sum(x); }
__device__ __forceinline__ float quads_max(float x) { x = fmaxf(x, dpp<ROR4>(x)); x = fmaxf(x, dpp<ROR8>(x)); return rows_max(x); }
__device__ __forceinline__ float wave_sum(float x) { x += dpp<XOR1>(x); x += dpp<XOR2>(x); return quads_sum(x); }
__device__ __forceinline__ float ex2(float x) { return __builtin_amdgcn_exp2f(x); }
__device__ __forceinline__ float silu(float v) { return v * __builtin_amdgcn_rcpf(1.f + ex2(-v * L2E)); }

template <int N> __device__ __forceinline__ void wait_vm() {
  __builtin_amdgcn_sched_barrier(0);
  asm volatile("s_waitcnt vmcnt(%0)" ::"i"(N) : "memory");
}
__device__ __forceinline__ void wait_lds() { asm volatile("s_waitcnt lgkmcnt(0)" ::: "memory"); }
__device__ __forceinline__ void wg_barrier() {
  asm volatile("s_waitcnt lgkmcnt(0)\n\ts_barrier" ::: "memory");
}
__device__ __forceinline__ unsigned now32() { return (unsigned)__builtin_amdgcn_s_memrealtime(); }
__device__ __forceinline__ rsrc_t make_rsrc(const void* p, int bytes) {
  return __builtin_amdgcn_make_buffer_rsrc((void*)p, (short)0, bytes, 0x00020000);
}
template <int AUX = LD_MID> __device__ __forceinline__ u32x4 ld_dev(rsrc_t r, unsigned voff, unsigned soff) { return __builtin_amdgcn_raw_buffer_load_b128(r, voff, soff, AUX); }
template <int AUX = ST_MID> __device__ __forceinline__ void st_dev(rsrc_t r, u32x4 v, unsigned voff, unsigned soff) { __builtin_amdgcn_raw_buffer_store_b128(v, r, voff, soff, AUX); }
__device__ __forceinline__ s16x4 tr_read(unsigned addr) { return __builtin_amdgcn_ds_read_tr16_b64_v4i16((LAS s16x4*)(size_t)addr); }
template <typename T> __device__ __forceinline__ T lds_ld(unsigned addr) { return *(const LAS T*)(size_t)addr; }
template <typename T> __device__ __forceinline__ void lds_st(unsigned addr, T v) { *(LAS T*)(size_t)addr = v; }

__device__ __forceinline__ void dma_piece(rsrc_t r, unsigned voff, unsigned soff, unsigned dst) {
  asm volatile("s_mov_b32 m0, %0\n\ts_nop 0\n\tbuffer_load_dwordx4 %1, %2, %3 offen nt lds" ::"s"(dst), "v"(voff), "s"(r), "s"(soff) : "memory");
}
__device__ __forceinline__ void dma_half(rsrc_t r, unsigned v0, unsigned v1, unsigned v2, unsigned v3, unsigned soff, unsigned dst) {
  asm volatile(
      "s_mov_b32 m0, %0\n\ts_nop 0\n\t"
      "buffer_load_dwordx4 %1, %5, %6 offen nt lds\n\t"
      "buffer_load_dwordx4 %2, %5, %6 offen offset:1024 nt lds\n\t"
      "buffer_load_dwordx4 %3, %5, %6 offen offset:2048 nt lds\n\t"
      "buffer_load_dwordx4 %4, %5, %6 offen offset:3072 nt lds"
      ::"s"(dst), "v"(v0), "v"(v1), "v"(v2), "v"(v3), "s"(r), "s"(soff)
      : "memory");
}
template <int A> __device__ __forceinline__ f32x4 mfma4(s16x4 a, s16x4 b, f32x4 c) { return __builtin_amdgcn_mfma_f32_4x4x4bf16_1k(a, b, c, 4, A, 0); }
template <int A> struct Proj {
  static __device__ __forceinline__ void run(const s16x4 (&av)[2], unsigned ba, f32x4 (&c)[4]) {
    c[A & 3] = mfma4<A & 15>(av[A >> 4], tr_read(ba + A * 512), c[A & 3]);
    if constexpr (A + 1 < 32) Proj<A + 1>::run(av, ba, c);
  }
};
__device__ __forceinline__ f32x2 proj64(unsigned a_img, int a_stride, unsigned w_img, int lane) {
  const int q = (lane & 15) >> 2, p = lane & 3;
  const unsigned aa = a_img + (lane & 1) * a_stride + (lane >> 2) * 8;
  const s16x4 av[2] = {lds_ld<s16x4>(aa), lds_ld<s16x4>(aa + 128)};
  const unsigned ba = w_img + q * 128 + (((2 * (lane >> 4) + (p >> 1)) ^ ((q >> 1) << 2)) << 4) + (p & 1) * 8;
  f32x4 c[4] = {};
  Proj<0>::run(av, ba, c);
  return f32x2{(c[0][0] + c[1][0]) + (c[2][0] + c[3][0]), (c[0][1] + c[1][1]) + (c[2][1] + c[3][1])};
}
__device__ __forceinline__ f32x2 gather_quarters(rsrc_t rws, unsigned base, int quarter_bytes, unsigned tag, int lane) {
  u32x4 g;
  for (unsigned spin = 0; spin < SPIN_CAP; ++spin) {
    asm volatile("" ::: "memory");
    g = ld_dev(rws, (lane >> 4) * quarter_bytes + (lane & 15) * 16, base);
    if (__all((g[1] == tag) & (g[3] == tag))) break;
    __builtin_amdgcn_s_sleep(QSLEEP);
  }
  return f32x2{rows_sum(__uint_as_float(g[0])), rows_sum(__uint_as_float(g[2]))};
}

struct KFrag { bf16x8 k[4]; };
struct VFrag { s16x4 v[4][2]; };
struct State { float m, l; f32x4 o[2]; };

__device__ __forceinline__ void read_k(KFrag& t, const unsigned (&ka)[4], int off) {
#pragma unroll
  for (int s = 0; s < 4; ++s) t.k[s] = lds_ld<bf16x8>(ka[s] + off);
  asm volatile("s_waitcnt lgkmcnt(0)" : "+v"(t.k[0]), "+v"(t.k[1]), "+v"(t.k[2]), "+v"(t.k[3])::"memory");
}
__device__ __forceinline__ void read_v(VFrag& t, const unsigned (&va)[2], int off) {
#pragma unroll
  for (int a = 0; a < 4; ++a)
#pragma unroll
    for (int h = 0; h < 2; ++h) t.v[a][h] = tr_read(va[h] + off + a * 1024);
  asm volatile("s_waitcnt lgkmcnt(0)"
               : "+v"(t.v[0][0]), "+v"(t.v[0][1]), "+v"(t.v[1][0]), "+v"(t.v[1][1]), "+v"(t.v[2][0]), "+v"(t.v[2][1]), "+v"(t.v[3][0]), "+v"(t.v[3][1])
               :
               : "memory");
}
__device__ __forceinline__ s16x4 score_part(const KFrag& t, const bf16x8 (&qf)[4], State& st) {
  f32x4 sc = {0.f, 0.f, 0.f, 0.f};
#pragma unroll
  for (int s = 0; s < 4; ++s) sc = __builtin_amdgcn_mfma_f32_16x16x32_bf16(t.k[s], qf[s], sc, 0, 0, 0);
  float x[4];
#pragma unroll
  for (int r = 0; r < 4; ++r) x[r] = rbf(rbf(sc[r]) * SCALE);
  const float mn = fmaxf(st.m, rows_max(fmaxf(fmaxf(x[0], x[1]), fmaxf(x[2], x[3]))));
  const float alpha = ex2((st.m - mn) * L2E);
  st.m = mn;
  float p[4];
#pragma unroll
  for (int r = 0; r < 4; ++r) p[r] = ex2((x[r] - mn) * L2E);
  st.l = fmaf(st.l, alpha, (p[0] + p[1]) + (p[2] + p[3]));
  const float a0 = dpp<0x00>(alpha), a1 = dpp<0x55>(alpha), a2 = dpp<0xAA>(alpha), a3 = dpp<0xFF>(alpha);
#pragma unroll
  for (int h = 0; h < 2; ++h) { st.o[h][0] *= a0; st.o[h][1] *= a1; st.o[h][2] *= a2; st.o[h][3] *= a3; }
  const u32x2 pp = {pk2(p[0], p[1]), pk2(p[2], p[3])};
  return __builtin_bit_cast(s16x4, pp);
}
__device__ __forceinline__ void value_part(const VFrag& t, s16x4 pa, State& st) {
#pragma unroll
  for (int h = 0; h < 2; ++h) st.o[h] = mfma4<0>(pa, t.v[0][h], st.o[h]);
#pragma unroll
  for (int h = 0; h < 2; ++h) st.o[h] = mfma4<4>(pa, t.v[1][h], st.o[h]);
#pragma unroll
  for (int h = 0; h < 2; ++h) st.o[h] = mfma4<8>(pa, t.v[2][h], st.o[h]);
#pragma unroll
  for (int h = 0; h < 2; ++h) st.o[h] = mfma4<12>(pa, t.v[3][h], st.o[h]);
}

#define STAMP(i) do {} while (0)
#define STAMP_FLUSH(base, n) do {} while (0)

__global__ void __launch_bounds__(NTHR, 1)
fused_decode_kernel(const u16* __restrict__ x, const u16* __restrict__ kc, const u16* __restrict__ vc, const u16* __restrict__ wq,
                    const u16* __restrict__ wo, const u16* __restrict__ w1, const u16* __restrict__ w2, u16* __restrict__ out, char* ws) {
  extern __shared__ __attribute__((aligned(16))) unsigned char lds[];
  const unsigned lds0 = (unsigned)(size_t)(LAS unsigned char*)lds;
  const int lane = threadIdx.x & 63, wave = __builtin_amdgcn_readfirstlane(threadIdx.x >> 6), wg = blockIdx.x;
  const int xg = wg & 7, xi = wg >> 3;
  asm volatile("" ::"s"(x), "s"(kc), "s"(vc), "s"(wq), "s"(wo), "s"(w1), "s"(w2), "s"(out), "s"(ws));
  const rsrc_t rws = make_rsrc(ws, WS_END);
  STAMP(0);

  if (wave < 4) {
    const rsrc_t rk = make_rsrc(kc, KV_BYTES), rv = make_rsrc(vc, KV_BYTES);
    const int g = lane >> 4, c = lane & 15;
    unsigned vk[4];
#pragma unroll
    for (int u = 0; u < 4; ++u) vk[u] = g * 256 + ((c ^ (4 * u + g)) << 4);
    const unsigned vv = g * 256 + ((c ^ (g << 2)) << 4);
    const unsigned ring = lds0 + L_RING + wave * (NSLOT * 8192);
    const unsigned tile0 = (unsigned)wg * (2048 * 256) + wave * 4096;
    unsigned ka[4], va[2];
#pragma unroll
    for (int s = 0; s < 4; ++s) ka[s] = ring + c * 256 + (((4 * s + g) ^ c) << 4);
#pragma unroll
    for (int h = 0; h < 2; ++h) va[h] = ring + 4096 + (c >> 2) * 256 + (((8 * h + 2 * g + ((c & 3) >> 1)) ^ ((c >> 2) << 2)) << 4) + (c & 1) * 8;
    auto issue_k = [&](int t, int slot) { dma_half(rk, vk[0], vk[1], vk[2], vk[3], tile0 + t * 16384, ring + slot * 8192); };
    auto issue_v = [&](int t, int slot) { dma_half(rv, vv, vv, vv, vv, tile0 + t * 16384, ring + slot * 8192 + 4096); };

    wg_barrier();
#pragma unroll
    for (int t = 0; t < NSLOT; ++t) {
      wait_vm<HEAD_WIN>(); issue_k(t, t);
      wait_vm<HEAD_WIN>(); issue_v(t, t);
    }
    unsigned tsv = TSTAMP ? (unsigned)__builtin_amdgcn_s_memrealtime() : 0u;
    KFrag pk[NPRE]; VFrag pv[NPRE];
#pragma unroll
    for (int t = 0; t < NPRE; ++t) {
      wait_vm<HEAD_WIN>(); read_k(pk[t], ka, (t % NSLOT) * 8192); issue_k(t + NSLOT, t % NSLOT);
      wait_vm<HEAD_WIN>();
      if (TSTAMP) { const unsigned tt = (unsigned)__builtin_amdgcn_s_memrealtime(); tsv = lane == t + 2 ? tt : tsv; }
      read_v(pv[t], va, (t % NSLOT) * 8192); issue_v(t + NSLOT, t % NSLOT);
    }
    STAMP(1);
    for (unsigned spin = 0; spin < SPIN_CAP * 16 && *(volatile LAS unsigned*)(size_t)(lds0 + L_QR) == 0; ++spin) __builtin_amdgcn_s_sleep(1);
    asm volatile("" ::: "memory");
    STAMP(2);
    bf16x8 qf[4];
#pragma unroll
    for (int s = 0; s < 4; ++s) qf[s] = lds_ld<bf16x8>(lds0 + L_QI + (c & 3) * 256 + (4 * s + g) * 16);
    const unsigned tag = lds_ld<unsigned>(lds0 + L_TAG);

    State st;
    st.m = M_INIT; st.l = 0.f;
    st.o[0] = f32x4{0.f, 0.f, 0.f, 0.f}; st.o[1] = f32x4{0.f, 0.f, 0.f, 0.f};
    unsigned prev = now32();
    auto pace = [&](int n) {
      if (PACE) {
        const unsigned tl = __builtin_amdgcn_readfirstlane(*(volatile LAS unsigned*)(size_t)(lds0 + L_TL));
        const int d = (int)(tl - prev);
        if (tl != 0 && d > 0) {
          const unsigned due = prev + (unsigned)__builtin_amdgcn_readfirstlane((int)((float)d * __builtin_amdgcn_rcpf((float)n)));
          while ((int)(now32() - due) < 0) __builtin_amdgcn_s_sleep(1);
        }
        prev = now32();
      }
    };
    auto step = [&](int t, int slot, auto win) {
      KFrag kf; VFrag vf;
      wait_vm<win.value>(); read_k(kf, ka, slot * 8192);
      pace(2 * (NSTEP - NSLOT - t)); issue_k(t + NSLOT, slot);
      const s16x4 pa = score_part(kf, qf, st);
      wait_vm<win.value>();
      if (TSTAMP) { const unsigned tt = (unsigned)__builtin_amdgcn_s_memrealtime(); tsv = lane == t + 2 ? tt : tsv; }
      if (PACE) {
        const unsigned tn = now32();
        *(volatile LAS u32x2*)(size_t)(lds0 + L_TP + wave * 8) = u32x2{(unsigned)t, tn};
        if (t == CPA) *(volatile LAS unsigned*)(size_t)(lds0 + L_TS + wave * 4) = tn | 1u;
      }
      read_v(vf, va, slot * 8192);
      pace(2 * (NSTEP - NSLOT - t) - 1); issue_v(t + NSLOT, slot);
      value_part(vf, pa, st);
    };
    using W1 = std::integral_constant<int, WIN>;
    using W2 = std::integral_constant<int, WIN2>;
#pragma unroll
    for (int j = 0; j < 2 * NSLOT; ++j) {
      step(NPRE + j, (NPRE + j) % NSLOT, W1{});
      if (j < NPRE) value_part(pv[j], score_part(pk[j], qf, st), st);
    }
    static_assert(2 * NSLOT >= NPRE && (NSTEP - NPRE) % NSLOT == 0, "tile schedule");
#pragma unroll
    for (int j = 0; j < NSLOT; ++j) step(NPRE + 2 * NSLOT + j, (NPRE + j) % NSLOT, W1{});
    for (int t0 = NPRE + 3 * NSLOT; t0 < NSTEP - NSLOT; t0 += NSLOT) {
#pragma unroll
      for (int j = 0; j < NSLOT; ++j) step(t0 + j, (NPRE + j) % NSLOT, W2{});
    }
    STAMP(3);
#pragma unroll
    for (int j = 0; j < NSLOT; ++j) {
      KFrag kf; VFrag vf;
      if (j == 0) wait_vm<20>(); else if (j == 1) wait_vm<12>(); else wait_vm<4>();
      read_k(kf, ka, ((NPRE + j) % NSLOT) * 8192);
      const s16x4 pa = score_part(kf, qf, st);
      if (j == 0) wait_vm<16>(); else if (j == 1) wait_vm<8>(); else wait_vm<0>();
      if (j == NSLOT - 1) STAMP(4);
      if (TSTAMP && j == NSLOT - 1) { const unsigned tt = (unsigned)__builtin_amdgcn_s_memrealtime(); tsv = lane == 31 ? tt : tsv;
        unsigned xcc; asm volatile("s_getreg_b32 %0, hwreg(HW_REG_XCC_ID, 0, 4)" : "=s"(xcc)); tsv = lane == 32 ? xcc : tsv;
        ((GAS unsigned*)(ws + WS_STAMP + (1 << 20)))[(wg * 4 + wave) * 64 + lane] = tsv; }
      read_v(vf, va, ((NPRE + j) % NSLOT) * 8192);
      value_part(vf, pa, st);
    }

    const float lsum = rows_sum(st.l);
#pragma unroll
    for (int h = 0; h < 2; ++h)
#pragma unroll
      for (int r = 0; r < 4; ++r) lds_st<float>(ring + (r * 128 + h * 64 + lane) * 4, st.o[h][r]);
    if (lane < 4) lds_st<f32x2>(lds0 + L_ML + (wave * 4 + lane) * 8, f32x2{st.m, lsum});
    STAMP(5);
    wg_barrier();
    STAMP(6);
    {
      f32x2 ml[4], ov[4];
#pragma unroll
      for (int w = 0; w < 4; ++w) {
        ml[w] = lds_ld<f32x2>(lds0 + L_ML + (w * 4 + wave) * 8);
        ov[w] = lds_ld<f32x2>(lds0 + L_RING + w * (NSLOT * 8192) + (wave * 128 + 2 * lane) * 4);
      }
      const float M = fmaxf(fmaxf(ml[0][0], ml[1][0]), fmaxf(ml[2][0], ml[3][0]));
      float L = 0.f, o0 = 0.f, o1 = 0.f;
#pragma unroll
      for (int w = 0; w < 4; ++w) {
        const float wt = ex2((ml[w][0] - M) * L2E);
        L = fmaf(wt, ml[w][1], L); o0 = fmaf(wt, ov[w][0], o0); o1 = fmaf(wt, ov[w][1], o1);
      }
      st_dev<ST_END>(rws, u32x4{__float_as_uint(o0), tag, __float_as_uint(o1), tag}, lane * 16, WS_P1 + wg * 4096 + wave * 1024);
      if (lane == 0) st_dev<ST_END>(rws, u32x4{__float_as_uint(M), tag, __float_as_uint(L), tag}, 0, WS_ML + wg * 64 + wave * 16);
    }
    const int ks = xg * 8 + (xi >> 2), cs = xi & 3;
    s16x4 wf[16];
    if (wave < 2) {
      const unsigned wa = lds0 + L_WO + (c >> 2) * 256 + (((8 * wave + 2 * g + ((c & 3) >> 1)) ^ ((c >> 2) << 2)) << 4) + (c & 1) * 8;
#pragma unroll
      for (int a = 0; a < 16; ++a) wf[a] = tr_read(wa + a * 1024);
    }
    STAMP(7);
    {
      const int b = wave >> 1, half = wave & 1, sp = lane >> 2, p = lane & 3;
      const int src0 = (b * 8 + xg) * 16, hl = (ks >> 1) & 3;
      const unsigned vo = sp * 4096 + p * 16, so = WS_P1 + src0 * 4096 + hl * 1024 + ((ks & 1) * 64 + half * 32) * 8;
      const unsigned vm = sp * 64, sm = WS_ML + src0 * 64 + hl * 16;
      u32x4 gv[4], gm;
      for (unsigned spin = 0; spin < SPIN_CAP; ++spin) {
        asm volatile("" ::: "memory");
        gm = ld_dev<LD_END>(rws, vm, sm);
        if (__popcll(__ballot(gm[1] != tag)) <= 4 * SPEC_TH) break;
        __builtin_amdgcn_s_sleep(4);
      }
      for (unsigned spin = 0; spin < SPIN_CAP; ++spin) {
        asm volatile("" ::: "memory");
        gm = ld_dev<LD_END>(rws, vm, sm);
#pragma unroll
        for (int i = 0; i < 4; ++i) gv[i] = ld_dev<LD_END>(rws, vo + i * 64, so);
        bool ok = (gm[1] == tag) & (gm[3] == tag);
#pragma unroll
        for (int i = 0; i < 4; ++i) ok = ok & (gv[i][1] == tag) & (gv[i][3] == tag);
        if (__all(ok)) break;
      }
      STAMP(8);
      const float ms = __uint_as_float(gm[0]), M = quads_max(ms);
      const float wt = ex2((ms - M) * L2E);
      const float inv = __builtin_amdgcn_rcpf(quads_sum(wt * __uint_as_float(gm[2])));
      float e[2];
#pragma unroll
      for (int j = 0; j < 2; ++j) {
        const float u0 = swap32_sum(wt * __uint_as_float(gv[0][2 * j]), wt * __uint_as_float(gv[2][2 * j]));
        const float u1 = swap32_sum(wt * __uint_as_float(gv[1][2 * j]), wt * __uint_as_float(gv[3][2 * j]));
        float v = swap16_sum(u0, u1);
        v += dpp<ROR4>(v); v += dpp<ROR8>(v);
        e[j] = v * inv;
      }
      if ((c >> 2) == 0) lds_st<unsigned>(lds0 + L_OS + b * 128 + (half * 16 + g * 4 + p) * 4, pk2(e[0], e[1]));
    }
    wg_barrier();
    STAMP(9);
    if (wave < 2) {
      const s16x4 oa = lds_ld<s16x4>(lds0 + L_OS + (lane & 1) * 128 + (lane >> 2) * 8);
      f32x4 c0 = {0.f, 0.f, 0.f, 0.f}, c1 = {0.f, 0.f, 0.f, 0.f};
      c0 = mfma4<0>(oa, wf[0], c0);   c1 = mfma4<1>(oa, wf[1], c1);   c0 = mfma4<2>(oa, wf[2], c0);   c1 = mfma4<3>(oa, wf[3], c1);
      c0 = mfma4<4>(oa, wf[4], c0);   c1 = mfma4<5>(oa, wf[5], c1);   c0 = mfma4<6>(oa, wf[6], c0);   c1 = mfma4<7>(oa, wf[7], c1);
      c0 = mfma4<8>(oa, wf[8], c0);   c1 = mfma4<9>(oa, wf[9], c1);   c0 = mfma4<10>(oa, wf[10], c0); c1 = mfma4<11>(oa, wf[11], c1);
      c0 = mfma4<12>(oa, wf[12], c0); c1 = mfma4<13>(oa, wf[13], c1); c0 = mfma4<14>(oa, wf[14], c0); c1 = mfma4<15>(oa, wf[15], c1);
      st_dev<ST_END>(rws, u32x4{__float_as_uint(c0[0] + c1[0]), tag, __float_as_uint(c0[1] + c1[1]), tag}, lane * 16, WS_P2 + ks * 8192 + (cs * 128 + wave * 64) * 16);
    }
    STAMP(10);
    STAMP_FLUSH(wave * 12, 12);
  } else {
    __builtin_amdgcn_s_setprio(HPRIO);
    const unsigned th0 = now32();
    auto sleep_until = [&](unsigned due) { while ((int)(now32() - due) < 0) __builtin_amdgcn_s_sleep(4); };
    if (lane < 16) lds_st<unsigned>(lds0 + L_TAG + lane * 4, 0u);
    const int cg = xg * 32 + xi;
    const rsrc_t rx = make_rsrc(x, 2 * DM * 2), rq = make_rsrc(wq, DM * HQD * 2);
    unsigned ev, xo0, xo1;
    u32x4 x0, x1;
    asm volatile(
        "s_nop 4\n\t"
        "buffer_load_dwordx4 %0, %5, %6, 0 offen\n\t"
        "buffer_load_dwordx4 %1, %5, %6, 0 offen offset:1024\n\t"
        "buffer_load_dword %2, off, %7, 0 sc1\n\t"
        "buffer_load_ushort %3, %8, %6, 0 offen\n\t"
        "buffer_load_ushort %4, %8, %6, 0 offen offset:1024"
        : "=&v"(x0), "=&v"(x1), "=&v"(ev), "=&v"(xo0), "=&v"(xo1)
        : "v"(lane * 16), "s"(rx), "s"(rws), "v"((cg * 2 + (lane & 1)) * 2)
        : "memory");
    const int qk = cg >> 6, qc = cg & 63;
    const bool f1 = xi < 14 && !NO_FFN, f2 = xi >= 14 && xi < 28 && !NO_FFN;
    const int fi = xg * 14 + (xi < 14 ? xi : xi - 14);
    const int hk = fi / 28, hc = fi % 28;
    const int gk = fi >> 3, gc = fi & 7;
    {
      const unsigned sw = ((lane & 7) ^ (((lane >> 4) & 1) << 2)) << 4;
      const unsigned vq = (lane >> 3) * (HQD * 2) + sw;
#pragma unroll
      for (int i = 0; i < 16; ++i) dma_piece(rq, vq, (qk * 128 + 8 * i) * (HQD * 2) + qc * 128, lds0 + L_WQ + i * 1024);
      const rsrc_t rf = make_rsrc(f1 ? w1 : w2, f1 || f2 ? DM * FF * 2 : 0);
      const int ld = f1 ? FF * 2 : DM * 2;
      const unsigned vf = (lane >> 3) * ld + sw, sf = f1 ? hk * 128 * (FF * 2) + hc * 128 : gk * 128 * (DM * 2) + gc * 128;
#pragma unroll
      for (int i = 0; i < 16; ++i) dma_piece(rf, vf, sf + 8 * i * ld, lds0 + L_WF + i * 1024);
      const int ks = xg * 8 + (xi >> 2), cs = xi & 3;
      const rsrc_t ro = make_rsrc(wo, HQD * DM * 2);
      const unsigned vo = (lane >> 4) * (DM * 2) + (((lane & 15) ^ ((lane >> 4) << 2)) << 4);
#pragma unroll
      for (int i = 0; i < 16; ++i) dma_piece(ro, vo, (ks * 64 + 4 * i) * (DM * 2) + cs * 256, lds0 + L_WO + i * 1024);
    }
    wg_barrier();
    asm volatile("s_waitcnt vmcnt(48)" : "+v"(x0), "+v"(x1), "+v"(ev), "+v"(xo0), "+v"(xo1)::"memory");
    STAMP(1);
    const unsigned tag = __builtin_amdgcn_readfirstlane(ev) + 1u;
    {
      float a[2][8];
#pragma unroll
      for (int j = 0; j < 4; ++j) { a[0][2 * j] = bflo(x0[j]); a[0][2 * j + 1] = bfhi(x0[j]); a[1][2 * j] = bflo(x1[j]); a[1][2 * j + 1] = bfhi(x1[j]); }
#pragma unroll
      for (int b = 0; b < 2; ++b) {
        const float mean = wave_sum(((a[b][0] + a[b][1]) + (a[b][2] + a[b][3])) + ((a[b][4] + a[b][5]) + (a[b][6] + a[b][7]))) * (1.f / DM);
        float q = 0.f;
#pragma unroll
        for (int j = 0; j < 8; ++j) q = fmaf(a[b][j] - mean, a[b][j] - mean, q);
        const float var = wave_sum(q) * (1.f / DM);
        const float mb = rbf(mean), inv = __builtin_amdgcn_rcpf(rbf(__builtin_amdgcn_sqrtf(rbf(rbf(var) + 1e-5f))));
        u32x4 xn;
#pragma unroll
        for (int j = 0; j < 4; ++j) xn[j] = pk2(rbf(a[b][2 * j] - mb) * inv, rbf(a[b][2 * j + 1] - mb) * inv);
        lds_st<u32x4>(lds0 + L_XN + b * 1024 + lane * 16, xn);
      }
    }
    if (lane == 0) lds_st<unsigned>(lds0 + L_TAG, tag);
    asm volatile("s_waitcnt vmcnt(32) lgkmcnt(0)" ::: "memory");
    STAMP(2);
    {
      const f32x2 y = proj64(lds0 + L_XN + qk * 256, 1024, lds0 + L_WQ, lane);
      st_dev(rws, u32x4{__float_as_uint(y[0]), tag, __float_as_uint(y[1]), tag}, lane * 16, WS_QP + (qk * HQD + qc * 64) * 16);
    }
    if (f1) {
      wait_vm<17>();
      const f32x2 y = proj64(lds0 + L_XN + hk * 256, 1024, lds0 + L_WF, lane);
      st_dev(rws, u32x4{__float_as_uint(y[0]), tag, __float_as_uint(y[1]), tag}, lane * 16, WS_HP + (hk * FF + hc * 64) * 16);
    }
    if (QA_AT) sleep_until(th0 + QA_AT);
    {
      const f32x2 q = gather_quarters(rws, WS_QP + (qc * 64 + qk * 16) * 16, HQD * 16, tag, lane);
      if (lane < 16) __builtin_amdgcn_raw_buffer_store_b64(u32x2{pk2(q[0], q[1]), tag}, rws, lane * 8, WS_Q + (qc * 64 + qk * 16) * 8, ST_MID);
    }
    if (f1) {
      const f32x2 h = gather_quarters(rws, WS_HP + (hc * 64 + hk * 16) * 16, FF * 16, tag, lane);
      if (lane < 16) __builtin_amdgcn_raw_buffer_store_b64(u32x2{pk2(silu(rbf(h[0])), silu(rbf(h[1]))), tag}, rws, lane * 8, WS_H + (hc * 64 + hk * 16) * 8, ST_MID);
    }
    if (QB_AT) sleep_until(th0 + QB_AT);
    STAMP(3);
    {
      const int bh = wg >> 4, b = bh >> 3, kvh = bh & 7;
      u32x4 gq[4];
      for (unsigned spin = 0; spin < SPIN_CAP; ++spin) {
        asm volatile("" ::: "memory");
#pragma unroll
        for (int i = 0; i < 4; ++i) gq[i] = ld_dev(rws, lane * 16 + i * 1024, WS_Q + kvh * 4096);
        bool ok = true;
#pragma unroll
        for (int i = 0; i < 4; ++i) ok = ok & (gq[i][1] == tag) & (gq[i][3] == tag);
        if (__all(ok)) break;
        __builtin_amdgcn_s_sleep(QSLEEP);
      }
#pragma unroll
      for (int i = 0; i < 4; ++i)
        lds_st<unsigned>(lds0 + L_QI + i * 256 + lane * 4, b ? (gq[i][0] >> 16) | (gq[i][2] & 0xffff0000u) : (gq[i][0] & 0xffffu) | (gq[i][2] << 16));
    }
    wait_lds();
    if (lane == 0) *(volatile LAS unsigned*)(size_t)(lds0 + L_QR) = 1u;
    STAMP(4);

    if (f2) {
      u32x4 gh;
      for (unsigned spin = 0; spin < SPIN_CAP; ++spin) {
        asm volatile("" ::: "memory");
        gh = ld_dev(rws, lane * 16, WS_H + gk * 1024);
        if (__all((gh[1] == tag) & (gh[3] == tag))) break;
        __builtin_amdgcn_s_sleep(32);
      }
      lds_st<unsigned>(lds0 + L_HS + lane * 4, (gh[0] & 0xffffu) | (gh[2] << 16));
      lds_st<unsigned>(lds0 + L_HS + 256 + lane * 4, (gh[0] >> 16) | (gh[2] & 0xffff0000u));
      wait_lds();
      const f32x2 y = proj64(lds0 + L_HS, 256, lds0 + L_WF, lane);
      st_dev(rws, u32x4{__float_as_uint(y[0]), tag, __float_as_uint(y[1]), tag}, lane * 16, WS_FG + (gk * DM + gc * 64) * 16);
    }
    STAMP(5);
    if (PACE) {
      sleep_until(th0 + TS2);
      u32x4 ta;
      for (unsigned spin = 0; spin < SPIN_CAP; ++spin) {
        ta = *(volatile LAS u32x4*)(size_t)(lds0 + L_TS);
        if (__all((ta[0] != 0) & (ta[1] != 0) & (ta[2] != 0) & (ta[3] != 0))) break;
        __builtin_amdgcn_s_sleep(8);
      }
      const u32x4 p0 = *(volatile LAS u32x4*)(size_t)(lds0 + L_TP), p1 = *(volatile LAS u32x4*)(size_t)(lds0 + L_TP + 16);
      {
        const unsigned a = th0 + (((ta[0] - th0) + (ta[1] - th0) + (ta[2] - th0) + (ta[3] - th0)) >> 2);
        const unsigned b = th0 + (((p0[1] - th0) + (p0[3] - th0) + (p1[1] - th0) + (p1[3] - th0)) >> 2);
        const unsigned c4 = p0[0] + p0[2] + p1[0] + p1[2];
        if (lane == 0) st_dev(rws, u32x4{a, tag, (b << 8) | c4, tag}, 0, WS_PC + wg * 128);
      }
      sleep_until(now32() + TSD);
      u32x4 gp;
      for (unsigned spin = 0; spin < SPIN_CAP; ++spin) {
        asm volatile("" ::: "memory");
        gp = ld_dev(rws, (lane & (NSAMP - 1)) * 128, WS_PC + (wg & ~(NSAMP - 1)) * 128);
        if (__all((gp[1] == tag) & (gp[3] == tag))) break;
        __builtin_amdgcn_s_sleep(24);
      }
      const float tb = (float)((int)((gp[2] & 0xffffff00u) - (th0 << 8)) >> 8), ta_ = (float)(int)(gp[0] - th0);
      const float cb = 0.25f * (float)(gp[2] & 0xffu);
      const float r = fmaxf(cb - (float)CPA, 0.25f) * __builtin_amdgcn_rcpf(fmaxf(tb - ta_, 25.f)) * RBOOST;
      const float fin = wave_sum((float)(NSTEP - 3) - cb + r * tb) * __builtin_amdgcn_rcpf(wave_sum(r));
      if (lane == 0) *(volatile LAS unsigned*)(size_t)(lds0 + L_TL) = (th0 + (unsigned)((int)fin - PACE_OFF)) | 1u;
    }
    STAMP(11);
    wg_barrier();
    STAMP(6);
    float g0, g1;
    {
      u32x4 gf = {};
      const int pr = lane >> 1;
      const unsigned vo = (pr < 14 ? pr : 0) * (DM * 16) + (lane & 1) * 16;
      for (unsigned spin = 0; spin < (NO_FFN ? 0 : SPIN_CAP); ++spin) {
        asm volatile("" ::: "memory");
        gf = ld_dev<LD_END>(rws, vo, WS_FG + cg * 32);
        if (__all((gf[1] == tag) & (gf[3] == tag))) break;
      }
      float s0 = pr < 14 ? __uint_as_float(gf[0]) : 0.f, s1 = pr < 14 ? __uint_as_float(gf[2]) : 0.f;
      s0 += dpp<XOR2>(s0); s1 += dpp<XOR2>(s1);
      g0 = rbf(silu(rbf(quads_sum(s0))));
      g1 = rbf(silu(rbf(quads_sum(s1))));
    }
    STAMP(7);
    wg_barrier();
    STAMP(8);
    {
      u32x4 gs[2];
      const unsigned vo = (lane >> 1) * 8192 + (lane & 1) * 16;
      for (unsigned spin = 0; spin < SPIN_CAP; ++spin) {
        asm volatile("" ::: "memory");
#pragma unroll
        for (int i = 0; i < 2; ++i) gs[i] = ld_dev<LD_END>(rws, vo, WS_P2 + i * 32 * 8192 + cg * 32);
        if (__all((gs[0][1] == tag) & (gs[0][3] == tag) & (gs[1][1] == tag) & (gs[1][3] == tag))) break;
      }
      STAMP(9);
      float s0 = __uint_as_float(gs[0][0]) + __uint_as_float(gs[1][0]), s1 = __uint_as_float(gs[0][2]) + __uint_as_float(gs[1][2]);
      s0 += dpp<XOR2>(s0); s1 += dpp<XOR2>(s1);
      s0 = rbf(quads_sum(s0)); s1 = rbf(quads_sum(s1));
      if (lane < 2) {
        out[cg * 2 + lane] = __builtin_bit_cast(u16, (__bf16)(bflo(xo0) + rbf(s0 * g0)));
        out[DM + cg * 2 + lane] = __builtin_bit_cast(u16, (__bf16)(bflo(xo1) + rbf(s1 * g1)));
      }
      if (wg == 0 && lane == 0) __builtin_amdgcn_raw_buffer_store_b32(tag, rws, 0, WS_EPOCH, 16);
    }
    STAMP(10);
    STAMP_FLUSH(48, 12);
  }
}

}

extern "C" void kernel_launch(void* const* d_in, const int* in_sizes, int n_in, void* d_out, int out_size, void* d_ws, size_t ws_size,
                              hipStream_t stream) {
  (void)hipFuncSetAttribute((const void*)fused_decode_kernel, hipFuncAttributeMaxDynamicSharedMemorySize, L_END);
  fused_decode_kernel<<<NWG, NTHR, L_END, stream>>>((const u16*)d_in[0], (const u16*)d_in[1], (const u16*)d_in[2], (const u16*)d_in[3],
                                                    (const u16*)d_in[4], (const u16*)d_in[6], (const u16*)d_in[7], (u16*)d_out, (char*)d_ws);
}
```

```cpp
#include <hip/hip_runtime.h>
#include <hip/hip_bf16.h>
#include <type_traits>

#define STAMPS 0
#define WIN 4
#define HEAD_WIN 4
#define HPRIO 3
#define QSLEEP 8
#define SPEC_TH 3
#define TSTAMP 0
#define PACE 1
#define CPA 7
#define TS2 1800
#define TSD 150
#define RBOOST 1.03f
#define NSAMP 32
#define QA_AT 0
#define QB_AT 0
#define PACE_OFF 150
#define WIN2 12
#define LD_MID 16
#define ST_MID 16
#define LD_END 16
#define ST_END 16
#define P1BF 1
#define MASK2 1
#define PRE2 1
#define PIPE1 0
#define PIPE2 0
#define TOUCH_OUT 0
#define TOUCH_KV 0
#define OUT_AUX 0
#define GMAP 0
#define TS3 0
#define PACE_OFF2 150
#define TSD2 120
#define NO_FFN 0

namespace {

typedef unsigned short u16;
typedef short s16x4 __attribute__((ext_vector_type(4)));
typedef __bf16 bf16x8 __attribute__((ext_vector_type(8)));
typedef __bf16 bf16x2 __attribute__((ext_vector_type(2)));
typedef float f32x2 __attribute__((ext_vector_type(2)));
typedef float f32x4 __attribute__((ext_vector_type(4)));
typedef unsigned u32x2 __attribute__((ext_vector_type(2)));
typedef unsigned u32x4 __attribute__((ext_vector_type(4)));
typedef __amdgpu_buffer_rsrc_t rsrc_t;
#define LAS __attribute__((address_space(3)))
#define GAS __attribute__((address_space(1)))

constexpr int NWG = 256, NTHR = 320;
constexpr int DM = 512, HQD = 4096, SK = 32768, DH = 128, FF = 1792;
constexpr int NSLOT = 3, NSTEP = 32, NPRE = 5;
constexpr int KV_BYTES = 2 * 8 * SK * DH * 2;
constexpr float SCALE = 0.08838834764831845f, L2E = 1.4426950408889634f;
constexpr float M_INIT = -3.0e38f;
constexpr unsigned SPIN_CAP = 1u << 18;
constexpr unsigned OOB = 0x80000000u;

constexpr int WS_EPOCH = 0;
constexpr int WS_Q = 4096;
constexpr int WS_H = WS_Q + HQD * 8;
constexpr int WS_ML = 65536;
constexpr int WS_PC = 98304;
constexpr int WS_PD = 131072;
constexpr int WS_QP = WS_PD + NWG * 128;
constexpr int WS_HP = WS_QP + 4 * HQD * 16;
constexpr int WS_FG = WS_HP + 4 * FF * 16;
constexpr int WS_P2 = WS_FG + 14 * DM * 16;
constexpr int WS_P1 = WS_P2 + 64 * DM * 16;
constexpr int WS_END = WS_P1 + NWG * 4096;
constexpr int WS_STAMP = 8 << 20;
static_assert(WS_H + FF * 8 <= WS_ML && WS_ML + NWG * 128 <= WS_PC && WS_PC + NWG * 128 <= WS_PD, "workspace map");

constexpr int L_RING = 0;
constexpr int L_WQ = 4 * NSLOT * 8192;
constexpr int L_WF = L_WQ + 16384;
constexpr int L_WO = L_WF + 16384;
constexpr int L_XN = L_WO + 16384;
constexpr int L_QI = L_XN + 2048;
constexpr int L_OS = L_QI + 1024;
constexpr int L_HS = L_OS + 256;
constexpr int L_ML = L_HS + 512;
constexpr int L_TAG = L_ML + 128;
constexpr int L_QR = L_TAG + 4;
constexpr int L_TL = L_TAG + 16;
constexpr int L_TS = L_TL + 16;
constexpr int L_TP = L_TS + 16;
constexpr int L_END = L_TP + 32;

__device__ __forceinline__ float rbf(float f) { return (float)(__bf16)f; }
__device__ __forceinline__ unsigned pk2(float lo, float hi) {
  bf16x2 v = {(__bf16)lo, (__bf16)hi};
  return __builtin_bit_cast(unsigned, v);
}
__device__ __forceinline__ float bflo(unsigned u) { return __uint_as_float(u << 16); }
__device__ __forceinline__ float bfhi(unsigned u) { return __uint_as_float(u & 0xffff0000u); }
template <int CTRL> __device__ __forceinline__ float dpp(float x) {
  return __builtin_bit_cast(float, __builtin_amdgcn_mov_dpp(__builtin_bit_cast(int, x), CTRL, 0xf, 0xf, true));
}
constexpr int XOR1 = 0xB1, XOR2 = 0x4E, ROR4 = 0x124, ROR8 = 0x128;
__device__ __forceinline__ float swap16_sum(float a, float b) {
  auto s = __builtin_amdgcn_permlane16_swap(__float_as_uint(a), __float_as_uint(b), false, false);
  return __uint_as_float(s[0]) + __uint_as_float(s[1]);
}
__device__ __forceinline__ float swap32_sum(float a, float b) {
  auto s = __builtin_amdgcn_permlane32_swap(__float_as_uint(a), __float_as_uint(b), false, false);
  return __uint_as_float(s[0]) + __uint_as_float(s[1]);
}
__device__ __forceinline__ float rows_sum(float x) { x = swap16_sum(x, x); return swap32_sum(x, x); }
__device__ __forceinline__ float rows_max(float x) {
  auto s = __builtin_amdgcn_permlane16_swap(__float_as_uint(x), __float_as_uint(x), false, false);
  x = fmaxf(__uint_as_float(s[0]), __uint_as_float(s[1]));
  auto t = __builtin_amdgcn_permlane32_swap(__float_as_uint(x), __float_as_uint(x), false, false);
  return fmaxf(__uint_as_float(t[0]), __uint_as_float(t[1]));
}
__device__ __forceinline__ float quads_sum(float x) { x += dpp<ROR4>(x); x += dpp<ROR8>(x); return rows_sum(x); }
__device__ __forceinline__ float quads_max(float x) { x = fmaxf(x, dpp<ROR4>(x)); x = fmaxf(x, dpp<ROR8>(x)); return rows_max(x); }
__device__ __forceinline__ float wave_sum(float x) { x += dpp<XOR1>(x); x += dpp<XOR2>(x); return quads_sum(x); }
__device__ __forceinline__ float ex2(float x) { return __builtin_amdgcn_exp2f(x); }
__device__ __forceinline__ float silu(float v) { return v * __builtin_amdgcn_rcpf(1.f + ex2(-v * L2E)); }

template <int N> __device__ __forceinline__ void wait_vm() {
  __builtin_amdgcn_sched_barrier(0);
  asm volatile("s_waitcnt vmcnt(%0)" ::"i"(N) : "memory");
}
__device__ __forceinline__ void wait_lds() { asm volatile("s_waitcnt lgkmcnt(0)" ::: "memory"); }
__device__ __forceinline__ void wg_barrier() {
  asm volatile("s_waitcnt lgkmcnt(0)\n\ts_barrier" ::: "memory");
}
__device__ __forceinline__ unsigned now32() { return (unsigned)__builtin_amdgcn_s_memrealtime(); }
__device__ __forceinline__ rsrc_t make_rsrc(const void* p, int bytes) {
  return __builtin_amdgcn_make_buffer_rsrc((void*)p, (short)0, bytes, 0x00020000);
}
template <int AUX = LD_MID> __device__ __forceinline__ u32x4 ld_dev(rsrc_t r, unsigned voff, unsigned soff) { return __builtin_amdgcn_raw_buffer_load_b128(r, voff, soff, AUX); }
template <int AUX = ST_MID> __device__ __forceinline__ void st_dev(rsrc_t r, u32x4 v, unsigned voff, unsigned soff) { __builtin_amdgcn_raw_buffer_store_b128(v, r, voff, soff, AUX); }
__device__ __forceinline__ s16x4 tr_read(unsigned addr) { return __builtin_amdgcn_ds_read_tr16_b64_v4i16((LAS s16x4*)(size_t)addr); }
template <typename T> __device__ __forceinline__ T lds_ld(unsigned addr) { return *(const LAS T*)(size_t)addr; }
template <typename T> __device__ __forceinline__ void lds_st(unsigned addr, T v) { *(LAS T*)(size_t)addr = v; }

__device__ __forceinline__ void dma_piece(rsrc_t r, unsigned voff, unsigned soff, unsigned dst) {
  asm volatile("s_mov_b32 m0, %0\n\ts_nop 0\n\tbuffer_load_dwordx4 %1, %2, %3 offen nt lds" ::"s"(dst), "v"(voff), "s"(r), "s"(soff) : "memory");
}
__device__ __forceinline__ void dma_half(rsrc_t r, unsigned v0, unsigned v1, unsigned v2, unsigned v3, unsigned soff, unsigned dst) {
  asm volatile(
      "s_mov_b32 m0, %0\n\ts_nop 0\n\t"
      "buffer_load_dwordx4 %1, %5, %6 offen nt lds\n\t"
      "buffer_load_dwordx4 %2, %5, %6 offen offset:1024 nt lds\n\t"
      "buffer_load_dwordx4 %3, %5, %6 offen offset:2048 nt lds\n\t"
      "buffer_load_dwordx4 %4, %5, %6 offen offset:3072 nt lds"
      ::"s"(dst), "v"(v0), "v"(v1), "v"(v2), "v"(v3), "s"(r), "s"(soff)
      : "memory");
}
template <int A> __device__ __forceinline__ f32x4 mfma4(s16x4 a, s16x4 b, f32x4 c) { return __builtin_amdgcn_mfma_f32_4x4x4bf16_1k(a, b, c, 4, A, 0); }
template <int A> struct Proj {
  static __device__ __forceinline__ void run(const s16x4 (&av)[2], unsigned ba, f32x4 (&c)[4]) {
    c[A & 3] = mfma4<A & 15>(av[A >> 4], tr_read(ba + A * 512), c[A & 3]);
    if constexpr (A + 1 < 32) Proj<A + 1>::run(av, ba, c);
  }
};
__device__ __forceinline__ f32x2 proj64(unsigned a_img, int a_stride, unsigned w_img, int lane) {
  const int q = (lane & 15) >> 2, p = lane & 3;
  const unsigned aa = a_img + (lane & 1) * a_stride + (lane >> 2) * 8;
  const s16x4 av[2] = {lds_ld<s16x4>(aa), lds_ld<s16x4>(aa + 128)};
  const unsigned ba = w_img + q * 128 + (((2 * (lane >> 4) + (p >> 1)) ^ ((q >> 1) << 2)) << 4) + (p & 1) * 8;
  f32x4 c[4] = {};
  Proj<0>::run(av, ba, c);
  return f32x2{(c[0][0] + c[1][0]) + (c[2][0] + c[3][0]), (c[0][1] + c[1][1]) + (c[2][1] + c[3][1])};
}
__device__ __forceinline__ f32x2 gather_quarters(rsrc_t rws, unsigned base, int quarter_bytes, unsigned tag, int lane) {
  u32x4 g;
  for (unsigned spin = 0; spin < SPIN_CAP; ++spin) {
    asm volatile("" ::: "memory");
    g = ld_dev(rws, (lane >> 4) * quarter_bytes + (lane & 15) * 16, base);
    if (__all((g[1] == tag) & (g[3] == tag))) break;
    __builtin_amdgcn_s_sleep(QSLEEP);
  }
  return f32x2{rows_sum(__uint_as_float(g[0])), rows_sum(__uint_as_float(g[2]))};
}

#define POLL_REQ(B, I) "buffer_load_dwordx4 v[" B "+4*" #I ":" B "+4*" #I "+3], %[a" #I "], %[r], %[s" #I "] offen sc1\n\t"
#define POLL_KEEP(B, I)                                          \
  "v_xor_b32 %[t], %[tag], v[" B "+4*" #I "+1]\n\t"              \
  "v_xor_b32 %[u], %[tag], v[" B "+4*" #I "+3]\n\t"              \
  "v_or_b32 %[t], %[t], %[u]\n\t"                                \
  "v_cmp_eq_u32 vcc, 0, %[t]\n\t"                                \
  "s_nop 1\n\t"                                                  \
  "v_cndmask_b32 %[x" #I "], %[x" #I "], v[" B "+4*" #I "], vcc\n\t"   \
  "v_cndmask_b32 %[y" #I "], %[y" #I "], v[" B "+4*" #I "+2], vcc\n\t" \
  "v_cndmask_b32 %[a" #I "], %[a" #I "], %[oob], vcc\n\t"
#define POLL_CLOB2 "v200", "v201", "v202", "v203", "v204", "v205", "v206", "v207", "v208", "v209", "v210", "v211", "v212", "v213", "v214", "v215"
#define POLL_CLOB3 POLL_CLOB2, "v216", "v217", "v218", "v219", "v220", "v221", "v222", "v223"
__device__ __forceinline__ void poll2(rsrc_t r, unsigned tag, unsigned (&x)[2], unsigned (&y)[2], unsigned (&a)[2], unsigned s0, unsigned s1) {
  unsigned t, u, cnt = SPIN_CAP;
  asm volatile(
      POLL_REQ("200", 0) POLL_REQ("200", 1)
      "1:\n\t"
      POLL_REQ("208", 0) POLL_REQ("208", 1)
      "s_waitcnt vmcnt(2)\n\t"
      POLL_KEEP("200", 0) POLL_KEEP("200", 1)
      "v_and_b32 %[t], %[a0], %[a1]\n\tv_cmp_le_i32 vcc, 0, %[t]\n\ts_and_b64 vcc, exec, vcc\n\ts_cbranch_vccz 2f\n\t"
      POLL_REQ("200", 0) POLL_REQ("200", 1)
      "s_waitcnt vmcnt(2)\n\t"
      POLL_KEEP("208", 0) POLL_KEEP("208", 1)
      "v_and_b32 %[t], %[a0], %[a1]\n\tv_cmp_le_i32 vcc, 0, %[t]\n\ts_and_b64 vcc, exec, vcc\n\ts_cbranch_vccz 2f\n\t"
      "s_sub_u32 %[c], %[c], 1\n\ts_cbranch_scc0 1b\n\t"
      "2:\n\t"
      "s_waitcnt vmcnt(0)"
      : [x0] "+v"(x[0]), [x1] "+v"(x[1]), [y0] "+v"(y[0]), [y1] "+v"(y[1]), [a0] "+v"(a[0]), [a1] "+v"(a[1]), [t] "=&v"(t), [u] "=&v"(u), [c] "+s"(cnt)
      : [r] "s"(r), [s0] "s"(s0), [s1] "s"(s1), [tag] "v"(tag), [oob] "v"(OOB)
      : "memory", "vcc", "scc", POLL_CLOB2);
}
__device__ __forceinline__ void poll3(rsrc_t r, unsigned tag, unsigned (&x)[3], unsigned (&y)[3], unsigned (&a)[3], unsigned s0, unsigned s1, unsigned s2) {
  unsigned t, u, cnt = SPIN_CAP;
  asm volatile(
      POLL_REQ("200", 0) POLL_REQ("200", 1) POLL_REQ("200", 2)
      "1:\n\t"
      POLL_REQ("212", 0) POLL_REQ("212", 1) POLL_REQ("212", 2)
      "s_waitcnt vmcnt(3)\n\t"
      POLL_KEEP("200", 0) POLL_KEEP("200", 1) POLL_KEEP("200", 2)
      "v_and_b32 %[t], %[a0], %[a1]\n\tv_and_b32 %[t], %[t], %[a2]\n\tv_cmp_le_i32 vcc, 0, %[t]\n\ts_and_b64 vcc, exec, vcc\n\ts_cbranch_vccz 2f\n\t"
      POLL_REQ("200", 0) POLL_REQ("200", 1) POLL_REQ("200", 2)
      "s_waitcnt vmcnt(3)\n\t"
      POLL_KEEP("212", 0) POLL_KEEP("212", 1) POLL_KEEP("212", 2)
      "v_and_b32 %[t], %[a0], %[a1]\n\tv_and_b32 %[t], %[t], %[a2]\n\tv_cmp_le_i32 vcc, 0, %[t]\n\ts_and_b64 vcc, exec, vcc\n\ts_cbranch_vccz 2f\n\t"
      "s_sub_u32 %[c], %[c], 1\n\ts_cbranch_scc0 1b\n\t"
      "2:\n\t"
      "s_waitcnt vmcnt(0)"
      : [x0] "+v"(x[0]), [x1] "+v"(x[1]), [x2] "+v"(x[2]), [y0] "+v"(y[0]), [y1] "+v"(y[1]), [y2] "+v"(y[2]), [a0] "+v"(a[0]), [a1] "+v"(a[1]),
        [a2] "+v"(a[2]), [t] "=&v"(t), [u] "=&v"(u), [c] "+s"(cnt)
      : [r] "s"(r), [s0] "s"(s0), [s1] "s"(s1), [s2] "s"(s2), [tag] "v"(tag), [oob] "v"(OOB)
      : "memory", "vcc", "scc", POLL_CLOB3);
}

struct KFrag { bf16x8 k[4]; };
struct VFrag { s16x4 v[4][2]; };
struct State { float m, l; f32x4 o[2]; };

__device__ __forceinline__ void read_k(KFrag& t, const unsigned (&ka)[4], int off) {
#pragma unroll
  for (int s = 0; s < 4; ++s) t.k[s] = lds_ld<bf16x8>(ka[s] + off);
  asm volatile("s_waitcnt lgkmcnt(0)" : "+v"(t.k[0]), "+v"(t.k[1]), "+v"(t.k[2]), "+v"(t.k[3])::"memory");
}
__device__ __forceinline__ void read_v(VFrag& t, const unsigned (&va)[2], int off) {
#pragma unroll
  for (int a = 0; a < 4; ++a)
#pragma unroll
    for (int h = 0; h < 2; ++h) t.v[a][h] = tr_read(va[h] + off + a * 1024);
  asm volatile("s_waitcnt lgkmcnt(0)"
               : "+v"(t.v[0][0]), "+v"(t.v[0][1]), "+v"(t.v[1][0]), "+v"(t.v[1][1]), "+v"(t.v[2][0]), "+v"(t.v[2][1]), "+v"(t.v[3][0]), "+v"(t.v[3][1])
               :
               : "memory");
}
__device__ __forceinline__ s16x4 score_part(const KFrag& t, const bf16x8 (&qf)[4], State& st) {
  f32x4 sc = {0.f, 0.f, 0.f, 0.f};
#pragma unroll
  for (int s = 0; s < 4; ++s) sc = __builtin_amdgcn_mfma_f32_16x16x32_bf16(t.k[s], qf[s], sc, 0, 0, 0);
  float x[4];
#pragma unroll
  for (int r = 0; r < 4; ++r) x[r] = rbf(rbf(sc[r]) * SCALE);
  const float mn = fmaxf(st.m, rows_max(fmaxf(fmaxf(x[0], x[1]), fmaxf(x[2], x[3]))));
  const float alpha = ex2((st.m - mn) * L2E);
  st.m = mn;
  float p[4];
#pragma unroll
  for (int r = 0; r < 4; ++r) p[r] = ex2((x[r] - mn) * L2E);
  st.l = fmaf(st.l, alpha, (p[0] + p[1]) + (p[2] + p[3]));
  const float a0 = dpp<0x00>(alpha), a1 = dpp<0x55>(alpha), a2 = dpp<0xAA>(alpha), a3 = dpp<0xFF>(alpha);
#pragma unroll
  for (int h = 0; h < 2; ++h) { st.o[h][0] *= a0; st.o[h][1] *= a1; st.o[h][2] *= a2; st.o[h][3] *= a3; }
  const u32x2 pp = {pk2(p[0], p[1]), pk2(p[2], p[3])};
  return __builtin_bit_cast(s16x4, pp);
}
__device__ __forceinline__ void value_part(const VFrag& t, s16x4 pa, State& st) {
#pragma unroll
  for (int h = 0; h < 2; ++h) st.o[h] = mfma4<0>(pa, t.v[0][h], st.o[h]);
#pragma unroll
  for (int h = 0; h < 2; ++h) st.o[h] = mfma4<4>(pa, t.v[1][h], st.o[h]);
#pragma unroll
  for (int h = 0; h < 2; ++h) st.o[h] = mfma4<8>(pa, t.v[2][h], st.o[h]);
#pragma unroll
  for (int h = 0; h < 2; ++h) st.o[h] = mfma4<12>(pa, t.v[3][h], st.o[h]);
}

#define STAMP(i) do {} while (0)
#define STAMP_FLUSH(base, n) do {} while (0)

__global__ void __launch_bounds__(NTHR, 1)
fused_decode_kernel(const u16* __restrict__ x, const u16* __restrict__ kc, const u16* __restrict__ vc, const u16* __restrict__ wq,
                    const u16* __restrict__ wo, const u16* __restrict__ w1, const u16* __restrict__ w2, u16* __restrict__ out, char* ws) {
  extern __shared__ __attribute__((aligned(16))) unsigned char lds[];
  const unsigned lds0 = (unsigned)(size_t)(LAS unsigned char*)lds;
  const int lane = threadIdx.x & 63, wave = __builtin_amdgcn_readfirstlane(threadIdx.x >> 6), wg = blockIdx.x;
  const int xg = wg & 7, xi = wg >> 3;
  const int aid = GMAP ? (((xi >> 4) * 8 + xg) << 4) | (xi & 15) : wg;
  asm volatile("" ::"s"(x), "s"(kc), "s"(vc), "s"(wq), "s"(wo), "s"(w1), "s"(w2), "s"(out), "s"(ws));
  const rsrc_t rws = make_rsrc(ws, WS_END);
  STAMP(0);

  if (wave < 4) {
    const rsrc_t rk = make_rsrc(kc, KV_BYTES), rv = make_rsrc(vc, KV_BYTES);
    const int g = lane >> 4, c = lane & 15;
    unsigned vk[4];
#pragma unroll
    for (int u = 0; u < 4; ++u) vk[u] = g * 256 + ((c ^ (4 * u + g)) << 4);
    const unsigned vv = g * 256 + ((c ^ (g << 2)) << 4);
    const unsigned ring = lds0 + L_RING + wave * (NSLOT * 8192);
    const unsigned tile0 = (unsigned)aid * (2048 * 256) + wave * 4096;
    unsigned ka[4], va[2];
#pragma unroll
    for (int s = 0; s < 4; ++s) ka[s] = ring + c * 256 + (((4 * s + g) ^ c) << 4);
#pragma unroll
    for (int h = 0; h < 2; ++h) va[h] = ring + 4096 + (c >> 2) * 256 + (((8 * h + 2 * g + ((c & 3) >> 1)) ^ ((c >> 2) << 2)) << 4) + (c & 1) * 8;
    auto issue_k = [&](int t, int slot) { dma_half(rk, vk[0], vk[1], vk[2], vk[3], tile0 + t * 16384, ring + slot * 8192); };
    auto issue_v = [&](int t, int slot) { dma_half(rv, vv, vv, vv, vv, tile0 + t * 16384, ring + slot * 8192 + 4096); };

    unsigned touch0 = 0, touch1 = 0;
    if (TOUCH_KV) asm volatile("buffer_load_dword %0, off, %2, %4 nt\n\tbuffer_load_dword %1, off, %3, %4 nt" : "=&v"(touch0), "=&v"(touch1) : "s"(rk), "s"(rv), "s"(tile0) : "memory");
    wg_barrier();
#pragma unroll
    for (int t = 0; t < NSLOT; ++t) {
      wait_vm<HEAD_WIN>(); issue_k(t, t);
      wait_vm<HEAD_WIN>(); issue_v(t, t);
    }
    asm volatile("" : "+v"(touch0), "+v"(touch1));
    unsigned tsv = TSTAMP ? (unsigned)__builtin_amdgcn_s_memrealtime() : 0u;
    KFrag pk[NPRE]; VFrag pv[NPRE];
#pragma unroll
    for (int t = 0; t < NPRE; ++t) {
      wait_vm<HEAD_WIN>(); read_k(pk[t], ka, (t % NSLOT) * 8192); issue_k(t + NSLOT, t % NSLOT);
      wait_vm<HEAD_WIN>();
      if (TSTAMP) { const unsigned tt = (unsigned)__builtin_amdgcn_s_memrealtime(); tsv = lane == t + 2 ? tt : tsv; }
      read_v(pv[t], va, (t % NSLOT) * 8192); issue_v(t + NSLOT, t % NSLOT);
    }
    STAMP(1);
    for (unsigned spin = 0; spin < SPIN_CAP * 16 && *(volatile LAS unsigned*)(size_t)(lds0 + L_QR) == 0; ++spin) __builtin_amdgcn_s_sleep(1);
    asm volatile("" ::: "memory");
    STAMP(2);
    bf16x8 qf[4];
#pragma unroll
    for (int s = 0; s < 4; ++s) qf[s] = lds_ld<bf16x8>(lds0 + L_QI + (c & 3) * 256 + (4 * s + g) * 16);
    const unsigned tag = lds_ld<unsigned>(lds0 + L_TAG);

    State st;
    st.m = M_INIT; st.l = 0.f;
    st.o[0] = f32x4{0.f, 0.f, 0.f, 0.f}; st.o[1] = f32x4{0.f, 0.f, 0.f, 0.f};
    unsigned prev = now32();
    auto pace = [&](int n) {
      if (PACE) {
        const unsigned tl = __builtin_amdgcn_readfirstlane(*(volatile LAS unsigned*)(size_t)(lds0 + L_TL));
        const int d = (int)(tl - prev);
        if (tl != 0 && d > 0) {
          const unsigned due = prev + (unsigned)__builtin_amdgcn_readfirstlane((int)((float)d * __builtin_amdgcn_rcpf((float)n)));
          while ((int)(now32() - due) < 0) __builtin_amdgcn_s_sleep(1);
        }
        prev = now32();
      }
    };
    auto step = [&](int t, int slot, auto win) {
      KFrag kf; VFrag vf;
      wait_vm<win.value>(); read_k(kf, ka, slot * 8192);
      pace(2 * (NSTEP - NSLOT - t)); issue_k(t + NSLOT, slot);
      const s16x4 pa = score_part(kf, qf, st);
      wait_vm<win.value>();
      if (TSTAMP) { const unsigned tt = (unsigned)__builtin_amdgcn_s_memrealtime(); tsv = lane == t + 2 ? tt : tsv; }
      if (PACE) {
        const unsigned tn = now32();
        *(volatile LAS u32x2*)(size_t)(lds0 + L_TP + wave * 8) = u32x2{(unsigned)t, tn};
        if (t == CPA) *(volatile LAS unsigned*)(size_t)(lds0 + L_TS + wave * 4) = tn | 1u;
      }
      read_v(vf, va, slot * 8192);
      pace(2 * (NSTEP - NSLOT - t) - 1); issue_v(t + NSLOT, slot);
      value_part(vf, pa, st);
    };
    using W1 = std::integral_constant<int, WIN>;
    using W2 = std::integral_constant<int, WIN2>;
#pragma unroll
    for (int j = 0; j < 2 * NSLOT; ++j) {
      step(NPRE + j, (NPRE + j) % NSLOT, W1{});
      if (j < NPRE) value_part(pv[j], score_part(pk[j], qf, st), st);
    }
    static_assert(2 * NSLOT >= NPRE && (NSTEP - NPRE) % NSLOT == 0, "tile schedule");
#pragma unroll
    for (int j = 0; j < NSLOT; ++j) step(NPRE + 2 * NSLOT + j, (NPRE + j) % NSLOT, W1{});
    for (int t0 = NPRE + 3 * NSLOT; t0 < NSTEP - NSLOT; t0 += NSLOT) {
#pragma unroll
      for (int j = 0; j < NSLOT; ++j) step(t0 + j, (NPRE + j) % NSLOT, W2{});
    }
    STAMP(3);
#pragma unroll
    for (int j = 0; j < NSLOT; ++j) {
      KFrag kf; VFrag vf;
      if (j == 0) wait_vm<20>(); else if (j == 1) wait_vm<12>(); else wait_vm<4>();
      read_k(kf, ka, ((NPRE + j) % NSLOT) * 8192);
      const s16x4 pa = score_part(kf, qf, st);
      if (j == 0) wait_vm<16>(); else if (j == 1) wait_vm<8>(); else wait_vm<0>();
      if (j == NSLOT - 1) STAMP(4);
      if (TSTAMP && j == NSLOT - 1) { const unsigned tt = (unsigned)__builtin_amdgcn_s_memrealtime(); tsv = lane == 31 ? tt : tsv;
        unsigned xcc; asm volatile("s_getreg_b32 %0, hwreg(HW_REG_XCC_ID, 0, 4)" : "=s"(xcc)); tsv = lane == 32 ? xcc : tsv;
        ((GAS unsigned*)(ws + WS_STAMP + (1 << 20)))[(wg * 4 + wave) * 64 + lane] = tsv; }
      read_v(vf, va, ((NPRE + j) % NSLOT) * 8192);
      value_part(vf, pa, st);
    }

    const float lsum = rows_sum(st.l);
#pragma unroll
    for (int h = 0; h < 2; ++h)
#pragma unroll
      for (int r = 0; r < 4; ++r) lds_st<float>(ring + (r * 128 + h * 64 + lane) * 4, st.o[h][r]);
    if (lane < 4) lds_st<f32x2>(lds0 + L_ML + (wave * 4 + lane) * 8, f32x2{st.m, lsum});
    STAMP(5);
    wg_barrier();
    STAMP(6);
    {
      f32x2 ml[4], ov[4];
#pragma unroll
      for (int w = 0; w < 4; ++w) {
        ml[w] = lds_ld<f32x2>(lds0 + L_ML + (w * 4 + wave) * 8);
        ov[w] = lds_ld<f32x2>(lds0 + L_RING + w * (NSLOT * 8192) + (wave * 128 + 2 * lane) * 4);
      }
      const float M = fmaxf(fmaxf(ml[0][0], ml[1][0]), fmaxf(ml[2][0], ml[3][0]));
      float o0 = 0.f, o1 = 0.f;
#pragma unroll
      for (int w = 0; w < 4; ++w) {
        const float wt = ex2((ml[w][0] - M) * L2E);
        o0 = fmaf(wt, ov[w][0], o0); o1 = fmaf(wt, ov[w][1], o1);
      }
      if (P1BF) __builtin_amdgcn_raw_buffer_store_b64(u32x2{pk2(o0, o1), tag}, rws, lane * 8, WS_P1 + aid * 2048 + wave * 512, ST_END);
      else st_dev<ST_END>(rws, u32x4{__float_as_uint(o0), tag, __float_as_uint(o1), tag}, lane * 16, WS_P1 + aid * 4096 + wave * 1024);
      if (wave == 3) {
        f32x2 hm[4];
#pragma unroll
        for (int w = 0; w < 4; ++w) hm[w] = lds_ld<f32x2>(lds0 + L_ML + (w * 4 + (lane & 3)) * 8);
        const float Mh = fmaxf(fmaxf(hm[0][0], hm[1][0]), fmaxf(hm[2][0], hm[3][0]));
        float Lh = 0.f;
#pragma unroll
        for (int w = 0; w < 4; ++w) Lh = fmaf(ex2((hm[w][0] - Mh) * L2E), hm[w][1], Lh);
        if (lane < 4) st_dev<ST_END>(rws, u32x4{__float_as_uint(Mh), tag, __float_as_uint(Lh), tag}, lane * 16, WS_ML + aid * 128);
      }
    }
    const int ks = xg * 8 + (xi >> 2), cs = xi & 3;
    s16x4 wf[16];
    if (wave < 2) {
      const unsigned wa = lds0 + L_WO + (c >> 2) * 256 + (((8 * wave + 2 * g + ((c & 3) >> 1)) ^ ((c >> 2) << 2)) << 4) + (c & 1) * 8;
#pragma unroll
      for (int a = 0; a < 16; ++a) wf[a] = tr_read(wa + a * 1024);
    }
    STAMP(7);
    {
      const int b = wave >> 1, half = wave & 1, sp = lane >> 2, p = lane & 3;
      const int src0 = (b * 8 + xg) * 16, hl = (ks >> 1) & 3;
      constexpr int ND = P1BF ? 2 : 4, REC = P1BF ? 2048 : 4096;
      const unsigned vo = sp * REC + p * 16, so = WS_P1 + src0 * REC + hl * (REC / 4) + ((ks & 1) * 2 + half) * (REC / 16);
      const unsigned vm = sp * 128, sm = WS_ML + src0 * 128 + hl * 16;
      u32x4 gv[ND] = {}, gm = {};
      bool hm = false, hd = false, spec = false;
      for (unsigned spin = 0; spin < SPIN_CAP; ++spin) {
        asm volatile("" ::: "memory");
        const bool ldd = !hd & (hm | spec);
        if (!hm) gm = ld_dev<LD_END>(rws, vm, sm);
        if (ldd) {
#pragma unroll
          for (int i = 0; i < ND; ++i) gv[i] = ld_dev<LD_END>(rws, vo + i * 64, so);
        }
        hm = (gm[1] == tag) & (gm[3] == tag);
        hd = true;
#pragma unroll
        for (int i = 0; i < ND; ++i) hd = hd & (gv[i][1] == tag) & (gv[i][3] == tag);
        if (__all(hm & hd)) break;
        spec = __popcll(__ballot(!hm)) <= 4 * SPEC_TH;
        if (PIPE1 && spec) break;
        if (!spec) __builtin_amdgcn_s_sleep(4);
      }
      if (PIPE1 && P1BF) {
        unsigned px[3] = {gm[0], gv[0][0], gv[1][0]}, py[3] = {gm[2], gv[0][2], gv[1][2]};
        unsigned pa[3] = {hm ? OOB : vm, hd ? OOB : vo, hd ? OOB : vo + 64};
        poll3(rws, tag, px, py, pa, sm, so, so);
        gm[0] = px[0]; gm[2] = py[0]; gv[0][0] = px[1]; gv[0][2] = py[1]; gv[1][0] = px[2]; gv[1][2] = py[2];
      }
      STAMP(8);
      const float ms = __uint_as_float(gm[0]), M = quads_max(ms);
      const float wt = ex2((ms - M) * L2E);
      const float inv = __builtin_amdgcn_rcpf(quads_sum(wt * __uint_as_float(gm[2])));
      float e[2];
      if constexpr (P1BF) {
        const float u0 = swap32_sum(wt * bflo(gv[0][0]), wt * bflo(gv[1][0])), u1 = swap32_sum(wt * bfhi(gv[0][0]), wt * bfhi(gv[1][0]));
        const float u2 = swap32_sum(wt * bflo(gv[0][2]), wt * bflo(gv[1][2])), u3 = swap32_sum(wt * bfhi(gv[0][2]), wt * bfhi(gv[1][2]));
        e[0] = swap16_sum(u0, u2); e[1] = swap16_sum(u1, u3);
      } else {
#pragma unroll
        for (int j = 0; j < 2; ++j) {
          const float u0 = swap32_sum(wt * __uint_as_float(gv[0][2 * j]), wt * __uint_as_float(gv[ND - 2][2 * j]));
          const float u1 = swap32_sum(wt * __uint_as_float(gv[1][2 * j]), wt * __uint_as_float(gv[ND - 1][2 * j]));
          e[j] = swap16_sum(u0, u1);
        }
      }
#pragma unroll
      for (int j = 0; j < 2; ++j) { e[j] += dpp<ROR4>(e[j]); e[j] += dpp<ROR8>(e[j]); e[j] *= inv; }
      const int od = P1BF ? 8 * (g >> 1) + 2 * p + (g & 1) : g * 4 + p;
      if ((c >> 2) == 0) lds_st<unsigned>(lds0 + L_OS + b * 128 + (half * 16 + od) * 4, pk2(e[0], e[1]));
    }
    wg_barrier();
    STAMP(9);
    if (wave < 2) {
      const s16x4 oa = lds_ld<s16x4>(lds0 + L_OS + (lane & 1) * 128 + (lane >> 2) * 8);
      f32x4 c0 = {0.f, 0.f, 0.f, 0.f}, c1 = {0.f, 0.f, 0.f, 0.f};
      c0 = mfma4<0>(oa, wf[0], c0);   c1 = mfma4<1>(oa, wf[1], c1);   c0 = mfma4<2>(oa, wf[2], c0);   c1 = mfma4<3>(oa, wf[3], c1);
      c0 = mfma4<4>(oa, wf[4], c0);   c1 = mfma4<5>(oa, wf[5], c1);   c0 = mfma4<6>(oa, wf[6], c0);   c1 = mfma4<7>(oa, wf[7], c1);
      c0 = mfma4<8>(oa, wf[8], c0);   c1 = mfma4<9>(oa, wf[9], c1);   c0 = mfma4<10>(oa, wf[10], c0); c1 = mfma4<11>(oa, wf[11], c1);
      c0 = mfma4<12>(oa, wf[12], c0); c1 = mfma4<13>(oa, wf[13], c1); c0 = mfma4<14>(oa, wf[14], c0); c1 = mfma4<15>(oa, wf[15], c1);
      st_dev<ST_END>(rws, u32x4{__float_as_uint(c0[0] + c1[0]), tag, __float_as_uint(c0[1] + c1[1]), tag}, lane * 16, WS_P2 + ks * 8192 + (cs * 128 + wave * 64) * 16);
    }
    STAMP(10);
    STAMP_FLUSH(wave * 12, 12);
  } else {
    __builtin_amdgcn_s_setprio(HPRIO);
    const unsigned th0 = now32();
    auto sleep_until = [&](unsigned due) { while ((int)(now32() - due) < 0) __builtin_amdgcn_s_sleep(4); };
    if (lane < 16) lds_st<unsigned>(lds0 + L_TAG + lane * 4, 0u);
    const int cg = xg * 32 + xi;
    const rsrc_t rx = make_rsrc(x, 2 * DM * 2), rq = make_rsrc(wq, DM * HQD * 2);
    unsigned ev, xo0, xo1;
    u32x4 x0, x1;
    asm volatile(
        "s_nop 4\n\t"
        "buffer_load_dwordx4 %0, %5, %6, 0 offen\n\t"
        "buffer_load_dwordx4 %1, %5, %6, 0 offen offset:1024\n\t"
        "buffer_load_dword %2, off, %7, 0 sc1\n\t"
        "buffer_load_ushort %3, %8, %6, 0 offen\n\t"
        "buffer_load_ushort %4, %8, %6, 0 offen offset:1024"
        : "=&v"(x0), "=&v"(x1), "=&v"(ev), "=&v"(xo0), "=&v"(xo1)
        : "v"(lane * 16), "s"(rx), "s"(rws), "v"((cg * 2 + (lane & 1)) * 2)
        : "memory");
    const rsrc_t rout = make_rsrc(out, 2 * DM * 2);
    unsigned touch = 0;
    if (TOUCH_OUT) asm volatile("buffer_load_dword %0, off, %1, 0" : "=v"(touch) : "s"(rout) : "memory");
    const int qk = cg >> 6, qc = cg & 63;
    const bool f1 = xi < 14 && !NO_FFN, f2 = xi >= 14 && xi < 28 && !NO_FFN;
    const int fi = xg * 14 + (xi < 14 ? xi : xi - 14);
    const int hk = fi / 28, hc = fi % 28;
    const int gk = fi >> 3, gc = fi & 7;
    {
      const unsigned sw = ((lane & 7) ^ (((lane >> 4) & 1) << 2)) << 4;
      const unsigned vq = (lane >> 3) * (HQD * 2) + sw;
#pragma unroll
      for (int i = 0; i < 16; ++i) dma_piece(rq, vq, (qk * 128 + 8 * i) * (HQD * 2) + qc * 128, lds0 + L_WQ + i * 1024);
      const rsrc_t rf = make_rsrc(f1 ? w1 : w2, f1 || f2 ? DM * FF * 2 : 0);
      const int ld = f1 ? FF * 2 : DM * 2;
      const unsigned vf = (lane >> 3) * ld + sw, sf = f1 ? hk * 128 * (FF * 2) + hc * 128 : gk * 128 * (DM * 2) + gc * 128;
#pragma unroll
      for (int i = 0; i < 16; ++i) dma_piece(rf, vf, sf + 8 * i * ld, lds0 + L_WF + i * 1024);
      const int ks = xg * 8 + (xi >> 2), cs = xi & 3;
      const rsrc_t ro = make_rsrc(wo, HQD * DM * 2);
      const unsigned vo = (lane >> 4) * (DM * 2) + (((lane & 15) ^ ((lane >> 4) << 2)) << 4);
#pragma unroll
      for (int i = 0; i < 16; ++i) dma_piece(ro, vo, (ks * 64 + 4 * i) * (DM * 2) + cs * 256, lds0 + L_WO + i * 1024);
    }
    wg_barrier();
    asm volatile("s_waitcnt vmcnt(48)" : "+v"(x0), "+v"(x1), "+v"(ev), "+v"(xo0), "+v"(xo1), "+v"(touch)::"memory");
    STAMP(1);
    const unsigned tag = max(__builtin_amdgcn_readfirstlane(ev) + 1u, 1u);
    {
      float a[2][8];
#pragma unroll
      for (int j = 0; j < 4; ++j) { a[0][2 * j] = bflo(x0[j]); a[0][2 * j + 1] = bfhi(x0[j]); a[1][2 * j] = bflo(x1[j]); a[1][2 * j + 1] = bfhi(x1[j]); }
#pragma unroll
      for (int b = 0; b < 2; ++b) {
        const float mean = wave_sum(((a[b][0] + a[b][1]) + (a[b][2] + a[b][3])) + ((a[b][4] + a[b][5]) + (a[b][6] + a[b][7]))) * (1.f / DM);
        float q = 0.f;
#pragma unroll
        for (int j = 0; j < 8; ++j) q = fmaf(a[b][j] - mean, a[b][j] - mean, q);
        const float var = wave_sum(q) * (1.f / DM);
        const float mb = rbf(mean), inv = __builtin_amdgcn_rcpf(rbf(__builtin_amdgcn_sqrtf(rbf(rbf(var) + 1e-5f))));
        u32x4 xn;
#pragma unroll
        for (int j = 0; j < 4; ++j) xn[j] = pk2(rbf(a[b][2 * j] - mb) * inv, rbf(a[b][2 * j + 1] - mb) * inv);
        lds_st<u32x4>(lds0 + L_XN + b * 1024 + lane * 16, xn);
      }
    }
    if (lane == 0) lds_st<unsigned>(lds0 + L_TAG, tag);
    asm volatile("s_waitcnt vmcnt(32) lgkmcnt(0)" ::: "memory");
    STAMP(2);
    {
      const f32x2 y = proj64(lds0 + L_XN + qk * 256, 1024, lds0 + L_WQ, lane);
      st_dev(rws, u32x4{__float_as_uint(y[0]), tag, __float_as_uint(y[1]), tag}, lane * 16, WS_QP + (qk * HQD + qc * 64) * 16);
    }
    if (f1) {
      wait_vm<17>();
      const f32x2 y = proj64(lds0 + L_XN + hk * 256, 1024, lds0 + L_WF, lane);
      st_dev(rws, u32x4{__float_as_uint(y[0]), tag, __float_as_uint(y[1]), tag}, lane * 16, WS_HP + (hk * FF + hc * 64) * 16);
    }
    if (QA_AT) sleep_until(th0 + QA_AT);
    {
      const f32x2 q = gather_quarters(rws, WS_QP + (qc * 64 + qk * 16) * 16, HQD * 16, tag, lane);
      if (lane < 16) __builtin_amdgcn_raw_buffer_store_b64(u32x2{pk2(q[0], q[1]), tag}, rws, lane * 8, WS_Q + (qc * 64 + qk * 16) * 8, ST_MID);
    }
    if (f1) {
      const f32x2 h = gather_quarters(rws, WS_HP + (hc * 64 + hk * 16) * 16, FF * 16, tag, lane);
      if (lane < 16) __builtin_amdgcn_raw_buffer_store_b64(u32x2{pk2(silu(rbf(h[0])), silu(rbf(h[1]))), tag}, rws, lane * 8, WS_H + (hc * 64 + hk * 16) * 8, ST_MID);
    }
    if (QB_AT) sleep_until(th0 + QB_AT);
    STAMP(3);
    {
      const int bh = aid >> 4, b = bh >> 3, kvh = bh & 7;
      u32x4 gq[4];
      for (unsigned spin = 0; spin < SPIN_CAP; ++spin) {
        asm volatile("" ::: "memory");
#pragma unroll
        for (int i = 0; i < 4; ++i) gq[i] = ld_dev(rws, lane * 16 + i * 1024, WS_Q + kvh * 4096);
        bool ok = true;
#pragma unroll
        for (int i = 0; i < 4; ++i) ok = ok & (gq[i][1] == tag) & (gq[i][3] == tag);
        if (__all(ok)) break;
        __builtin_amdgcn_s_sleep(QSLEEP);
      }
#pragma unroll
      for (int i = 0; i < 4; ++i)
        lds_st<unsigned>(lds0 + L_QI + i * 256 + lane * 4, b ? (gq[i][0] >> 16) | (gq[i][2] & 0xffff0000u) : (gq[i][0] & 0xffffu) | (gq[i][2] << 16));
    }
    wait_lds();
    if (lane == 0) *(volatile LAS unsigned*)(size_t)(lds0 + L_QR) = 1u;
    STAMP(4);

    if (f2) {
      u32x4 gh;
      for (unsigned spin = 0; spin < SPIN_CAP; ++spin) {
        asm volatile("" ::: "memory");
        gh = ld_dev(rws, lane * 16, WS_H + gk * 1024);
        if (__all((gh[1] == tag) & (gh[3] == tag))) break;
        __builtin_amdgcn_s_sleep(32);
      }
      lds_st<unsigned>(lds0 + L_HS + lane * 4, (gh[0] & 0xffffu) | (gh[2] << 16));
      lds_st<unsigned>(lds0 + L_HS + 256 + lane * 4, (gh[0] >> 16) | (gh[2] & 0xffff0000u));
      wait_lds();
      const f32x2 y = proj64(lds0 + L_HS, 256, lds0 + L_WF, lane);
      st_dev(rws, u32x4{__float_as_uint(y[0]), tag, __float_as_uint(y[1]), tag}, lane * 16, WS_FG + (gk * DM + gc * 64) * 16);
    }
    STAMP(5);
    if (PACE) {
      sleep_until(th0 + TS2);
      u32x4 ta;
      for (unsigned spin = 0; spin < SPIN_CAP; ++spin) {
        ta = *(volatile LAS u32x4*)(size_t)(lds0 + L_TS);
        if (__all((ta[0] != 0) & (ta[1] != 0) & (ta[2] != 0) & (ta[3] != 0))) break;
        __builtin_amdgcn_s_sleep(8);
      }
      const u32x4 p0 = *(volatile LAS u32x4*)(size_t)(lds0 + L_TP), p1 = *(volatile LAS u32x4*)(size_t)(lds0 + L_TP + 16);
      {
        const unsigned a = th0 + (((ta[0] - th0) + (ta[1] - th0) + (ta[2] - th0) + (ta[3] - th0)) >> 2);
        const unsigned b = th0 + (((p0[1] - th0) + (p0[3] - th0) + (p1[1] - th0) + (p1[3] - th0)) >> 2);
        const unsigned c4 = p0[0] + p0[2] + p1[0] + p1[2];
        if (lane == 0) st_dev(rws, u32x4{a, tag, (b << 8) | c4, tag}, 0, WS_PC + wg * 128);
      }
      sleep_until(now32() + TSD);
      u32x4 gp;
      for (unsigned spin = 0; spin < SPIN_CAP; ++spin) {
        asm volatile("" ::: "memory");
        gp = ld_dev(rws, (lane & (NSAMP - 1)) * 128, WS_PC + (wg & ~(NSAMP - 1)) * 128);
        if (__all((gp[1] == tag) & (gp[3] == tag))) break;
        __builtin_amdgcn_s_sleep(24);
      }
      const float tb = (float)((int)((gp[2] & 0xffffff00u) - (th0 << 8)) >> 8), ta_ = (float)(int)(gp[0] - th0);
      const float cb = 0.25f * (float)(gp[2] & 0xffu);
      const float r = fmaxf(cb - (float)CPA, 0.25f) * __builtin_amdgcn_rcpf(fmaxf(tb - ta_, 25.f)) * RBOOST;
      const float rsum = wave_sum(r);
      const float fin = wave_sum((float)(NSTEP - 3) - cb + r * tb) * __builtin_amdgcn_rcpf(rsum);
      if (lane == 0) *(volatile LAS unsigned*)(size_t)(lds0 + L_TL) = (th0 + (unsigned)((int)fin - PACE_OFF)) | 1u;
      if (TS3) {
        sleep_until(th0 + TS3);
        const u32x4 q0 = *(volatile LAS u32x4*)(size_t)(lds0 + L_TP), q1 = *(volatile LAS u32x4*)(size_t)(lds0 + L_TP + 16);
        const unsigned b = th0 + (((q0[1] - th0) + (q0[3] - th0) + (q1[1] - th0) + (q1[3] - th0)) >> 2);
        const unsigned pv = (b << 8) | (q0[0] + q0[2] + q1[0] + q1[2]);
        if (lane == 0) st_dev(rws, u32x4{pv, tag, pv, tag}, 0, WS_PD + wg * 128);
        sleep_until(now32() + TSD2);
        u32x4 gq2;
        for (unsigned spin = 0; spin < SPIN_CAP; ++spin) {
          asm volatile("" ::: "memory");
          gq2 = ld_dev(rws, (lane & (NSAMP - 1)) * 128, WS_PD + (wg & ~(NSAMP - 1)) * 128);
          if (__all((gq2[1] == tag) & (gq2[3] == tag))) break;
          __builtin_amdgcn_s_sleep(16);
        }
        const float tc = (float)((int)((gq2[0] & 0xffffff00u) - (th0 << 8)) >> 8), cc = 0.25f * (float)(gq2[0] & 0xffu);
        const float r2 = fmaxf(rsum, wave_sum((cc - cb) * __builtin_amdgcn_rcpf(fmaxf(tc - tb, 25.f))));
        const float fin2 = wave_sum((float)(NSTEP - 3) - cc) * __builtin_amdgcn_rcpf(r2) + wave_sum(tc) * (1.f / 64.f);
        if (lane == 0) *(volatile LAS unsigned*)(size_t)(lds0 + L_TL) = (th0 + (unsigned)((int)fin2 - PACE_OFF2)) | 1u;
      }
    }
    STAMP(11);
    wg_barrier();
    STAMP(6);
    u32x4 gs[2] = {};
    const unsigned vs = (lane >> 1) * 8192 + (lane & 1) * 16;
    if (PRE2) {
      gs[0] = ld_dev<LD_END>(rws, vs, WS_P2 + cg * 32);
      gs[1] = ld_dev<LD_END>(rws, vs, WS_P2 + 32 * 8192 + cg * 32);
    }
    float g0, g1;
    {
      u32x4 gf = {};
      const int pr = lane >> 1;
      const unsigned vo = (pr < 14 ? pr : 0) * (DM * 16) + (lane & 1) * 16;
      for (unsigned spin = 0; spin < (NO_FFN ? 0 : SPIN_CAP); ++spin) {
        asm volatile("" ::: "memory");
        gf = ld_dev<LD_END>(rws, vo, WS_FG + cg * 32);
        if (__all((gf[1] == tag) & (gf[3] == tag))) break;
      }
      float s0 = pr < 14 ? __uint_as_float(gf[0]) : 0.f, s1 = pr < 14 ? __uint_as_float(gf[2]) : 0.f;
      s0 += dpp<XOR2>(s0); s1 += dpp<XOR2>(s1);
      g0 = rbf(silu(rbf(quads_sum(s0))));
      g1 = rbf(silu(rbf(quads_sum(s1))));
    }
    asm volatile("" : "+v"(gs[0]), "+v"(gs[1]), "+v"(g0), "+v"(g1));
    STAMP(7);
    wg_barrier();
    STAMP(8);
    {
      const unsigned vo = vs;
      if (PIPE2) {
        unsigned px[2] = {0u, 0u}, py[2] = {0u, 0u}, pa[2] = {vo, vo};
        poll2(rws, tag, px, py, pa, WS_P2 + cg * 32, WS_P2 + 32 * 8192 + cg * 32);
        gs[0][0] = px[0]; gs[0][2] = py[0]; gs[1][0] = px[1]; gs[1][2] = py[1];
      } else {
      bool h0 = false, h1 = false;
      for (unsigned spin = 0; spin < SPIN_CAP; ++spin) {
        asm volatile("" ::: "memory");
        if (!MASK2 || !h0) gs[0] = ld_dev<LD_END>(rws, vo, WS_P2 + cg * 32);
        if (!MASK2 || !h1) gs[1] = ld_dev<LD_END>(rws, vo, WS_P2 + 32 * 8192 + cg * 32);
        h0 = (gs[0][1] == tag) & (gs[0][3] == tag);
        h1 = (gs[1][1] == tag) & (gs[1][3] == tag);
        if (__all(h0 & h1)) break;
      }
      }
      STAMP(9);
      float s0 = __uint_as_float(gs[0][0]) + __uint_as_float(gs[1][0]), s1 = __uint_as_float(gs[0][2]) + __uint_as_float(gs[1][2]);
      s0 += dpp<XOR2>(s0); s1 += dpp<XOR2>(s1);
      s0 = rbf(quads_sum(s0)); s1 = rbf(quads_sum(s1));
      if (lane < 2) {
        __builtin_amdgcn_raw_buffer_store_b16(__builtin_bit_cast(u16, (__bf16)(bflo(xo0) + rbf(s0 * g0))), rout, lane * 2, cg * 4, OUT_AUX);
        __builtin_amdgcn_raw_buffer_store_b16(__builtin_bit_cast(u16, (__bf16)(bflo(xo1) + rbf(s1 * g1))), rout, lane * 2, DM * 2 + cg * 4, OUT_AUX);
      }
      if (wg == 0 && lane == 0) __builtin_amdgcn_raw_buffer_store_b32(tag, rws, 0, WS_EPOCH, 16);
    }
    STAMP(10);
    STAMP_FLUSH(48, 12);
  }
}

}

extern "C" void kernel_launch(void* const* d_in, const int* in_sizes, int n_in, void* d_out, int out_size, void* d_ws, size_t ws_size,
                              hipStream_t stream) {
  (void)hipFuncSetAttribute((const void*)fused_decode_kernel, hipFuncAttributeMaxDynamicSharedMemorySize, L_END);
  fused_decode_kernel<<<NWG, NTHR, L_END, stream>>>((const u16*)d_in[0], (const u16*)d_in[1], (const u16*)d_in[2], (const u16*)d_in[3],
                                                    (const u16*)d_in[4], (const u16*)d_in[6], (const u16*)d_in[7], (u16*)d_out, (char*)d_ws);
}
```

```cpp
#include <hip/hip_runtime.h>
#include <hip/hip_bf16.h>
#include <type_traits>

#define STAMPS 0
#define TSTAMP 0
#define WIN 4
#define WIN2 12
#define QSLEEP 8
#define SPEC_TH 3
#define PACE 1
#define CPA 7
#define TS2 1800
#define TSD 150
#define RBOOST 1.015f
#define PACE_OFF 115
#define NSAMP 32
#define WO4 0
#define CODE_KB 32

namespace {

typedef unsigned short u16;
typedef short s16x4 __attribute__((ext_vector_type(4)));
typedef __bf16 bf16x8 __attribute__((ext_vector_type(8)));
typedef __bf16 bf16x2 __attribute__((ext_vector_type(2)));
typedef float f32x2 __attribute__((ext_vector_type(2)));
typedef float f32x4 __attribute__((ext_vector_type(4)));
typedef unsigned u32x2 __attribute__((ext_vector_type(2)));
typedef unsigned u32x4 __attribute__((ext_vector_type(4)));
typedef __amdgpu_buffer_rsrc_t rsrc_t;
#define LAS __attribute__((address_space(3)))
#define GAS __attribute__((address_space(1)))

constexpr int NWG = 256, NTHR = 320;
constexpr int DM = 512, HQD = 4096, SK = 32768, DH = 128, FF = 1792;
constexpr int NSLOT = 3, NSTEP = 32, NPRE = 5;
constexpr int KV_BYTES = 2 * 8 * SK * DH * 2;
constexpr float SCALE = 0.08838834764831845f, L2E = 1.4426950408889634f;
constexpr float M_INIT = -3.0e38f;
constexpr unsigned SPIN_CAP = 1u << 18;

constexpr int WS_EPOCH = 0;
constexpr int WS_Q = 4096;
constexpr int WS_H = WS_Q + HQD * 8;
constexpr int WS_ML = 65536;
constexpr int WS_PC = WS_ML + NWG * 128;
constexpr int WS_QP = WS_PC + NWG * 128;
constexpr int WS_HP = WS_QP + 4 * HQD * 8;
constexpr int WS_FG = WS_HP + 4 * FF * 8;
constexpr int WS_P2 = WS_FG + 14 * DM * 8;
constexpr int WS_P1 = WS_P2 + 64 * DM * 8;
constexpr int WS_END = WS_P1 + NWG * 2048;
constexpr int WS_STAMP = 8 << 20;
static_assert(WS_H + FF * 8 <= WS_ML, "workspace map");

constexpr int L_RING = 0;
constexpr int L_WQ = 4 * NSLOT * 8192;
constexpr int L_WF = L_WQ + 16384;
constexpr int L_WO = L_WF + 16384;
constexpr int L_XN = L_WO + 16384;
constexpr int L_QI = L_XN + 2048;
constexpr int L_OS = L_QI + 1024;
constexpr int L_HS = L_OS + 256;
constexpr int L_ML = L_HS + 512;
constexpr int L_TAG = L_ML + 128;
constexpr int L_QR = L_TAG + 4;
constexpr int L_TL = L_TAG + 16;
constexpr int L_TS = L_TL + 16;
constexpr int L_TP = L_TS + 16;
constexpr int L_END = L_TP + 32;

__device__ __forceinline__ float rbf(float f) { return (float)(__bf16)f; }
__device__ __forceinline__ unsigned pk2(float lo, float hi) {
  bf16x2 v = {(__bf16)lo, (__bf16)hi};
  return __builtin_bit_cast(unsigned, v);
}
__device__ __forceinline__ float bflo(unsigned u) { return __uint_as_float(u << 16); }
__device__ __forceinline__ float bfhi(unsigned u) { return __uint_as_float(u & 0xffff0000u); }
template <int CTRL> __device__ __forceinline__ float dpp(float x) {
  return __builtin_bit_cast(float, __builtin_amdgcn_mov_dpp(__builtin_bit_cast(int, x), CTRL, 0xf, 0xf, true));
}
constexpr int XOR1 = 0xB1, XOR2 = 0x4E, ROR4 = 0x124, ROR8 = 0x128;
__device__ __forceinline__ float swap16_sum(float a, float b) {
  auto s = __builtin_amdgcn_permlane16_swap(__float_as_uint(a), __float_as_uint(b), false, false);
  return __uint_as_float(s[0]) + __uint_as_float(s[1]);
}
__device__ __forceinline__ float swap32_sum(float a, float b) {
  auto s = __builtin_amdgcn_permlane32_swap(__float_as_uint(a), __float_as_uint(b), false, false);
  return __uint_as_float(s[0]) + __uint_as_float(s[1]);
}
__device__ __forceinline__ float rows_sum(float x) { x = swap16_sum(x, x); return swap32_sum(x, x); }
__device__ __forceinline__ float rows_max(float x) {
  auto s = __builtin_amdgcn_permlane16_swap(__float_as_uint(x), __float_as_uint(x), false, false);
  x = fmaxf(__uint_as_float(s[0]), __uint_as_float(s[1]));
  auto t = __builtin_amdgcn_permlane32_swap(__float_as_uint(x), __float_as_uint(x), false, false);
  return fmaxf(__uint_as_float(t[0]), __uint_as_float(t[1]));
}
__device__ __forceinline__ float quads_sum(float x) { x += dpp<ROR4>(x); x += dpp<ROR8>(x); return rows_sum(x); }
__device__ __forceinline__ float quads_max(float x) { x = fmaxf(x, dpp<ROR4>(x)); x = fmaxf(x, dpp<ROR8>(x)); return rows_max(x); }
__device__ __forceinline__ float wave_sum(float x) { x += dpp<XOR1>(x); x += dpp<XOR2>(x); return quads_sum(x); }
__device__ __forceinline__ float ex2(float x) { return __builtin_amdgcn_exp2f(x); }
__device__ __forceinline__ float silu(float v) { return v * __builtin_amdgcn_rcpf(1.f + ex2(-v * L2E)); }

template <int N> __device__ __forceinline__ void wait_vm() {
  __builtin_amdgcn_sched_barrier(0);
  asm volatile("s_waitcnt vmcnt(%0)" ::"i"(N) : "memory");
}
__device__ __forceinline__ void wait_lds() { asm volatile("s_waitcnt lgkmcnt(0)" ::: "memory"); }
__device__ __forceinline__ void wg_barrier() {
  asm volatile("s_waitcnt lgkmcnt(0)\n\ts_barrier" ::: "memory");
}
__device__ __forceinline__ unsigned now32() { return (unsigned)__builtin_amdgcn_s_memrealtime(); }
__device__ __forceinline__ rsrc_t make_rsrc(const void* p, int bytes) {
  return __builtin_amdgcn_make_buffer_rsrc((void*)p, (short)0, bytes, 0x00020000);
}
constexpr int SC1 = 16;
__device__ __forceinline__ u32x4 ld_dev(rsrc_t r, unsigned voff, unsigned soff) { return __builtin_amdgcn_raw_buffer_load_b128(r, voff, soff, SC1); }
__device__ __forceinline__ void st_dev(rsrc_t r, u32x4 v, unsigned voff, unsigned soff) { __builtin_amdgcn_raw_buffer_store_b128(v, r, voff, soff, SC1); }
__device__ __forceinline__ bool pair_ok(const u32x4& g, unsigned tag) { return (g[1] == tag) & (g[3] == tag); }
__device__ __forceinline__ bool quad_ok(const u32x4& g, unsigned tag) {
  return (__builtin_amdgcn_perm(g[1], g[0], 0x0c0c0400u) | __builtin_amdgcn_perm(g[3], g[2], 0x04000c0cu)) == tag;
}
__device__ __forceinline__ float quad_val(unsigned w) { return __uint_as_float(w & 0xffffff00u); }
__device__ __forceinline__ void st_quad(rsrc_t r, float a, float b, unsigned tag, int lane, unsigned soff) {
  const float c = dpp<XOR1>(a), d = dpp<XOR1>(b);
  const u32x4 q = {(__float_as_uint(a) & 0xffffff00u) | (tag & 255u), (__float_as_uint(b) & 0xffffff00u) | ((tag >> 8) & 255u),
                   (__float_as_uint(c) & 0xffffff00u) | ((tag >> 16) & 255u), (__float_as_uint(d) & 0xffffff00u) | (tag >> 24)};
  if (!(lane & 1)) st_dev(r, q, lane * 8, soff);
}
__device__ __forceinline__ float quad_sum(float v0, float v1, float v2, float v3) {
  float x = swap16_sum(swap32_sum(v0, v2), swap32_sum(v1, v3));
  x += dpp<ROR8>(x); x += dpp<ROR4>(x); x += dpp<XOR2>(x); x += dpp<XOR1>(x);
  return x;
}
__device__ __forceinline__ s16x4 tr_read(unsigned addr) { return __builtin_amdgcn_ds_read_tr16_b64_v4i16((LAS s16x4*)(size_t)addr); }
template <typename T> __device__ __forceinline__ T lds_ld(unsigned addr) { return *(const LAS T*)(size_t)addr; }
template <typename T> __device__ __forceinline__ void lds_st(unsigned addr, T v) { *(LAS T*)(size_t)addr = v; }

__device__ __forceinline__ void dma_piece(rsrc_t r, unsigned voff, unsigned soff, unsigned dst) {
  asm volatile("s_mov_b32 m0, %0\n\ts_nop 0\n\tbuffer_load_dwordx4 %1, %2, %3 offen nt lds" ::"s"(dst), "v"(voff), "s"(r), "s"(soff) : "memory");
}
__device__ __forceinline__ void dma_half(rsrc_t r, unsigned v0, unsigned v1, unsigned v2, unsigned v3, unsigned soff, unsigned dst) {
  asm volatile(
      "s_mov_b32 m0, %0\n\ts_nop 0\n\t"
      "buffer_load_dwordx4 %1, %5, %6 offen nt lds\n\t"
      "buffer_load_dwordx4 %2, %5, %6 offen offset:1024 nt lds\n\t"
      "buffer_load_dwordx4 %3, %5, %6 offen offset:2048 nt lds\n\t"
      "buffer_load_dwordx4 %4, %5, %6 offen offset:3072 nt lds"
      ::"s"(dst), "v"(v0), "v"(v1), "v"(v2), "v"(v3), "s"(r), "s"(soff)
      : "memory");
}
template <int A> __device__ __forceinline__ f32x4 mfma4(s16x4 a, s16x4 b, f32x4 c) { return __builtin_amdgcn_mfma_f32_4x4x4bf16_1k(a, b, c, 4, A, 0); }
template <int A, int N> struct Proj {
  static __device__ __forceinline__ void run(const s16x4 (&av)[2], unsigned ba, f32x4 (&c)[4]) {
    c[A & 3] = mfma4<A & 15>(av[A >> 4], tr_read(ba + A * 512), c[A & 3]);
    if constexpr (A + 1 < N) Proj<A + 1, N>::run(av, ba, c);
  }
};
template <int A> struct ProjReg {
  static __device__ __forceinline__ void run(s16x4 a, const s16x4 (&w)[16], f32x4 (&c)[4]) {
    c[A & 3] = mfma4<A>(a, w[A], c[A & 3]);
    if constexpr (A + 1 < 16) ProjReg<A + 1>::run(a, w, c);
  }
};
__device__ __forceinline__ f32x2 proj64(unsigned a_img, int a_stride, unsigned w_img, int lane) {
  const int q = (lane & 15) >> 2, p = lane & 3;
  const unsigned aa = a_img + (lane & 1) * a_stride + (lane >> 2) * 8;
  const s16x4 av[2] = {lds_ld<s16x4>(aa), lds_ld<s16x4>(aa + 128)};
  const unsigned ba = w_img + q * 128 + (((2 * (lane >> 4) + (p >> 1)) ^ ((q >> 1) << 2)) << 4) + (p & 1) * 8;
  f32x4 c[4] = {};
  Proj<0, 32>::run(av, ba, c);
  return f32x2{(c[0][0] + c[1][0]) + (c[2][0] + c[3][0]), (c[0][1] + c[1][1]) + (c[2][1] + c[3][1])};
}
__device__ __forceinline__ f32x4 gather_quarters(rsrc_t rws, unsigned base, int quarter_bytes, unsigned tag, int lane) {
  u32x4 g;
  for (unsigned spin = 0; spin < SPIN_CAP; ++spin) {
    asm volatile("" ::: "memory");
    g = ld_dev(rws, (lane >> 4) * quarter_bytes + (lane & 7) * 16, base);
    if (__all(quad_ok(g, tag))) break;
    __builtin_amdgcn_s_sleep(QSLEEP);
  }
  return f32x4{rows_sum(quad_val(g[0])), rows_sum(quad_val(g[1])), rows_sum(quad_val(g[2])), rows_sum(quad_val(g[3]))};
}

template <int N> using Int = std::integral_constant<int, N>;
struct KFrag { bf16x8 k[4]; };
struct VFrag { s16x4 v[4][2]; };
struct State { float m, l; f32x4 o[2]; };

__device__ __forceinline__ void read_k(KFrag& t, const unsigned (&ka)[4], int off) {
#pragma unroll
  for (int s = 0; s < 4; ++s) t.k[s] = lds_ld<bf16x8>(ka[s] + off);
  asm volatile("s_waitcnt lgkmcnt(0)" : "+v"(t.k[0]), "+v"(t.k[1]), "+v"(t.k[2]), "+v"(t.k[3])::"memory");
}
__device__ __forceinline__ void read_v(VFrag& t, const unsigned (&va)[2], int off) {
#pragma unroll
  for (int a = 0; a < 4; ++a)
#pragma unroll
    for (int h = 0; h < 2; ++h) t.v[a][h] = tr_read(va[h] + off + a * 1024);
  asm volatile("s_waitcnt lgkmcnt(0)"
               : "+v"(t.v[0][0]), "+v"(t.v[0][1]), "+v"(t.v[1][0]), "+v"(t.v[1][1]), "+v"(t.v[2][0]), "+v"(t.v[2][1]), "+v"(t.v[3][0]), "+v"(t.v[3][1])
               :
               : "memory");
}
__device__ __forceinline__ s16x4 score_part(const KFrag& t, const bf16x8 (&qf)[4], State& st) {
  f32x4 sc = {0.f, 0.f, 0.f, 0.f};
#pragma unroll
  for (int s = 0; s < 4; ++s) sc = __builtin_amdgcn_mfma_f32_16x16x32_bf16(t.k[s], qf[s], sc, 0, 0, 0);
  float x[4];
#pragma unroll
  for (int r = 0; r < 4; ++r) x[r] = rbf(rbf(sc[r]) * SCALE);
  const float mn = fmaxf(st.m, rows_max(fmaxf(fmaxf(x[0], x[1]), fmaxf(x[2], x[3]))));
  const float alpha = ex2((st.m - mn) * L2E);
  st.m = mn;
  float p[4];
#pragma unroll
  for (int r = 0; r < 4; ++r) p[r] = ex2((x[r] - mn) * L2E);
  st.l = fmaf(st.l, alpha, (p[0] + p[1]) + (p[2] + p[3]));
  const float a0 = dpp<0x00>(alpha), a1 = dpp<0x55>(alpha), a2 = dpp<0xAA>(alpha), a3 = dpp<0xFF>(alpha);
#pragma unroll
  for (int h = 0; h < 2; ++h) { st.o[h][0] *= a0; st.o[h][1] *= a1; st.o[h][2] *= a2; st.o[h][3] *= a3; }
  const u32x2 pp = {pk2(p[0], p[1]), pk2(p[2], p[3])};
  return __builtin_bit_cast(s16x4, pp);
}
__device__ __forceinline__ void value_part(const VFrag& t, s16x4 pa, State& st) {
#pragma unroll
  for (int h = 0; h < 2; ++h) st.o[h] = mfma4<0>(pa, t.v[0][h], st.o[h]);
#pragma unroll
  for (int h = 0; h < 2; ++h) st.o[h] = mfma4<4>(pa, t.v[1][h], st.o[h]);
#pragma unroll
  for (int h = 0; h < 2; ++h) st.o[h] = mfma4<8>(pa, t.v[2][h], st.o[h]);
#pragma unroll
  for (int h = 0; h < 2; ++h) st.o[h] = mfma4<12>(pa, t.v[3][h], st.o[h]);
}

#define STAMP(i) do {} while (0)
#define STAMP_FLUSH(base, n) do {} while (0)

__global__ void __launch_bounds__(NTHR, 1)
fused_decode_kernel(const u16* __restrict__ x, const u16* __restrict__ kc, const u16* __restrict__ vc, const u16* __restrict__ wq,
                    const u16* __restrict__ wo, const u16* __restrict__ w1, const u16* __restrict__ w2, u16* __restrict__ out, char* ws) {
  unsigned long long pc;
  asm volatile("s_getpc_b64 %0" : "=s"(pc));
  extern __shared__ __attribute__((aligned(16))) unsigned char lds[];
  const unsigned lds0 = (unsigned)(size_t)(LAS unsigned char*)lds;
  const int lane = threadIdx.x & 63, wave = __builtin_amdgcn_readfirstlane(threadIdx.x >> 6), wg = blockIdx.x;
  const int xg = wg & 7, xi = wg >> 3;
  asm volatile("" ::"s"(x), "s"(kc), "s"(vc), "s"(wq), "s"(wo), "s"(w1), "s"(w2), "s"(out), "s"(ws));
  const rsrc_t rws = make_rsrc(ws, WS_END);
  const unsigned sw128 = ((lane & 7) ^ (((lane >> 4) & 1) << 2)) << 4;
  STAMP(0);

  if (wave < 4) {
    const rsrc_t rk = make_rsrc(kc, KV_BYTES), rv = make_rsrc(vc, KV_BYTES);
    const int g = lane >> 4, c = lane & 15;
    unsigned vk[4];
#pragma unroll
    for (int u = 0; u < 4; ++u) vk[u] = g * 256 + ((c ^ (4 * u + g)) << 4);
    const unsigned vv = g * 256 + ((c ^ (g << 2)) << 4);
    const unsigned ring = lds0 + L_RING + wave * (NSLOT * 8192);
    const unsigned tile0 = (unsigned)wg * (2048 * 256) + wave * 4096;
    unsigned ka[4], va[2];
#pragma unroll
    for (int s = 0; s < 4; ++s) ka[s] = ring + c * 256 + (((4 * s + g) ^ c) << 4);
#pragma unroll
    for (int h = 0; h < 2; ++h) va[h] = ring + 4096 + (c >> 2) * 256 + (((8 * h + 2 * g + ((c & 3) >> 1)) ^ ((c >> 2) << 2)) << 4) + (c & 1) * 8;
    auto issue_k = [&](int t, int slot) { dma_half(rk, vk[0], vk[1], vk[2], vk[3], tile0 + t * 16384, ring + slot * 8192); };
    auto issue_v = [&](int t, int slot) { dma_half(rv, vv, vv, vv, vv, tile0 + t * 16384, ring + slot * 8192 + 4096); };

    wg_barrier();
#pragma unroll
    for (int t = 0; t < NSLOT; ++t) {
      wait_vm<WIN>(); issue_k(t, t);
      wait_vm<WIN>(); issue_v(t, t);
    }
    unsigned tsv = TSTAMP ? now32() : 0u;
    KFrag pk[NPRE]; VFrag pv[NPRE];
#pragma unroll
    for (int t = 0; t < NPRE; ++t) {
      wait_vm<WIN>(); read_k(pk[t], ka, (t % NSLOT) * 8192); issue_k(t + NSLOT, t % NSLOT);
      wait_vm<WIN>();
      if (TSTAMP) { const unsigned tt = now32(); tsv = lane == t + 2 ? tt : tsv; }
      read_v(pv[t], va, (t % NSLOT) * 8192); issue_v(t + NSLOT, t % NSLOT);
    }
    STAMP(1);
    for (unsigned spin = 0; spin < SPIN_CAP * 16 && *(volatile LAS unsigned*)(size_t)(lds0 + L_QR) == 0; ++spin) __builtin_amdgcn_s_sleep(1);
    asm volatile("" ::: "memory");
    STAMP(2);
    bf16x8 qf[4];
#pragma unroll
    for (int s = 0; s < 4; ++s) qf[s] = lds_ld<bf16x8>(lds0 + L_QI + (c & 3) * 256 + (4 * s + g) * 16);
    const unsigned tag = lds_ld<unsigned>(lds0 + L_TAG);

    State st;
    st.m = M_INIT; st.l = 0.f;
    st.o[0] = f32x4{0.f, 0.f, 0.f, 0.f}; st.o[1] = f32x4{0.f, 0.f, 0.f, 0.f};
    unsigned prev = now32();
    auto pace = [&](int n) {
      if (PACE) {
        const unsigned tl = __builtin_amdgcn_readfirstlane(*(volatile LAS unsigned*)(size_t)(lds0 + L_TL));
        const int d = (int)(tl - prev);
        if (tl != 0 && d > 0) {
          const unsigned due = prev + (unsigned)__builtin_amdgcn_readfirstlane((int)((float)d * __builtin_amdgcn_rcpf((float)n)));
          while ((int)(now32() - due) < 0) __builtin_amdgcn_s_sleep(1);
        }
        prev = now32();
      }
    };
    auto step = [&](int t, int slot, auto win) {
      KFrag kf; VFrag vf;
      wait_vm<win.value>(); read_k(kf, ka, slot * 8192);
      pace(2 * (NSTEP - NSLOT - t)); issue_k(t + NSLOT, slot);
      const s16x4 pa = score_part(kf, qf, st);
      wait_vm<win.value>();
      if (TSTAMP) { const unsigned tt = now32(); tsv = lane == t + 2 ? tt : tsv; }
      if (PACE) {
        const unsigned tn = now32();
        *(volatile LAS u32x2*)(size_t)(lds0 + L_TP + wave * 8) = u32x2{(unsigned)t, tn};
        if (t == CPA) *(volatile LAS unsigned*)(size_t)(lds0 + L_TS + wave * 4) = tn | 1u;
      }
      read_v(vf, va, slot * 8192);
      pace(2 * (NSTEP - NSLOT - t) - 1); issue_v(t + NSLOT, slot);
      value_part(vf, pa, st);
    };
    using W1 = std::integral_constant<int, WIN>;
    using W2 = std::integral_constant<int, WIN2>;
    static_assert(2 * NSLOT >= NPRE && (NSTEP - NPRE) % NSLOT == 0 && WIN <= 20 && WIN2 <= 20, "tile schedule");
#pragma unroll
    for (int j = 0; j < 2 * NSLOT; ++j) {
      step(NPRE + j, (NPRE + j) % NSLOT, W1{});
      if (j < NPRE) value_part(pv[j], score_part(pk[j], qf, st), st);
    }
#pragma unroll
    for (int j = 0; j < NSLOT; ++j) step(NPRE + 2 * NSLOT + j, (NPRE + j) % NSLOT, W1{});
    for (int t0 = NPRE + 3 * NSLOT; t0 < NSTEP - NSLOT; t0 += NSLOT) {
#pragma unroll
      for (int j = 0; j < NSLOT; ++j) step(t0 + j, (NPRE + j) % NSLOT, W2{});
    }
    STAMP(3);
    const int ks = xg * 8 + (xi >> 2), cs = xi & 3;
    if (WO4 || wave < 2) {
      const rsrc_t ro = make_rsrc(wo, HQD * DM * 2);
#pragma unroll
      for (int i = 0; i < (WO4 ? 4 : 8); ++i) {
        const int pi = WO4 ? (wave >> 1) * 4 + i : i;
        dma_piece(ro, (lane >> 3) * (DM * 2) + sw128, (ks * 64 + 8 * pi) * (DM * 2) + cs * 256 + (wave & 1) * 128, lds0 + L_WO + (wave & 1) * 8192 + pi * 1024);
      }
    }
    auto wait_tail = [&](auto n) {
      if (WO4) wait_vm<n.value + 4>(); else if (wave < 2) wait_vm<n.value + 8>(); else wait_vm<n.value>();
    };
    float lsum = 0.f;
#pragma unroll
    for (int j = 0; j < NSLOT; ++j) {
      KFrag kf; VFrag vf;
      if (j == 0) wait_tail(Int<20>{}); else if (j == 1) wait_tail(Int<12>{}); else wait_tail(Int<4>{});
      read_k(kf, ka, ((NPRE + j) % NSLOT) * 8192);
      const s16x4 pa = score_part(kf, qf, st);
      if (j == NSLOT - 1) {
        lsum = rows_sum(st.l);
        if (lane < 4) lds_st<f32x2>(lds0 + L_ML + (wave * 4 + lane) * 8, f32x2{st.m, lsum});
      }
      if (j == 0) wait_tail(Int<16>{}); else if (j == 1) wait_tail(Int<8>{}); else wait_tail(Int<0>{});
      if (j == NSLOT - 1) STAMP(4);
      read_v(vf, va, ((NPRE + j) % NSLOT) * 8192);
      value_part(vf, pa, st);
    }

#pragma unroll
    for (int h = 0; h < 2; ++h)
#pragma unroll
      for (int r = 0; r < 4; ++r) lds_st<float>(ring + (r * 128 + h * 64 + lane) * 4, st.o[h][r]);
    STAMP(5);
    wg_barrier();
    STAMP(6);
    {
      f32x2 ml[4], ov[4];
#pragma unroll
      for (int w = 0; w < 4; ++w) {
        ml[w] = lds_ld<f32x2>(lds0 + L_ML + (w * 4 + wave) * 8);
        ov[w] = lds_ld<f32x2>(lds0 + L_RING + w * (NSLOT * 8192) + (wave * 128 + 2 * lane) * 4);
      }
      const float M = fmaxf(fmaxf(ml[0][0], ml[1][0]), fmaxf(ml[2][0], ml[3][0]));
      float o0 = 0.f, o1 = 0.f;
#pragma unroll
      for (int w = 0; w < 4; ++w) {
        const float wt = ex2((ml[w][0] - M) * L2E);
        o0 = fmaf(wt, ov[w][0], o0); o1 = fmaf(wt, ov[w][1], o1);
      }
      st_quad(rws, o0, o1, tag, lane, WS_P1 + wg * 2048 + wave * 512);
    }
    const unsigned wa = lds0 + L_WO + (wave & 1) * 8192 + (c >> 2) * 128 + (((2 * g + ((c & 3) >> 1)) ^ ((c >> 3) << 2)) << 4) + (c & 1) * 8;
    s16x4 wf[16];
    if (!WO4 && wave < 2) {
      wait_vm<1>();
#pragma unroll
      for (int a = 0; a < 16; ++a) wf[a] = tr_read(wa + a * 512);
    }
    STAMP(7);
    {
      const int b = wave >> 1, half = wave & 1, sp = lane >> 2, p = lane & 3;
      const int src0 = (b * 8 + xg) * 16, hl = (ks >> 1) & 3;
      const unsigned vo = sp * 2048 + p * 16, so = WS_P1 + src0 * 2048 + hl * 512 + ((ks & 1) * 2 + half) * 128;
      const unsigned vm = sp * 128, sm = WS_ML + src0 * 128 + hl * 16;
      u32x4 gv[2] = {}, gm = {};
      bool hm = false, hd = false, spec = false;
      for (unsigned spin = 0; spin < SPIN_CAP; ++spin) {
        asm volatile("" ::: "memory");
        const bool ldd = !hd & (hm | spec);
        if (!hm) gm = ld_dev(rws, vm, sm);
        if (ldd) { gv[0] = ld_dev(rws, vo, so); gv[1] = ld_dev(rws, vo + 64, so); }
        hm = pair_ok(gm, tag);
        hd = quad_ok(gv[0], tag) & quad_ok(gv[1], tag);
        if (__all(hm & hd)) break;
        spec = __popcll(__ballot(!hm)) <= 4 * SPEC_TH;
        if (!spec) __builtin_amdgcn_s_sleep(4);
      }
      STAMP(8);
      const float ms = __uint_as_float(gm[0]), M = quads_max(ms);
      const float wt = ex2((ms - M) * L2E);
      const float inv = __builtin_amdgcn_rcpf(quads_sum(wt * __uint_as_float(gm[2])));
      float u[4], e[2];
#pragma unroll
      for (int j = 0; j < 4; ++j) u[j] = swap32_sum(wt * quad_val(gv[0][j]), wt * quad_val(gv[1][j]));
#pragma unroll
      for (int j = 0; j < 2; ++j) { e[j] = swap16_sum(u[j], u[j + 2]); e[j] += dpp<ROR4>(e[j]); e[j] += dpp<ROR8>(e[j]); e[j] *= inv; }
      if ((c >> 2) == 0) lds_st<unsigned>(lds0 + L_OS + b * 128 + (half * 16 + 8 * (g >> 1) + 2 * p + (g & 1)) * 4, pk2(e[0], e[1]));
    }
    wg_barrier();
    STAMP(9);
    if (wave < 2) {
      const s16x4 oa = lds_ld<s16x4>(lds0 + L_OS + (lane & 1) * 128 + (lane >> 2) * 8);
      f32x4 acc[4] = {};
      if (WO4) {
        const s16x4 av[2] = {oa, oa};
        Proj<0, 16>::run(av, wa, acc);
      } else {
        ProjReg<0>::run(oa, wf, acc);
      }
      st_quad(rws, (acc[0][0] + acc[1][0]) + (acc[2][0] + acc[3][0]), (acc[0][1] + acc[1][1]) + (acc[2][1] + acc[3][1]), tag, lane,
              WS_P2 + ks * (DM * 8) + (cs * 128 + wave * 64) * 8);
    }
    STAMP(10);
    if (STAMPS == 1) STAMP_FLUSH(wave * 12, 12);
  } else {
    __builtin_amdgcn_s_setprio(3);
    const unsigned th0 = now32();
    auto sleep_until = [&](unsigned due) { while ((int)(now32() - due) < 0) __builtin_amdgcn_s_sleep(4); };
    if (lane < 20) lds_st<unsigned>(lds0 + L_TAG + lane * 4, 0u);
    const int cg = xg * 32 + xi;
    const int qk = cg >> 6, qc = cg & 63;
    const bool f1 = xi < 14, f2 = xi >= 14 && xi < 28;
    const int fi = xg * 14 + (xi < 14 ? xi : xi - 14);
    const int hk = fi / 28, hc = fi % 28;
    const int gk = fi >> 3, gc = fi & 7;
    const unsigned vout = (((lane >> 4) & 1) * DM + cg * 2 + (lane >> 5)) * 2;
    const rsrc_t rx = make_rsrc(x, 2 * DM * 2);
    unsigned ev, xo;
    u32x4 x0, x1, code;
    asm volatile(
        "s_nop 4\n\t"
        "buffer_load_dwordx4 %0, %5, %6, 0 offen\n\t"
        "buffer_load_dwordx4 %1, %5, %6, 0 offen offset:1024\n\t"
        "buffer_load_dword %2, off, %7, 0 sc1\n\t"
        "buffer_load_ushort %3, %8, %6, 0 offen\n\t"
        "buffer_load_dwordx4 %4, %5, %9, %10 offen"
        : "=&v"(x0), "=&v"(x1), "=&v"(ev), "=&v"(xo), "=&v"(code)
        : "v"(lane * 16), "s"(rx), "s"(rws), "v"(vout), "s"(make_rsrc((const void*)(pc & ~255ull), CODE_KB * 1024)), "s"(xi * 1024)
        : "memory");
    {
      const rsrc_t rq = make_rsrc(wq, DM * HQD * 2);
#pragma unroll
      for (int i = 0; i < 16; ++i) dma_piece(rq, (lane >> 3) * (HQD * 2) + sw128, (qk * 128 + 8 * i) * (HQD * 2) + qc * 128, lds0 + L_WQ + i * 1024);
      const rsrc_t rf = make_rsrc(f1 ? w1 : w2, f1 || f2 ? DM * FF * 2 : 0);
      const int ld = f1 ? FF * 2 : DM * 2;
      const unsigned sf = f1 ? hk * 128 * (FF * 2) + hc * 128 : gk * 128 * (DM * 2) + gc * 128;
#pragma unroll
      for (int i = 0; i < 16; ++i) dma_piece(rf, (lane >> 3) * ld + sw128, sf + 8 * i * ld, lds0 + L_WF + i * 1024);
    }
    wg_barrier();
    asm volatile("s_waitcnt vmcnt(32)" : "+v"(x0), "+v"(x1), "+v"(ev), "+v"(xo), "+v"(code)::"memory");
    STAMP(1);
    const unsigned epoch = max(__builtin_amdgcn_readfirstlane(ev) + 1u, 1u), tag = epoch * 0x9E3779B1u;
    {
      float a[2][8];
#pragma unroll
      for (int j = 0; j < 4; ++j) { a[0][2 * j] = bflo(x0[j]); a[0][2 * j + 1] = bfhi(x0[j]); a[1][2 * j] = bflo(x1[j]); a[1][2 * j + 1] = bfhi(x1[j]); }
#pragma unroll
      for (int b = 0; b < 2; ++b) {
        const float mean = wave_sum(((a[b][0] + a[b][1]) + (a[b][2] + a[b][3])) + ((a[b][4] + a[b][5]) + (a[b][6] + a[b][7]))) * (1.f / DM);
        float q = 0.f;
#pragma unroll
        for (int j = 0; j < 8; ++j) q = fmaf(a[b][j] - mean, a[b][j] - mean, q);
        const float var = wave_sum(q) * (1.f / DM);
        const float mb = rbf(mean), inv = __builtin_amdgcn_rcpf(rbf(__builtin_amdgcn_sqrtf(rbf(rbf(var) + 1e-5f))));
        u32x4 xn;
#pragma unroll
        for (int j = 0; j < 4; ++j) xn[j] = pk2(rbf(a[b][2 * j] - mb) * inv, rbf(a[b][2 * j + 1] - mb) * inv);
        lds_st<u32x4>(lds0 + L_XN + b * 1024 + lane * 16, xn);
      }
    }
    if (lane == 0) lds_st<unsigned>(lds0 + L_TAG, tag);
    asm volatile("s_waitcnt vmcnt(16) lgkmcnt(0)" ::: "memory");
    STAMP(2);
    {
      const f32x2 y = proj64(lds0 + L_XN + qk * 256, 1024, lds0 + L_WQ, lane);
      st_quad(rws, y[0], y[1], tag, lane, WS_QP + (qk * HQD + qc * 64) * 8);
    }
    if (f1) {
      wait_vm<1>();
      const f32x2 y = proj64(lds0 + L_XN + hk * 256, 1024, lds0 + L_WF, lane);
      st_quad(rws, y[0], y[1], tag, lane, WS_HP + (hk * FF + hc * 64) * 8);
    }
    {
      const f32x4 q = gather_quarters(rws, WS_QP + (qc * 64 + qk * 16) * 8, HQD * 8, tag, lane);
      if (lane < 8) st_dev(rws, u32x4{pk2(q[0], q[1]), tag, pk2(q[2], q[3]), tag}, lane * 16, WS_Q + (qc * 64 + qk * 16) * 8);
    }
    if (f1) {
      const f32x4 h = gather_quarters(rws, WS_HP + (hc * 64 + hk * 16) * 8, FF * 8, tag, lane);
      if (lane < 8) st_dev(rws, u32x4{pk2(silu(rbf(h[0])), silu(rbf(h[1]))), tag, pk2(silu(rbf(h[2])), silu(rbf(h[3]))), tag}, lane * 16, WS_H + (hc * 64 + hk * 16) * 8);
    }
    STAMP(3);
    {
      const int b = wg >> 7, kvh = (wg >> 4) & 7;
      u32x4 gq[4];
      for (unsigned spin = 0; spin < SPIN_CAP; ++spin) {
        asm volatile("" ::: "memory");
#pragma unroll
        for (int i = 0; i < 4; ++i) gq[i] = ld_dev(rws, lane * 16 + i * 1024, WS_Q + kvh * 4096);
        if (__all(pair_ok(gq[0], tag) & pair_ok(gq[1], tag) & pair_ok(gq[2], tag) & pair_ok(gq[3], tag))) break;
        __builtin_amdgcn_s_sleep(QSLEEP);
      }
      const unsigned sel = b ? 0x07060302u : 0x05040100u;
#pragma unroll
      for (int i = 0; i < 4; ++i) lds_st<unsigned>(lds0 + L_QI + i * 256 + lane * 4, __builtin_amdgcn_perm(gq[i][2], gq[i][0], sel));
    }
    wait_lds();
    if (lane == 0) *(volatile LAS unsigned*)(size_t)(lds0 + L_QR) = 1u;
    STAMP(4);

    if (f2) {
      u32x4 gh;
      for (unsigned spin = 0; spin < SPIN_CAP; ++spin) {
        asm volatile("" ::: "memory");
        gh = ld_dev(rws, lane * 16, WS_H + gk * 1024);
        if (__all(pair_ok(gh, tag))) break;
        __builtin_amdgcn_s_sleep(32);
      }
      lds_st<unsigned>(lds0 + L_HS + lane * 4, __builtin_amdgcn_perm(gh[2], gh[0], 0x05040100u));
      lds_st<unsigned>(lds0 + L_HS + 256 + lane * 4, __builtin_amdgcn_perm(gh[2], gh[0], 0x07060302u));
      wait_lds();
      const f32x2 y = proj64(lds0 + L_HS, 256, lds0 + L_WF, lane);
      st_quad(rws, y[0], y[1], tag, lane, WS_FG + (gk * DM + gc * 64) * 8);
    }
    STAMP(5);
    if (PACE) {
      sleep_until(th0 + TS2);
      u32x4 ta;
      for (unsigned spin = 0; spin < SPIN_CAP; ++spin) {
        ta = *(volatile LAS u32x4*)(size_t)(lds0 + L_TS);
        if (__all((ta[0] != 0) & (ta[1] != 0) & (ta[2] != 0) & (ta[3] != 0))) break;
        __builtin_amdgcn_s_sleep(8);
      }
      const u32x4 p0 = *(volatile LAS u32x4*)(size_t)(lds0 + L_TP), p1 = *(volatile LAS u32x4*)(size_t)(lds0 + L_TP + 16);
      {
        const unsigned a = th0 + (((ta[0] - th0) + (ta[1] - th0) + (ta[2] - th0) + (ta[3] - th0)) >> 2);
        const unsigned b = th0 + (((p0[1] - th0) + (p0[3] - th0) + (p1[1] - th0) + (p1[3] - th0)) >> 2);
        const unsigned c4 = p0[0] + p0[2] + p1[0] + p1[2];
        if (lane == 0) st_dev(rws, u32x4{a, tag, (b << 8) | c4, tag}, 0, WS_PC + wg * 128);
      }
      sleep_until(now32() + TSD);
      u32x4 gp;
      for (unsigned spin = 0; spin < SPIN_CAP; ++spin) {
        asm volatile("" ::: "memory");
        gp = ld_dev(rws, (lane & (NSAMP - 1)) * 128, WS_PC + (wg & ~(NSAMP - 1)) * 128);
        if (__all(pair_ok(gp, tag))) break;
        __builtin_amdgcn_s_sleep(24);
      }
      const float tb = (float)((int)((gp[2] & 0xffffff00u) - (th0 << 8)) >> 8), ta_ = (float)(int)(gp[0] - th0);
      const float cb = 0.25f * (float)(gp[2] & 0xffu);
      const float r = fmaxf(cb - (float)CPA, 0.25f) * __builtin_amdgcn_rcpf(fmaxf(tb - ta_, 25.f)) * RBOOST;
      const float fin = wave_sum((float)(NSTEP - 3) - cb + r * tb) * __builtin_amdgcn_rcpf(wave_sum(r));
      if (lane == 0) *(volatile LAS unsigned*)(size_t)(lds0 + L_TL) = (th0 + (unsigned)((int)fin - PACE_OFF)) | 1u;
    }
    STAMP(11);
    wg_barrier();
    STAMP(6);
    {
      f32x2 hm[4];
#pragma unroll
      for (int w = 0; w < 4; ++w) hm[w] = lds_ld<f32x2>(lds0 + L_ML + (w * 4 + (lane & 3)) * 8);
      const float M = fmaxf(fmaxf(hm[0][0], hm[1][0]), fmaxf(hm[2][0], hm[3][0]));
      float L = 0.f;
#pragma unroll
      for (int w = 0; w < 4; ++w) L = fmaf(ex2((hm[w][0] - M) * L2E), hm[w][1], L);
      if (lane < 4) st_dev(rws, u32x4{__float_as_uint(M), tag, __float_as_uint(L), tag}, lane * 16, WS_ML + wg * 128);
    }
    u32x4 gs = ld_dev(rws, lane * (DM * 8), WS_P2 + cg * 16);
    float gate;
    {
      u32x4 gf;
      for (unsigned spin = 0; spin < SPIN_CAP; ++spin) {
        asm volatile("" ::: "memory");
        gf = ld_dev(rws, (lane < 14 ? lane : 0) * (DM * 8), WS_FG + cg * 16);
        if (__all(quad_ok(gf, tag))) break;
      }
      if (lane >= 14) gf = u32x4{0u, 0u, 0u, 0u};
      gate = rbf(silu(rbf(quad_sum(quad_val(gf[0]), quad_val(gf[1]), quad_val(gf[2]), quad_val(gf[3])))));
    }
    asm volatile("" : "+v"(gs), "+v"(gate));
    STAMP(7);
    wg_barrier();
    STAMP(8);
    {
      bool have = false;
      for (unsigned spin = 0; spin < SPIN_CAP; ++spin) {
        asm volatile("" ::: "memory");
        if (!have) gs = ld_dev(rws, lane * (DM * 8), WS_P2 + cg * 16);
        have = quad_ok(gs, tag);
        if (__all(have)) break;
      }
      STAMP(9);
      const float s = rbf(quad_sum(quad_val(gs[0]), quad_val(gs[1]), quad_val(gs[2]), quad_val(gs[3])));
      if ((lane & 15) == 0)
        __builtin_amdgcn_raw_buffer_store_b16(__builtin_bit_cast(u16, (__bf16)(bflo(xo) + rbf(s * gate))), make_rsrc(out, 2 * DM * 2), vout, 0, 0);
      if (wg == 0 && lane == 0) __builtin_amdgcn_raw_buffer_store_b32(epoch, rws, 0, WS_EPOCH, SC1);
    }
    STAMP(10);
    STAMP_FLUSH(48, 12);
  }
}

}

extern "C" void kernel_launch(void* const* d_in, const int* in_sizes, int n_in, void* d_out, int out_size, void* d_ws, size_t ws_size,
                              hipStream_t stream) {
  (void)hipFuncSetAttribute((const void*)fused_decode_kernel, hipFuncAttributeMaxDynamicSharedMemorySize, L_END);
  fused_decode_kernel<<<NWG, NTHR, L_END, stream>>>((const u16*)d_in[0], (const u16*)d_in[1], (const u16*)d_in[2], (const u16*)d_in[3],
                                                    (const u16*)d_in[4], (const u16*)d_in[6], (const u16*)d_in[7], (u16*)d_out, (char*)d_ws);
}
```

```cpp
#include <hip/hip_runtime.h>
#include <hip/hip_bf16.h>
#include <type_traits>

namespace {

typedef unsigned short u16;
typedef short s16x4 __attribute__((ext_vector_type(4)));
typedef __bf16 bf16x8 __attribute__((ext_vector_type(8)));
typedef __bf16 bf16x2 __attribute__((ext_vector_type(2)));
typedef float f32x2 __attribute__((ext_vector_type(2)));
typedef float f32x4 __attribute__((ext_vector_type(4)));
typedef unsigned u32x2 __attribute__((ext_vector_type(2)));
typedef unsigned u32x4 __attribute__((ext_vector_type(4)));
typedef __amdgpu_buffer_rsrc_t rsrc_t;
#define LAS __attribute__((address_space(3)))

constexpr int NWG = 256, NTHR = 320;
constexpr int DM = 512, HQD = 4096, SK = 32768, DH = 128, FF = 1792;
constexpr int NSLOT = 6, AHEAD = NSLOT - 1, NTILE = 32, NUNIT = 2 * NTILE, NPRE = 11;
constexpr int WIN = 4, WIN2 = 8;
constexpr int CODE_KB = 31;
constexpr int CPA = 13, TS2 = 1500, TSD = 100, PACE_OFF = 65, OVER = 4, NSAMP = 32, POLL_AT = 170, ML_DELAY = 200;
constexpr float RBOOST = 0.99f, DELTA = 55.f;
constexpr int KV_BYTES = 2 * 8 * SK * DH * 2;
constexpr float SCALE = 0.08838834764831845f, L2E = 1.4426950408889634f;
constexpr float M_INIT = -3.0e38f;
constexpr unsigned SPIN_CAP = 1u << 18;
constexpr unsigned OOB = 0x80000000u;

constexpr int WS_EPOCH = 0;
constexpr int WS_Q = 4096;
constexpr int WS_H = WS_Q + HQD * 8;
constexpr int WS_ML = 65536;
constexpr int WS_PC = WS_ML + NWG * 128;
constexpr int WS_QP = WS_PC + NWG * 128;
constexpr int WS_HP = WS_QP + 4 * HQD * 8;
constexpr int WS_FG = WS_HP + 4 * FF * 8;
constexpr int WS_P2 = WS_FG + 14 * DM * 8;
constexpr int WS_P1 = WS_P2 + 64 * DM * 8;
constexpr int WS_END = WS_P1 + NWG * 2048;
static_assert(WS_H + FF * 8 <= WS_ML, "workspace map");

constexpr int L_RING = 0;
constexpr int L_WQ = 4 * NSLOT * 4096;
constexpr int L_SC = L_WQ;
constexpr int L_WF = L_WQ + 16384;
constexpr int L_WO = L_WF + 16384;
constexpr int L_XN = L_WO + 16384;
constexpr int L_QI = L_XN + 2048;
constexpr int L_OS = L_QI + 1024;
constexpr int L_HS = L_OS + 256;
constexpr int L_ML = L_HS + 512;
constexpr int L_TAG = L_ML + 128;
constexpr int L_QR = L_TAG + 4;
constexpr int L_TL = L_TAG + 16;
constexpr int L_TS = L_TL + 16;
constexpr int L_TP = L_TS + 16;
constexpr int L_MR = L_TP + 32;
constexpr int L_GR = L_MR + 16;
constexpr int L_GS = L_GR + 16;
constexpr int L_END = L_GS + 32;

__device__ __forceinline__ float rbf(float f) { return (float)(__bf16)f; }
__device__ __forceinline__ unsigned pk2(float lo, float hi) {
  bf16x2 v = {(__bf16)lo, (__bf16)hi};
  return __builtin_bit_cast(unsigned, v);
}
__device__ __forceinline__ float bflo(unsigned u) { return __uint_as_float(u << 16); }
__device__ __forceinline__ float bfhi(unsigned u) { return __uint_as_float(u & 0xffff0000u); }
template <int CTRL> __device__ __forceinline__ float dpp(float x) {
  return __builtin_bit_cast(float, __builtin_amdgcn_mov_dpp(__builtin_bit_cast(int, x), CTRL, 0xf, 0xf, true));
}
constexpr int XOR1 = 0xB1, XOR2 = 0x4E, ROR4 = 0x124, ROR8 = 0x128;
__device__ __forceinline__ float swap16_sum(float a, float b) {
  auto s = __builtin_amdgcn_permlane16_swap(__float_as_uint(a), __float_as_uint(b), false, false);
  return __uint_as_float(s[0]) + __uint_as_float(s[1]);
}
__device__ __forceinline__ float swap32_sum(float a, float b) {
  auto s = __builtin_amdgcn_permlane32_swap(__float_as_uint(a), __float_as_uint(b), false, false);
  return __uint_as_float(s[0]) + __uint_as_float(s[1]);
}
__device__ __forceinline__ float rows_sum(float x) { x = swap16_sum(x, x); return swap32_sum(x, x); }
__device__ __forceinline__ float rows_max(float x) {
  auto s = __builtin_amdgcn_permlane16_swap(__float_as_uint(x), __float_as_uint(x), false, false);
  x = fmaxf(__uint_as_float(s[0]), __uint_as_float(s[1]));
  auto t = __builtin_amdgcn_permlane32_swap(__float_as_uint(x), __float_as_uint(x), false, false);
  return fmaxf(__uint_as_float(t[0]), __uint_as_float(t[1]));
}
__device__ __forceinline__ float quads_sum(float x) { x += dpp<ROR4>(x); x += dpp<ROR8>(x); return rows_sum(x); }
__device__ __forceinline__ float quads_max(float x) { x = fmaxf(x, dpp<ROR4>(x)); x = fmaxf(x, dpp<ROR8>(x)); return rows_max(x); }
__device__ __forceinline__ float wave_sum(float x) { x += dpp<XOR1>(x); x += dpp<XOR2>(x); return quads_sum(x); }
__device__ __forceinline__ float ex2(float x) { return __builtin_amdgcn_exp2f(x); }
__device__ __forceinline__ float silu(float v) { return v * __builtin_amdgcn_rcpf(1.f + ex2(-v * L2E)); }

template <int N> __device__ __forceinline__ void wait_vm() {
  __builtin_amdgcn_sched_barrier(0);
  asm volatile("s_waitcnt vmcnt(%0)" ::"i"(N) : "memory");
}
__device__ __forceinline__ void wait_lds() { asm volatile("s_waitcnt lgkmcnt(0)" ::: "memory"); }
__device__ __forceinline__ void wg_barrier() {
  asm volatile("s_waitcnt lgkmcnt(0)\n\ts_barrier" ::: "memory");
}
__device__ __forceinline__ void lds_wait_nonzero(unsigned addr) {
  for (unsigned spin = 0; spin < SPIN_CAP * 16 && *(volatile LAS unsigned*)(size_t)addr == 0; ++spin) __builtin_amdgcn_s_sleep(1);
  asm volatile("" ::: "memory");
}
__device__ __forceinline__ unsigned now32() { return (unsigned)__builtin_amdgcn_s_memrealtime(); }
__device__ __forceinline__ void sleep_until(unsigned due) { while ((int)(now32() - due) < 0) __builtin_amdgcn_s_sleep(2); }
__device__ __forceinline__ rsrc_t make_rsrc(const void* p, int bytes) {
  return __builtin_amdgcn_make_buffer_rsrc((void*)p, (short)0, bytes, 0x00020000);
}
constexpr int SC1 = 16;
__device__ __forceinline__ u32x4 ld_dev(rsrc_t r, unsigned voff, unsigned soff) { return __builtin_amdgcn_raw_buffer_load_b128(r, voff, soff, SC1); }
__device__ __forceinline__ void st_dev(rsrc_t r, u32x4 v, unsigned voff, unsigned soff) { __builtin_amdgcn_raw_buffer_store_b128(v, r, voff, soff, SC1); }
__device__ __forceinline__ bool pair_ok(const u32x4& g, unsigned tag) { return (g[1] == tag) & (g[3] == tag); }
__device__ __forceinline__ bool quad_ok(const u32x4& g, unsigned tag) {
  return (__builtin_amdgcn_perm(g[1], g[0], 0x0c0c0400u) | __builtin_amdgcn_perm(g[3], g[2], 0x04000c0cu)) == tag;
}
__device__ __forceinline__ float quad_val(unsigned w) { return __uint_as_float(w & 0xffffff00u); }
struct QuadOut {
  unsigned t0, t1, t2, t3, voff;
  __device__ __forceinline__ QuadOut(unsigned tag, int lane) : t0(tag & 255u), t1((tag >> 8) & 255u), t2((tag >> 16) & 255u), t3(tag >> 24), voff(lane & 1 ? OOB : lane * 8) {
    asm volatile("" : "+v"(t0), "+v"(t1), "+v"(t2), "+v"(t3), "+v"(voff));
  }
  __device__ __forceinline__ void store(rsrc_t r, float a, float b, unsigned soff) const {
    const float c = dpp<XOR1>(a), d = dpp<XOR1>(b);
    st_dev(r, u32x4{(__float_as_uint(a) & 0xffffff00u) | t0, (__float_as_uint(b) & 0xffffff00u) | t1, (__float_as_uint(c) & 0xffffff00u) | t2,
                    (__float_as_uint(d) & 0xffffff00u) | t3}, voff, soff);
  }
};
__device__ __forceinline__ float quad_sum(float v0, float v1, float v2, float v3) {
  float x = swap16_sum(swap32_sum(v0, v2), swap32_sum(v1, v3));
  x += dpp<ROR8>(x); x += dpp<ROR4>(x); x += dpp<XOR2>(x); x += dpp<XOR1>(x);
  return x;
}
__device__ __forceinline__ s16x4 tr_read(unsigned addr) { return __builtin_amdgcn_ds_read_tr16_b64_v4i16((LAS s16x4*)(size_t)addr); }
template <typename T> __device__ __forceinline__ T lds_ld(unsigned addr) { return *(const LAS T*)(size_t)addr; }
template <typename T> __device__ __forceinline__ void lds_st(unsigned addr, T v) { *(LAS T*)(size_t)addr = v; }

__device__ __forceinline__ void dma_piece(rsrc_t r, unsigned voff, unsigned soff, unsigned dst) {
  asm volatile("s_mov_b32 m0, %0\n\ts_nop 0\n\tbuffer_load_dwordx4 %1, %2, %3 offen nt lds" ::"s"(dst), "v"(voff), "s"(r), "s"(soff) : "memory");
}
__device__ __forceinline__ void dma_half(rsrc_t r, unsigned v0, unsigned v1, unsigned v2, unsigned v3, unsigned soff, unsigned dst) {
  asm volatile(
      "s_mov_b32 m0, %0\n\ts_nop 0\n\t"
      "buffer_load_dwordx4 %1, %5, %6 offen nt lds\n\t"
      "buffer_load_dwordx4 %2, %5, %6 offen offset:1024 nt lds\n\t"
      "buffer_load_dwordx4 %3, %5, %6 offen offset:2048 nt lds\n\t"
      "buffer_load_dwordx4 %4, %5, %6 offen offset:3072 nt lds"
      ::"s"(dst), "v"(v0), "v"(v1), "v"(v2), "v"(v3), "s"(r), "s"(soff)
      : "memory");
}
template <int A> __device__ __forceinline__ f32x4 mfma4(s16x4 a, s16x4 b, f32x4 c) { return __builtin_amdgcn_mfma_f32_4x4x4bf16_1k(a, b, c, 4, A, 0); }
template <int A, int N> struct Proj {
  static __device__ __forceinline__ void run(const s16x4 (&av)[2], unsigned ba, f32x4 (&c)[4]) {
    c[A & 3] = mfma4<A & 15>(av[A >> 4], tr_read(ba + A * 512), c[A & 3]);
    if constexpr (A + 1 < N) Proj<A + 1, N>::run(av, ba, c);
  }
};
template <int A> struct ProjReg {
  static __device__ __forceinline__ void run(s16x4 a, const s16x4 (&w)[16], f32x4 (&c)[2]) {
    c[A & 1] = mfma4<A>(a, w[A], c[A & 1]);
    if constexpr (A + 1 < 16) ProjReg<A + 1>::run(a, w, c);
  }
};
__device__ __forceinline__ f32x2 proj64(unsigned a_img, int a_stride, unsigned w_img, int lane) {
  const int q = (lane & 15) >> 2, p = lane & 3;
  const unsigned aa = a_img + (lane & 1) * a_stride + (lane >> 2) * 8;
  const s16x4 av[2] = {lds_ld<s16x4>(aa), lds_ld<s16x4>(aa + 128)};
  const unsigned ba = w_img + q * 128 + (((2 * (lane >> 4) + (p >> 1)) ^ ((q >> 1) << 2)) << 4) + (p & 1) * 8;
  f32x4 c[4] = {};
  Proj<0, 32>::run(av, ba, c);
  return f32x2{(c[0][0] + c[1][0]) + (c[2][0] + c[3][0]), (c[0][1] + c[1][1]) + (c[2][1] + c[3][1])};
}
__device__ __forceinline__ f32x4 gather_quarters(rsrc_t rws, unsigned base, int quarter_bytes, unsigned tag, int lane) {
  u32x4 g;
  for (unsigned spin = 0; spin < SPIN_CAP; ++spin) {
    asm volatile("" ::: "memory");
    g = ld_dev(rws, (lane >> 4) * quarter_bytes + (lane & 7) * 16, base);
    if (__all(quad_ok(g, tag))) break;
    __builtin_amdgcn_s_sleep(8);
  }
  return f32x4{rows_sum(quad_val(g[0])), rows_sum(quad_val(g[1])), rows_sum(quad_val(g[2])), rows_sum(quad_val(g[3]))};
}

template <int N> using Int = std::integral_constant<int, N>;
struct KFrag { bf16x8 k[4]; };
struct VFrag { s16x4 v[4][2]; };

__device__ __forceinline__ void read_k(KFrag& t, const unsigned (&ka)[4], unsigned off) {
#pragma unroll
  for (int s = 0; s < 4; ++s) t.k[s] = lds_ld<bf16x8>(ka[s] + off);
  asm volatile("s_waitcnt lgkmcnt(0)" : "+v"(t.k[0]), "+v"(t.k[1]), "+v"(t.k[2]), "+v"(t.k[3])::"memory");
}
__device__ __forceinline__ void read_v(VFrag& t, const unsigned (&va)[2], unsigned off) {
#pragma unroll
  for (int a = 0; a < 4; ++a)
#pragma unroll
    for (int h = 0; h < 2; ++h) t.v[a][h] = tr_read(va[h] + off + a * 1024);
  asm volatile("s_waitcnt lgkmcnt(0)"
               : "+v"(t.v[0][0]), "+v"(t.v[0][1]), "+v"(t.v[1][0]), "+v"(t.v[1][1]), "+v"(t.v[2][0]), "+v"(t.v[2][1]), "+v"(t.v[3][0]), "+v"(t.v[3][1])
               :
               : "memory");
}
__device__ __forceinline__ void score_tile(const KFrag& t, const bf16x8 (&qf)[4], float& m, unsigned dst, bool writer) {
  f32x4 sc = {0.f, 0.f, 0.f, 0.f};
#pragma unroll
  for (int s = 0; s < 4; ++s) sc = __builtin_amdgcn_mfma_f32_16x16x32_bf16(t.k[s], qf[s], sc, 0, 0, 0);
  m = fmaxf(fmaxf(m, fmaxf(sc[0], sc[1])), fmaxf(sc[2], sc[3]));
  const unsigned r0 = pk2(sc[0], sc[1]), r1 = pk2(sc[2], sc[3]);
  if (writer) lds_st<u32x2>(dst, u32x2{pk2(bflo(r0) * SCALE, bfhi(r0) * SCALE), pk2(bflo(r1) * SCALE, bfhi(r1) * SCALE)});
}
__device__ __forceinline__ s16x4 probs(unsigned src, float M, float f) {
  const u32x2 xp = lds_ld<u32x2>(src);
  const u32x2 pp = {pk2(f * ex2((bflo(xp[0]) - M) * L2E), f * ex2((bfhi(xp[0]) - M) * L2E)), pk2(f * ex2((bflo(xp[1]) - M) * L2E), f * ex2((bfhi(xp[1]) - M) * L2E))};
  return __builtin_bit_cast(s16x4, pp);
}
__device__ __forceinline__ void value_tile(const VFrag& t, s16x4 pa, f32x4 (&o)[2]) {
#pragma unroll
  for (int h = 0; h < 2; ++h) o[h] = mfma4<0>(pa, t.v[0][h], o[h]);
#pragma unroll
  for (int h = 0; h < 2; ++h) o[h] = mfma4<4>(pa, t.v[1][h], o[h]);
#pragma unroll
  for (int h = 0; h < 2; ++h) o[h] = mfma4<8>(pa, t.v[2][h], o[h]);
#pragma unroll
  for (int h = 0; h < 2; ++h) o[h] = mfma4<12>(pa, t.v[3][h], o[h]);
}

__global__ void __launch_bounds__(NTHR, 2)
fused_decode_kernel(const u16* __restrict__ x, const u16* __restrict__ kc, const u16* __restrict__ vc, const u16* __restrict__ wq,
                    const u16* __restrict__ wo, const u16* __restrict__ w1, const u16* __restrict__ w2, u16* __restrict__ out, char* ws) {
  unsigned long long pc;
  asm volatile("s_getpc_b64 %0" : "=s"(pc));
  extern __shared__ __attribute__((aligned(16))) unsigned char lds[];
  const unsigned lds0 = (unsigned)(size_t)(LAS unsigned char*)lds;
  const int lane = threadIdx.x & 63, wave = __builtin_amdgcn_readfirstlane(threadIdx.x >> 6), wg = blockIdx.x;
  const int xg = wg & 7, xi = wg >> 3;
  asm volatile("" ::"s"(x), "s"(kc), "s"(vc), "s"(wq), "s"(wo), "s"(w1), "s"(w2), "s"(out), "s"(ws));
  const rsrc_t rws = make_rsrc(ws, WS_END);
  const unsigned sw128 = ((lane & 7) ^ (((lane >> 4) & 1) << 2)) << 4;

  if (wave < 4) {
    const rsrc_t rk = make_rsrc(kc, KV_BYTES), rv = make_rsrc(vc, KV_BYTES);
    const int g = lane >> 4, c = lane & 15;
    unsigned vk[4];
#pragma unroll
    for (int u = 0; u < 4; ++u) vk[u] = g * 256 + ((c ^ (4 * u + g)) << 4);
    const unsigned vv = g * 256 + ((c ^ (g << 2)) << 4);
    const unsigned ring = lds0 + L_RING + wave * (NSLOT * 4096);
    const unsigned tile0 = (unsigned)wg * (2048 * 256) + wave * 4096;
    unsigned ka[4], va[2];
#pragma unroll
    for (int s = 0; s < 4; ++s) ka[s] = ring + c * 256 + (((4 * s + g) ^ c) << 4);
#pragma unroll
    for (int h = 0; h < 2; ++h) va[h] = ring + (c >> 2) * 256 + (((8 * h + 2 * g + ((c & 3) >> 1)) ^ ((c >> 2) << 2)) << 4) + (c & 1) * 8;
    const unsigned sca = lds0 + L_SC + wave * 4096 + (g * 4 + (c & 3)) * 8;
    auto issue_k = [&](int t, unsigned off) { dma_half(rk, vk[0], vk[1], vk[2], vk[3], tile0 + t * 16384, ring + off); };
    auto issue_v = [&](int t, unsigned off) { dma_half(rv, vv, vv, vv, vv, tile0 + t * 16384, ring + off); };

    wg_barrier();
#pragma unroll
    for (int u = 0; u < AHEAD; ++u) { wait_vm<WIN>(); issue_k(u, u * 4096); }
    KFrag pk[NPRE];
#pragma unroll
    for (int u = 0; u < NPRE; ++u) {
      wait_vm<WIN>();
      issue_k(u + AHEAD, ((u + AHEAD) % NSLOT) * 4096);
      read_k(pk[u], ka, (u % NSLOT) * 4096);
    }
    lds_wait_nonzero(lds0 + L_QR);
    bf16x8 qf[4];
#pragma unroll
    for (int s = 0; s < 4; ++s) qf[s] = lds_ld<bf16x8>(lds0 + L_QI + (c & 3) * 256 + (4 * s + g) * 16);
    const unsigned tag = lds_ld<unsigned>(lds0 + L_TAG);

    unsigned prev = now32();
    auto landed = [&](int u, auto win) {
      const u32x2 plan = *(volatile LAS u32x2*)(size_t)(lds0 + L_TL);
      wait_vm<win.value>();
      unsigned tn = now32();
      *(volatile LAS u32x2*)(size_t)(lds0 + L_TP + wave * 8) = u32x2{(unsigned)u, tn};
      if (u == CPA) *(volatile LAS unsigned*)(size_t)(lds0 + L_TS + wave * 4) = tn | 1u;
      const unsigned tl = __builtin_amdgcn_readfirstlane(plan[0]);
      const int d = (int)(tl - prev);
      if (tl != 0 && d > 0) {
        const float n = (float)(NUNIT - AHEAD - u);
        const bool behind = (float)d + DELTA < n * __uint_as_float(plan[1]);
        const unsigned due = prev + (unsigned)__builtin_amdgcn_readfirstlane(behind ? 0 : (int)((float)d * __builtin_amdgcn_rcpf(n)));
        while ((int)(tn - due) < 0) { __builtin_amdgcn_s_sleep(1); tn = now32(); }
      }
      prev = tn + OVER;
    };
    static_assert(WIN <= 16 && WIN2 <= 16 && 2 * NPRE <= NTILE - AHEAD && CPA >= NPRE, "unit schedule");
    float m = M_INIT;
    KFrag kf;
#pragma unroll
    for (int u = NPRE; u < 2 * NPRE; ++u) {
      landed(u, Int<WIN>{}); issue_k(u + AHEAD, ((u + AHEAD) % NSLOT) * 4096);
      read_k(kf, ka, (u % NSLOT) * 4096);
      score_tile(kf, qf, m, sca + u * 128, c < 4);
      score_tile(pk[u - NPRE], qf, m, sca + (u - NPRE) * 128, c < 4);
    }
    unsigned off = ((2 * NPRE) % NSLOT) * 4096, offp = ((2 * NPRE + AHEAD) % NSLOT) * 4096;
    auto next_slot = [&]() { offp = off; off = off == (NSLOT - 1) * 4096 ? 0u : off + 4096; };
#pragma unroll 1
    for (int u = 2 * NPRE; u < NTILE - AHEAD; ++u) {
      landed(u, Int<WIN>{}); issue_k(u + AHEAD, offp);
      read_k(kf, ka, off);
      score_tile(kf, qf, m, sca + u * 128, c < 4);
      next_slot();
    }
#pragma unroll 1
    for (int u = NTILE - AHEAD; u < NTILE; ++u) {
      landed(u, Int<WIN2>{}); issue_v(u + AHEAD - NTILE, offp);
      read_k(kf, ka, off);
      score_tile(kf, qf, m, sca + u * 128, c < 4);
      next_slot();
    }
    const float M = rbf(rbf(rows_max(m)) * SCALE);
    if (lane < 4) lds_st<float>(lds0 + L_ML + lane * 16 + wave * 4, M);
    wait_lds();
    if (lane == 0) *(volatile LAS unsigned*)(size_t)(lds0 + L_MR + wave * 4) = 1u;
    f32x4 o[2] = {};
    VFrag vf;
#pragma unroll 1
    for (int u = NTILE; u < NUNIT - AHEAD; ++u) {
      landed(u, Int<WIN2>{}); issue_v(u + AHEAD - NTILE, offp);
      const s16x4 pa = probs(sca + (u - NTILE) * 128, M, 1.f);
      read_v(vf, va, off);
      value_tile(vf, pa, o);
      next_slot();
    }
    const int ks = xg * 8 + (xi >> 2), cs = xi & 3;
    if (wave < 2) {
      const rsrc_t ro = make_rsrc(wo, HQD * DM * 2);
#pragma unroll
      for (int i = 0; i < 8; ++i)
        dma_piece(ro, (lane >> 3) * (DM * 2) + sw128, (ks * 64 + 8 * i) * (DM * 2) + cs * 256 + wave * 128, lds0 + L_WO + wave * 8192 + i * 1024);
    }
    lds_wait_nonzero(lds0 + L_GR);
    const f32x2 gs = lds_ld<f32x2>(lds0 + L_GS + (c & 3) * 8);
    const float f = ex2((M - gs[0]) * L2E) * gs[1];
    {
      const float f0 = dpp<0x00>(f), f1 = dpp<0x55>(f), f2 = dpp<0xAA>(f), f3 = dpp<0xFF>(f);
#pragma unroll
      for (int h = 0; h < 2; ++h) { o[h][0] *= f0; o[h][1] *= f1; o[h][2] *= f2; o[h][3] *= f3; }
    }
    const QuadOut qo(tag, lane);
    unsigned s_p1 = WS_P1 + wg * 2048 + wave * 512, s_p2 = WS_P2 + ks * (DM * 8) + (cs * 128 + wave * 64) * 8;
    unsigned a_mw = ring + lane * 4, a_mr = lds0 + L_RING + (wave * 128 + 2 * lane) * 4;
    unsigned a_ow = lds0 + L_OS + (wave >> 1) * 128 + ((wave & 1) * 32 + 4 * (lane & 7) + g) * 2, a_or = lds0 + L_OS + (lane & 1) * 128 + (lane >> 2) * 8;
    asm volatile("" : "+s"(s_p1), "+s"(s_p2), "+v"(a_mw), "+v"(a_mr), "+v"(a_ow), "+v"(a_or));
    auto last = [&](auto j) {
      constexpr int u = NUNIT - AHEAD + j.value, younger = (NUNIT - 1 - u) * 4;
      s16x4 pa = probs(sca + (u - NTILE) * 128, M, f);
      asm volatile("" : "+v"(pa));
      if (wave < 2) wait_vm<younger + 8>(); else wait_vm<younger>();
      read_v(vf, va, (u % NSLOT) * 4096);
      value_tile(vf, pa, o);
    };
    last(Int<0>{}); last(Int<1>{}); last(Int<2>{}); last(Int<3>{}); last(Int<4>{});

#pragma unroll
    for (int h = 0; h < 2; ++h)
#pragma unroll
      for (int r = 0; r < 4; ++r) lds_st<float>(a_mw + (r * 128 + h * 64) * 4, o[h][r]);
    wg_barrier();
    {
      f32x2 ov[4];
#pragma unroll
      for (int w = 0; w < 4; ++w) ov[w] = lds_ld<f32x2>(a_mr + w * (NSLOT * 4096));
      const f32x2 sum = (ov[0] + ov[1]) + (ov[2] + ov[3]);
      qo.store(rws, sum[0], sum[1], s_p1);
    }
    s16x4 wf[16];
    if (wave < 2) {
      const unsigned wa = lds0 + L_WO + wave * 8192 + (c >> 2) * 128 + (((2 * g + ((c & 3) >> 1)) ^ ((c >> 3) << 2)) << 4) + (c & 1) * 8;
      wait_vm<1>();
#pragma unroll
      for (int a = 0; a < 16; ++a) wf[a] = tr_read(wa + a * 512);
    }
    {
      const int b = wave >> 1, half = wave & 1;
      const unsigned vo = (lane >> 3) * 2048 + (lane & 7) * 16;
      const unsigned so = WS_P1 + (b * 8 + xg) * 16 * 2048 + ((ks >> 1) & 3) * 512 + ((ks & 1) * 2 + half) * 128;
      sleep_until(__builtin_amdgcn_readfirstlane(*(volatile LAS unsigned*)(size_t)(lds0 + L_TL)) + POLL_AT);
      u32x4 gv[2] = {};
      bool h0 = false, h1 = false;
      for (unsigned spin = 0; spin < SPIN_CAP; ++spin) {
        asm volatile("" ::: "memory");
        if (!h0) gv[0] = ld_dev(rws, vo, so);
        if (!h1) gv[1] = ld_dev(rws, vo + 8 * 2048, so);
        h0 = quad_ok(gv[0], tag); h1 = quad_ok(gv[1], tag);
        if (__all(h0 & h1)) break;
      }
      float u[4];
#pragma unroll
      for (int j = 0; j < 4; ++j) u[j] = quad_val(gv[0][j]) + quad_val(gv[1][j]);
      float e = swap16_sum(swap32_sum(u[0], u[2]), swap32_sum(u[1], u[3]));
      e += dpp<ROR8>(e);
      lds_st<__bf16>(a_ow, (__bf16)e);
    }
    wg_barrier();
    if (wave < 2) {
      const s16x4 oa = lds_ld<s16x4>(a_or);
      f32x4 acc[2] = {};
      ProjReg<0>::run(oa, wf, acc);
      qo.store(rws, acc[0][0] + acc[1][0], acc[0][1] + acc[1][1], s_p2);
    }
  } else {
    __builtin_amdgcn_s_setprio(3);
    const int cg = xg * 32 + xi;
    const int qk = cg >> 6, qc = cg & 63;
    const bool f1 = xi < 14, f2 = xi >= 14 && xi < 28;
    const int fi = xg * 14 + (xi < 14 ? xi : xi - 14);
    const int hk = fi / 28, hc = fi % 28;
    const int gk = fi >> 3, gc = fi & 7;
    const unsigned vout = (((lane >> 4) & 1) * DM + cg * 2 + (lane >> 5)) * 2;
    const rsrc_t rx = make_rsrc(x, 2 * DM * 2);
    unsigned ev, xo, tv, to;
    u32x4 x0, x1, code;
    asm volatile(
        "s_nop 4\n\t"
        "buffer_load_dwordx4 %0, %7, %8, 0 offen\n\t"
        "buffer_load_dwordx4 %1, %7, %8, 0 offen offset:1024\n\t"
        "buffer_load_dword %2, off, %9, 0 sc1\n\t"
        "buffer_load_ushort %3, %10, %8, 0 offen\n\t"
        "buffer_load_dwordx4 %4, %7, %11, %12 offen\n\t"
        "buffer_load_dword %5, off, %13, %14 nt\n\t"
        "buffer_load_dword %6, off, %15, %16 nt"
        : "=&v"(x0), "=&v"(x1), "=&v"(ev), "=&v"(xo), "=&v"(code), "=&v"(tv), "=&v"(to)
        : "v"(lane * 16), "s"(rx), "s"(rws), "v"(vout), "s"(make_rsrc((const void*)(pc & ~255ull), CODE_KB * 1024)), "s"(xi * 1024),
          "s"(make_rsrc(vc, KV_BYTES)), "s"(wg * (2048 * 256)), "s"(make_rsrc(wo, HQD * DM * 2)), "s"((xg * 8 + (xi >> 2)) * 64 * (DM * 2))
        : "memory");
    {
      const rsrc_t rq = make_rsrc(wq, DM * HQD * 2);
#pragma unroll
      for (int i = 0; i < 16; ++i) dma_piece(rq, (lane >> 3) * (HQD * 2) + sw128, (qk * 128 + 8 * i) * (HQD * 2) + qc * 128, lds0 + L_WQ + i * 1024);
      __builtin_amdgcn_sched_barrier(0);
      const rsrc_t rf = make_rsrc(f1 ? w1 : w2, f1 || f2 ? DM * FF * 2 : 0);
      const int ld = f1 ? FF * 2 : DM * 2;
      const unsigned sf = f1 ? hk * 128 * (FF * 2) + hc * 128 : gk * 128 * (DM * 2) + gc * 128;
#pragma unroll
      for (int i = 0; i < 16; ++i) dma_piece(rf, (lane >> 3) * ld + sw128, sf + 8 * i * ld, lds0 + L_WF + i * 1024);
    }
    if (lane < 28) lds_st<unsigned>(lds0 + L_TAG + lane * 4, 0u);
    wg_barrier();
    const unsigned th0 = now32();
    asm volatile("s_waitcnt vmcnt(32)" : "+v"(x0), "+v"(x1), "+v"(ev), "+v"(xo), "+v"(code), "+v"(tv), "+v"(to)::"memory");
    const unsigned epoch = max(__builtin_amdgcn_readfirstlane(ev) + 1u, 1u), tag = epoch * 0x9E3779B1u;
    {
      float a[2][8];
#pragma unroll
      for (int j = 0; j < 4; ++j) { a[0][2 * j] = bflo(x0[j]); a[0][2 * j + 1] = bfhi(x0[j]); a[1][2 * j] = bflo(x1[j]); a[1][2 * j + 1] = bfhi(x1[j]); }
#pragma unroll
      for (int b = 0; b < 2; ++b) {
        const float mean = wave_sum(((a[b][0] + a[b][1]) + (a[b][2] + a[b][3])) + ((a[b][4] + a[b][5]) + (a[b][6] + a[b][7]))) * (1.f / DM);
        float q = 0.f;
#pragma unroll
        for (int j = 0; j < 8; ++j) q = fmaf(a[b][j] - mean, a[b][j] - mean, q);
        const float var = wave_sum(q) * (1.f / DM);
        const float mb = rbf(mean), inv = __builtin_amdgcn_rcpf(rbf(__builtin_amdgcn_sqrtf(rbf(rbf(var) + 1e-5f))));
        u32x4 xn;
#pragma unroll
        for (int j = 0; j < 4; ++j) xn[j] = pk2(rbf(a[b][2 * j] - mb) * inv, rbf(a[b][2 * j + 1] - mb) * inv);
        lds_st<u32x4>(lds0 + L_XN + b * 1024 + lane * 16, xn);
      }
    }
    if (lane == 0) lds_st<unsigned>(lds0 + L_TAG, tag);
    const QuadOut qo(tag, lane);
    asm volatile("s_waitcnt vmcnt(16) lgkmcnt(0)" ::: "memory");
    {
      const f32x2 y = proj64(lds0 + L_XN + qk * 256, 1024, lds0 + L_WQ, lane);
      qo.store(rws, y[0], y[1], WS_QP + (qk * HQD + qc * 64) * 8);
    }
    if (f1) {
      wait_vm<1>();
      const f32x2 y = proj64(lds0 + L_XN + hk * 256, 1024, lds0 + L_WF, lane);
      qo.store(rws, y[0], y[1], WS_HP + (hk * FF + hc * 64) * 8);
    }
    {
      const f32x4 q = gather_quarters(rws, WS_QP + (qc * 64 + qk * 16) * 8, HQD * 8, tag, lane);
      if (lane < 8) st_dev(rws, u32x4{pk2(q[0], q[1]), tag, pk2(q[2], q[3]), tag}, lane * 16, WS_Q + (qc * 64 + qk * 16) * 8);
    }
    if (f1) {
      const f32x4 h = gather_quarters(rws, WS_HP + (hc * 64 + hk * 16) * 8, FF * 8, tag, lane);
      if (lane < 8) st_dev(rws, u32x4{pk2(silu(rbf(h[0])), silu(rbf(h[1]))), tag, pk2(silu(rbf(h[2])), silu(rbf(h[3]))), tag}, lane * 16, WS_H + (hc * 64 + hk * 16) * 8);
    }
    {
      const int b = wg >> 7, kvh = (wg >> 4) & 7;
      u32x4 gq[4];
      for (unsigned spin = 0; spin < SPIN_CAP; ++spin) {
        asm volatile("" ::: "memory");
#pragma unroll
        for (int i = 0; i < 4; ++i) gq[i] = ld_dev(rws, lane * 16 + i * 1024, WS_Q + kvh * 4096);
        if (__all(pair_ok(gq[0], tag) & pair_ok(gq[1], tag) & pair_ok(gq[2], tag) & pair_ok(gq[3], tag))) break;
        __builtin_amdgcn_s_sleep(8);
      }
      const unsigned sel = b ? 0x07060302u : 0x05040100u;
#pragma unroll
      for (int i = 0; i < 4; ++i) lds_st<unsigned>(lds0 + L_QI + i * 256 + lane * 4, __builtin_amdgcn_perm(gq[i][2], gq[i][0], sel));
    }
    wait_lds();
    if (lane == 0) *(volatile LAS unsigned*)(size_t)(lds0 + L_QR) = 1u;

    if (f2) {
      u32x4 gh;
      for (unsigned spin = 0; spin < SPIN_CAP; ++spin) {
        asm volatile("" ::: "memory");
        gh = ld_dev(rws, lane * 16, WS_H + gk * 1024);
        if (__all(pair_ok(gh, tag))) break;
        __builtin_amdgcn_s_sleep(32);
      }
      lds_st<unsigned>(lds0 + L_HS + lane * 4, __builtin_amdgcn_perm(gh[2], gh[0], 0x05040100u));
      lds_st<unsigned>(lds0 + L_HS + 256 + lane * 4, __builtin_amdgcn_perm(gh[2], gh[0], 0x07060302u));
      wait_lds();
      const f32x2 y = proj64(lds0 + L_HS, 256, lds0 + L_WF, lane);
      qo.store(rws, y[0], y[1], WS_FG + (gk * DM + gc * 64) * 8);
    }
    {
      sleep_until(th0 + TS2);
      u32x4 ta;
      for (unsigned spin = 0; spin < SPIN_CAP; ++spin) {
        ta = *(volatile LAS u32x4*)(size_t)(lds0 + L_TS);
        if (__all((ta[0] != 0) & (ta[1] != 0) & (ta[2] != 0) & (ta[3] != 0))) break;
        __builtin_amdgcn_s_sleep(8);
      }
      const u32x4 p0 = *(volatile LAS u32x4*)(size_t)(lds0 + L_TP), p1 = *(volatile LAS u32x4*)(size_t)(lds0 + L_TP + 16);
      {
        const unsigned a = th0 + (((ta[0] - th0) + (ta[1] - th0) + (ta[2] - th0) + (ta[3] - th0)) >> 2);
        const unsigned b = th0 + (((p0[1] - th0) + (p0[3] - th0) + (p1[1] - th0) + (p1[3] - th0)) >> 2);
        const unsigned c4 = p0[0] + p0[2] + p1[0] + p1[2];
        if (lane == 0) st_dev(rws, u32x4{a, tag, (b << 8) | c4, tag}, 0, WS_PC + wg * 128);
      }
      sleep_until(now32() + TSD);
      u32x4 gp;
      for (unsigned spin = 0; spin < SPIN_CAP; ++spin) {
        asm volatile("" ::: "memory");
        gp = ld_dev(rws, (lane & (NSAMP - 1)) * 128, WS_PC + (wg & ~(NSAMP - 1)) * 128);
        if (__all(pair_ok(gp, tag))) break;
        __builtin_amdgcn_s_sleep(24);
      }
      const float tb = (float)((int)((gp[2] & 0xffffff00u) - (th0 << 8)) >> 8), ta_ = (float)(int)(gp[0] - th0);
      const float cb = 0.25f * (float)(gp[2] & 0xffu);
      const float r = fmaxf(cb - (float)CPA, 0.25f) * __builtin_amdgcn_rcpf(fmaxf(tb - ta_, 25.f)) * RBOOST;
      const float inv = __builtin_amdgcn_rcpf(wave_sum(r));
      const float fin = fminf(fmaxf(wave_sum((float)(NUNIT - 4) - cb + r * tb) * inv, 0.f), 8000.f);
      if (lane == 0) *(volatile LAS u32x2*)(size_t)(lds0 + L_TL) = u32x2{(th0 + (unsigned)((int)fin - PACE_OFF)) | 1u, __float_as_uint(64.f * inv)};
    }
    {
      for (unsigned spin = 0; spin < SPIN_CAP * 4; ++spin) {
        const u32x4 mr = *(volatile LAS u32x4*)(size_t)(lds0 + L_MR);
        if (__all((mr[0] & mr[1] & mr[2] & mr[3]) != 0)) break;
        __builtin_amdgcn_s_sleep(8);
      }
      asm volatile("" ::: "memory");
      __builtin_amdgcn_s_setprio(0);
      const f32x4 ma = lds_ld<f32x4>(lds0 + L_ML + (lane & 1) * 32), mb = lds_ld<f32x4>(lds0 + L_ML + (lane & 1) * 32 + 16);
      const float Ma = fmaxf(fmaxf(ma[0], ma[1]), fmaxf(ma[2], ma[3])), Mb = fmaxf(fmaxf(mb[0], mb[1]), fmaxf(mb[2], mb[3]));
      float La = 0.f, Lb = 0.f;
#pragma unroll 4
      for (int i = 0; i < 16; ++i) {
        const u32x4 xp = lds_ld<u32x4>(lds0 + L_SC + i * 1024 + lane * 16);
        La += (ex2((bflo(xp[0]) - Ma) * L2E) + ex2((bfhi(xp[0]) - Ma) * L2E)) + (ex2((bflo(xp[1]) - Ma) * L2E) + ex2((bfhi(xp[1]) - Ma) * L2E));
        Lb += (ex2((bflo(xp[2]) - Mb) * L2E) + ex2((bfhi(xp[2]) - Mb) * L2E)) + (ex2((bflo(xp[3]) - Mb) * L2E) + ex2((bfhi(xp[3]) - Mb) * L2E));
      }
      La += dpp<XOR2>(La); Lb += dpp<XOR2>(Lb);
      La = quads_sum(La); Lb = quads_sum(Lb);
      __builtin_amdgcn_s_setprio(3);
      if (lane < 4) st_dev(rws, u32x4{__float_as_uint(lane & 2 ? Mb : Ma), tag, __float_as_uint(lane & 2 ? Lb : La), tag}, ((lane & 1) * 2 + (lane >> 1)) * 16, WS_ML + wg * 128);
      sleep_until(now32() + ML_DELAY);
      u32x4 gm;
      for (unsigned spin = 0; spin < SPIN_CAP; ++spin) {
        asm volatile("" ::: "memory");
        gm = ld_dev(rws, (lane >> 2) * 128 + (lane & 3) * 16, WS_ML + (wg & ~15) * 128);
        if (__all(pair_ok(gm, tag))) break;
        __builtin_amdgcn_s_sleep(32);
      }
      const float ms = __uint_as_float(gm[0]), Mg = quads_max(ms);
      const float inv = __builtin_amdgcn_rcpf(quads_sum(ex2((ms - Mg) * L2E) * __uint_as_float(gm[2])));
      if (lane < 4) lds_st<f32x2>(lds0 + L_GS + lane * 8, f32x2{Mg, inv});
      wait_lds();
      if (lane == 0) *(volatile LAS unsigned*)(size_t)(lds0 + L_GR) = 1u;
    }
    float gate;
    {
      u32x4 gf;
      for (unsigned spin = 0; spin < SPIN_CAP; ++spin) {
        asm volatile("" ::: "memory");
        gf = ld_dev(rws, (lane < 14 ? lane : 0) * (DM * 8), WS_FG + cg * 16);
        if (__all(quad_ok(gf, tag))) break;
      }
      if (lane >= 14) gf = u32x4{0u, 0u, 0u, 0u};
      gate = rbf(silu(rbf(quad_sum(quad_val(gf[0]), quad_val(gf[1]), quad_val(gf[2]), quad_val(gf[3])))));
      asm volatile("" : "+v"(gate));
    }
    wg_barrier();
    u32x4 gs = ld_dev(rws, lane * (DM * 8), WS_P2 + cg * 16);
    float xr = bflo(xo);
    unsigned vst = (lane & 15) == 0 ? vout : OOB;
    const rsrc_t rout = make_rsrc(out, 2 * DM * 2);
    asm volatile("" : "+v"(gs), "+v"(gate), "+v"(xr), "+v"(vst) : "s"(rout));
    wg_barrier();
    {
      bool have = false;
      for (unsigned spin = 0; spin < SPIN_CAP; ++spin) {
        asm volatile("" ::: "memory");
        if (!have) gs = ld_dev(rws, lane * (DM * 8), WS_P2 + cg * 16);
        have = quad_ok(gs, tag);
        if (__all(have)) break;
      }
      const float s = rbf(quad_sum(quad_val(gs[0]), quad_val(gs[1]), quad_val(gs[2]), quad_val(gs[3])));
      __builtin_amdgcn_raw_buffer_store_b16(__builtin_bit_cast(u16, (__bf16)(xr + rbf(s * gate))), rout, vst, 0, 0);
      if (wg == 0 && lane == 0) __builtin_amdgcn_raw_buffer_store_b32(epoch, rws, 0, WS_EPOCH, SC1);
    }
  }
}

}

extern "C" void kernel_launch(void* const* d_in, const int* in_sizes, int n_in, void* d_out, int out_size, void* d_ws, size_t ws_size,
                              hipStream_t stream) {
  (void)hipFuncSetAttribute((const void*)fused_decode_kernel, hipFuncAttributeMaxDynamicSharedMemorySize, L_END);
  fused_decode_kernel<<<NWG, NTHR, L_END, stream>>>((const u16*)d_in[0], (const u16*)d_in[1], (const u16*)d_in[2], (const u16*)d_in[3],
                                                    (const u16*)d_in[4], (const u16*)d_in[6], (const u16*)d_in[7], (u16*)d_out, (char*)d_ws);
}
```

```cpp
#include <hip/hip_runtime.h>
#include <hip/hip_bf16.h>
#include <type_traits>

#define MEAS 0
namespace {

typedef unsigned short u16;
typedef short s16x4 __attribute__((ext_vector_type(4)));
typedef __bf16 bf16x8 __attribute__((ext_vector_type(8)));
typedef __bf16 bf16x2 __attribute__((ext_vector_type(2)));
typedef float f32x2 __attribute__((ext_vector_type(2)));
typedef float f32x4 __attribute__((ext_vector_type(4)));
typedef unsigned u32x2 __attribute__((ext_vector_type(2)));
typedef unsigned u32x4 __attribute__((ext_vector_type(4)));
typedef __amdgpu_buffer_rsrc_t rsrc_t;
#define LAS __attribute__((address_space(3)))

constexpr int NWG = 256, NTHR = 320;
constexpr int DM = 512, HQD = 4096, SK = 32768, DH = 128, FF = 1792;
constexpr int NSLOT = 6, AHEAD = NSLOT - 1, NTILE = 32, NUNIT = 2 * NTILE, NPRE = 11;
constexpr int WIN = 4, WIN2 = 8;
constexpr int CODE_KB = 31;
constexpr int CPA = 13, TS2 = 1500, TSD = 100, PACE_OFF = 65, OVER = 4, NSAMP = 32, POLL_AT = 170, ML_DELAY = 200;
constexpr float RBOOST = 0.99f, DELTA = 55.f;
constexpr int KV_BYTES = 2 * 8 * SK * DH * 2;
constexpr float SCALE = 0.08838834764831845f, L2E = 1.4426950408889634f;
constexpr float M_INIT = -3.0e38f;
constexpr unsigned SPIN_CAP = 1u << 18;
constexpr unsigned OOB = 0x80000000u;

constexpr int WS_EPOCH = 0;
constexpr int WS_Q = 4096;
constexpr int WS_H = WS_Q + HQD * 8;
constexpr int WS_ML = 65536;
constexpr int WS_PC = WS_ML + NWG * 128;
constexpr int WS_QP = WS_PC + NWG * 128;
constexpr int WS_HP = WS_QP + 4 * HQD * 8;
constexpr int WS_FG = WS_HP + 4 * FF * 8;
constexpr int WS_P2 = WS_FG + 14 * DM * 8;
constexpr int WS_P1 = WS_P2 + 64 * DM * 8;
constexpr int WS_END = WS_P1 + NWG * 2048;
static_assert(WS_H + FF * 8 <= WS_ML, "workspace map");

constexpr int L_RING = 0;
constexpr int L_WQ = 4 * NSLOT * 4096;
constexpr int L_SC = L_WQ;
constexpr int L_WF = L_WQ + 16384;
constexpr int L_WO = L_WF + 16384;
constexpr int L_XN = L_WO + 16384;
constexpr int L_QI = L_XN + 2048;
constexpr int L_OS = L_QI + 1024;
constexpr int L_HS = L_OS + 256;
constexpr int L_ML = L_HS + 512;
constexpr int L_TAG = L_ML + 128;
constexpr int L_QR = L_TAG + 4;
constexpr int L_TL = L_TAG + 16;
constexpr int L_TS = L_TL + 16;
constexpr int L_TP = L_TS + 16;
constexpr int L_MR = L_TP + 32;
constexpr int L_GR = L_MR + 16;
constexpr int L_GS = L_GR + 16;
constexpr int L_END = L_GS + 32;

__device__ __forceinline__ float rbf(float f) { return (float)(__bf16)f; }
__device__ __forceinline__ unsigned pk2(float lo, float hi) {
  bf16x2 v = {(__bf16)lo, (__bf16)hi};
  return __builtin_bit_cast(unsigned, v);
}
__device__ __forceinline__ float bflo(unsigned u) { return __uint_as_float(u << 16); }
__device__ __forceinline__ float bfhi(unsigned u) { return __uint_as_float(u & 0xffff0000u); }
template <int CTRL> __device__ __forceinline__ float dpp(float x) {
  return __builtin_bit_cast(float, __builtin_amdgcn_mov_dpp(__builtin_bit_cast(int, x), CTRL, 0xf, 0xf, true));
}
constexpr int XOR1 = 0xB1, XOR2 = 0x4E, ROR4 = 0x124, ROR8 = 0x128;
__device__ __forceinline__ float swap16_sum(float a, float b) {
  auto s = __builtin_amdgcn_permlane16_swap(__float_as_uint(a), __float_as_uint(b), false, false);
  return __uint_as_float(s[0]) + __uint_as_float(s[1]);
}
__device__ __forceinline__ float swap32_sum(float a, float b) {
  auto s = __builtin_amdgcn_permlane32_swap(__float_as_uint(a), __float_as_uint(b), false, false);
  return __uint_as_float(s[0]) + __uint_as_float(s[1]);
}
__device__ __forceinline__ float rows_sum(float x) { x = swap16_sum(x, x); return swap32_sum(x, x); }
__device__ __forceinline__ float rows_max(float x) {
  auto s = __builtin_amdgcn_permlane16_swap(__float_as_uint(x), __float_as_uint(x), false, false);
  x = fmaxf(__uint_as_float(s[0]), __uint_as_float(s[1]));
  auto t = __builtin_amdgcn_permlane32_swap(__float_as_uint(x), __float_as_uint(x), false, false);
  return fmaxf(__uint_as_float(t[0]), __uint_as_float(t[1]));
}
__device__ __forceinline__ float quads_sum(float x) { x += dpp<ROR4>(x); x += dpp<ROR8>(x); return rows_sum(x); }
__device__ __forceinline__ float quads_max(float x) { x = fmaxf(x, dpp<ROR4>(x)); x = fmaxf(x, dpp<ROR8>(x)); return rows_max(x); }
__device__ __forceinline__ float wave_sum(float x) { x += dpp<XOR1>(x); x += dpp<XOR2>(x); return quads_sum(x); }
__device__ __forceinline__ float ex2(float x) { return __builtin_amdgcn_exp2f(x); }
__device__ __forceinline__ float silu(float v) { return v * __builtin_amdgcn_rcpf(1.f + ex2(-v * L2E)); }

template <int N> __device__ __forceinline__ void wait_vm() {
  __builtin_amdgcn_sched_barrier(0);
  asm volatile("s_waitcnt vmcnt(%0)" ::"i"(N) : "memory");
}
__device__ __forceinline__ void wait_lds() { asm volatile("s_waitcnt lgkmcnt(0)" ::: "memory"); }
__device__ __forceinline__ void wg_barrier() {
  asm volatile("s_waitcnt lgkmcnt(0)\n\ts_barrier" ::: "memory");
}
__device__ __forceinline__ void lds_wait_nonzero(unsigned addr) {
  for (unsigned spin = 0; spin < SPIN_CAP * 16 && *(volatile LAS unsigned*)(size_t)addr == 0; ++spin) __builtin_amdgcn_s_sleep(1);
  asm volatile("" ::: "memory");
}
__device__ __forceinline__ unsigned now32() { return (unsigned)__builtin_amdgcn_s_memrealtime(); }
__device__ __forceinline__ void sleep_until(unsigned due) { while ((int)(now32() - due) < 0) __builtin_amdgcn_s_sleep(2); }
__device__ __forceinline__ rsrc_t make_rsrc(const void* p, int bytes) {
  return __builtin_amdgcn_make_buffer_rsrc((void*)p, (short)0, bytes, 0x00020000);
}
constexpr int SC1 = 16;
__device__ __forceinline__ u32x4 ld_dev(rsrc_t r, unsigned voff, unsigned soff) { return __builtin_amdgcn_raw_buffer_load_b128(r, voff, soff, SC1); }
__device__ __forceinline__ void st_dev(rsrc_t r, u32x4 v, unsigned voff, unsigned soff) { __builtin_amdgcn_raw_buffer_store_b128(v, r, voff, soff, SC1); }
__device__ __forceinline__ bool pair_ok(const u32x4& g, unsigned tag) { return (g[1] == tag) & (g[3] == tag); }
__device__ __forceinline__ bool quad_ok(const u32x4& g, unsigned tag) {
  return (__builtin_amdgcn_perm(g[1], g[0], 0x0c0c0400u) | __builtin_amdgcn_perm(g[3], g[2], 0x04000c0cu)) == tag;
}
__device__ __forceinline__ float quad_val(unsigned w) { return __uint_as_float(w & 0xffffff00u); }
struct QuadOut {
  unsigned t0, t1, t2, t3, voff;
  __device__ __forceinline__ QuadOut(unsigned tag, int lane) : t0(tag & 255u), t1((tag >> 8) & 255u), t2((tag >> 16) & 255u), t3(tag >> 24), voff(lane & 1 ? OOB : lane * 8) {
    asm volatile("" : "+v"(t0), "+v"(t1), "+v"(t2), "+v"(t3), "+v"(voff));
  }
  __device__ __forceinline__ void store(rsrc_t r, float a, float b, unsigned soff) const {
    const float c = dpp<XOR1>(a), d = dpp<XOR1>(b);
    st_dev(r, u32x4{(__float_as_uint(a) & 0xffffff00u) | t0, (__float_as_uint(b) & 0xffffff00u) | t1, (__float_as_uint(c) & 0xffffff00u) | t2,
                    (__float_as_uint(d) & 0xffffff00u) | t3}, voff, soff);
  }
};
__device__ __forceinline__ float quad_sum(float v0, float v1, float v2, float v3) {
  float x = swap16_sum(swap32_sum(v0, v2), swap32_sum(v1, v3));
  x += dpp<ROR8>(x); x += dpp<ROR4>(x); x += dpp<XOR2>(x); x += dpp<XOR1>(x);
  return x;
}
__device__ __forceinline__ s16x4 tr_read(unsigned addr) { return __builtin_amdgcn_ds_read_tr16_b64_v4i16((LAS s16x4*)(size_t)addr); }
template <typename T> __device__ __forceinline__ T lds_ld(unsigned addr) { return *(const LAS T*)(size_t)addr; }
template <typename T> __device__ __forceinline__ void lds_st(unsigned addr, T v) { *(LAS T*)(size_t)addr = v; }

__device__ __forceinline__ void dma_piece(rsrc_t r, unsigned voff, unsigned soff, unsigned dst) {
  asm volatile("s_mov_b32 m0, %0\n\ts_nop 0\n\tbuffer_load_dwordx4 %1, %2, %3 offen nt lds" ::"s"(dst), "v"(voff), "s"(r), "s"(soff) : "memory");
}
__device__ __forceinline__ void dma_half(rsrc_t r, unsigned v0, unsigned v1, unsigned v2, unsigned v3, unsigned soff, unsigned dst) {
  asm volatile(
      "s_mov_b32 m0, %0\n\ts_nop 0\n\t"
      "buffer_load_dwordx4 %1, %5, %6 offen nt lds\n\t"
      "buffer_load_dwordx4 %2, %5, %6 offen offset:1024 nt lds\n\t"
      "buffer_load_dwordx4 %3, %5, %6 offen offset:2048 nt lds\n\t"
      "buffer_load_dwordx4 %4, %5, %6 offen offset:3072 nt lds"
      ::"s"(dst), "v"(v0), "v"(v1), "v"(v2), "v"(v3), "s"(r), "s"(soff)
      : "memory");
}
template <int A> __device__ __forceinline__ f32x4 mfma4(s16x4 a, s16x4 b, f32x4 c) { return __builtin_amdgcn_mfma_f32_4x4x4bf16_1k(a, b, c, 4, A, 0); }
template <int A, int N> struct Proj {
  static __device__ __forceinline__ void run(const s16x4 (&av)[2], unsigned ba, f32x4 (&c)[4]) {
    c[A & 3] = mfma4<A & 15>(av[A >> 4], tr_read(ba + A * 512), c[A & 3]);
    if constexpr (A + 1 < N) Proj<A + 1, N>::run(av, ba, c);
  }
};
template <int A> struct ProjReg {
  static __device__ __forceinline__ void run(s16x4 a, const s16x4 (&w)[16], f32x4 (&c)[2]) {
    c[A & 1] = mfma4<A>(a, w[A], c[A & 1]);
    if constexpr (A + 1 < 16) ProjReg<A + 1>::run(a, w, c);
  }
};
__device__ __forceinline__ f32x2 proj64(unsigned a_img, int a_stride, unsigned w_img, int lane) {
  const int q = (lane & 15) >> 2, p = lane & 3;
  const unsigned aa = a_img + (lane & 1) * a_stride + (lane >> 2) * 8;
  const s16x4 av[2] = {lds_ld<s16x4>(aa), lds_ld<s16x4>(aa + 128)};
  const unsigned ba = w_img + q * 128 + (((2 * (lane >> 4) + (p >> 1)) ^ ((q >> 1) << 2)) << 4) + (p & 1) * 8;
  f32x4 c[4] = {};
  Proj<0, 32>::run(av, ba, c);
  return f32x2{(c[0][0] + c[1][0]) + (c[2][0] + c[3][0]), (c[0][1] + c[1][1]) + (c[2][1] + c[3][1])};
}
__device__ __forceinline__ f32x4 gather_quarters(rsrc_t rws, unsigned base, int quarter_bytes, unsigned tag, int lane) {
  u32x4 g;
  for (unsigned spin = 0; spin < SPIN_CAP; ++spin) {
    asm volatile("" ::: "memory");
    g = ld_dev(rws, (lane >> 4) * quarter_bytes + (lane & 7) * 16, base);
    if (__all(quad_ok(g, tag))) break;
    __builtin_amdgcn_s_sleep(8);
  }
  return f32x4{rows_sum(quad_val(g[0])), rows_sum(quad_val(g[1])), rows_sum(quad_val(g[2])), rows_sum(quad_val(g[3]))};
}

template <int N> using Int = std::integral_constant<int, N>;
struct KFrag { bf16x8 k[4]; };
struct VFrag { s16x4 v[4][2]; };

__device__ __forceinline__ void read_k(KFrag& t, const unsigned (&ka)[4], unsigned off) {
#pragma unroll
  for (int s = 0; s < 4; ++s) t.k[s] = lds_ld<bf16x8>(ka[s] + off);
  asm volatile("s_waitcnt lgkmcnt(0)" : "+v"(t.k[0]), "+v"(t.k[1]), "+v"(t.k[2]), "+v"(t.k[3])::"memory");
}
__device__ __forceinline__ void read_v(VFrag& t, const unsigned (&va)[2], unsigned off) {
#pragma unroll
  for (int a = 0; a < 4; ++a)
#pragma unroll
    for (int h = 0; h < 2; ++h) t.v[a][h] = tr_read(va[h] + off + a * 1024);
  asm volatile("s_waitcnt lgkmcnt(0)"
               : "+v"(t.v[0][0]), "+v"(t.v[0][1]), "+v"(t.v[1][0]), "+v"(t.v[1][1]), "+v"(t.v[2][0]), "+v"(t.v[2][1]), "+v"(t.v[3][0]), "+v"(t.v[3][1])
               :
               : "memory");
}
__device__ __forceinline__ void score_tile(const KFrag& t, const bf16x8 (&qf)[4], float& m, unsigned dst, bool writer) {
  f32x4 sc = {0.f, 0.f, 0.f, 0.f};
#pragma unroll
  for (int s = 0; s < 4; ++s) sc = __builtin_amdgcn_mfma_f32_16x16x32_bf16(t.k[s], qf[s], sc, 0, 0, 0);
  m = fmaxf(fmaxf(m, fmaxf(sc[0], sc[1])), fmaxf(sc[2], sc[3]));
  const unsigned r0 = pk2(sc[0], sc[1]), r1 = pk2(sc[2], sc[3]);
  if (writer) lds_st<u32x2>(dst, u32x2{pk2(bflo(r0) * SCALE, bfhi(r0) * SCALE), pk2(bflo(r1) * SCALE, bfhi(r1) * SCALE)});
}
__device__ __forceinline__ s16x4 probs(unsigned src, float M, float f) {
  const u32x2 xp = lds_ld<u32x2>(src);
  const u32x2 pp = {pk2(f * ex2((bflo(xp[0]) - M) * L2E), f * ex2((bfhi(xp[0]) - M) * L2E)), pk2(f * ex2((bflo(xp[1]) - M) * L2E), f * ex2((bfhi(xp[1]) - M) * L2E))};
  return __builtin_bit_cast(s16x4, pp);
}
__device__ __forceinline__ void value_tile(const VFrag& t, s16x4 pa, f32x4 (&o)[2]) {
#pragma unroll
  for (int h = 0; h < 2; ++h) o[h] = mfma4<0>(pa, t.v[0][h], o[h]);
#pragma unroll
  for (int h = 0; h < 2; ++h) o[h] = mfma4<4>(pa, t.v[1][h], o[h]);
#pragma unroll
  for (int h = 0; h < 2; ++h) o[h] = mfma4<8>(pa, t.v[2][h], o[h]);
#pragma unroll
  for (int h = 0; h < 2; ++h) o[h] = mfma4<12>(pa, t.v[3][h], o[h]);
}

__global__ void __launch_bounds__(NTHR, 2)
fused_decode_kernel(const u16* __restrict__ x, const u16* __restrict__ kc, const u16* __restrict__ vc, const u16* __restrict__ wq,
                    const u16* __restrict__ wo, const u16* __restrict__ w1, const u16* __restrict__ w2, u16* __restrict__ out, char* ws) {
  unsigned long long pc;
  asm volatile("s_getpc_b64 %0" : "=s"(pc));
  extern __shared__ __attribute__((aligned(16))) unsigned char lds[];
  const unsigned lds0 = (unsigned)(size_t)(LAS unsigned char*)lds;
  const int lane = threadIdx.x & 63, wave = __builtin_amdgcn_readfirstlane(threadIdx.x >> 6), wg = blockIdx.x;
  const int xg = wg & 7, xi = wg >> 3;
  asm volatile("" ::"s"(x), "s"(kc), "s"(vc), "s"(wq), "s"(wo), "s"(w1), "s"(w2), "s"(out), "s"(ws));
  const rsrc_t rws = make_rsrc(ws, WS_END);
  const unsigned sw128 = ((lane & 7) ^ (((lane >> 4) & 1) << 2)) << 4;

  if (wave < 4) {
    const rsrc_t rk = make_rsrc(kc, KV_BYTES), rv = make_rsrc(vc, KV_BYTES);
    const int g = lane >> 4, c = lane & 15;
    unsigned vk[4];
#pragma unroll
    for (int u = 0; u < 4; ++u) vk[u] = g * 256 + ((c ^ (4 * u + g)) << 4);
    const unsigned vv = g * 256 + ((c ^ (g << 2)) << 4);
    const unsigned ring = lds0 + L_RING + wave * (NSLOT * 4096);
    const unsigned tile0 = (unsigned)wg * (2048 * 256) + wave * 4096;
    unsigned ka[4], va[2];
#pragma unroll
    for (int s = 0; s < 4; ++s) ka[s] = ring + c * 256 + (((4 * s + g) ^ c) << 4);
#pragma unroll
    for (int h = 0; h < 2; ++h) va[h] = ring + (c >> 2) * 256 + (((8 * h + 2 * g + ((c & 3) >> 1)) ^ ((c >> 2) << 2)) << 4) + (c & 1) * 8;
    const unsigned sca = lds0 + L_SC + wave * 4096 + (g * 4 + (c & 3)) * 8;
    auto issue_k = [&](int t, unsigned off) { dma_half(rk, vk[0], vk[1], vk[2], vk[3], tile0 + t * 16384, ring + off); };
    auto issue_v = [&](int t, unsigned off) { dma_half(rv, vv, vv, vv, vv, tile0 + t * 16384, ring + off); };

    wg_barrier();
#pragma unroll
    for (int u = 0; u < AHEAD; ++u) { wait_vm<WIN>(); issue_k(u, u * 4096); }
    KFrag pk[NPRE];
#pragma unroll
    for (int u = 0; u < NPRE; ++u) {
      wait_vm<WIN>();
      issue_k(u + AHEAD, ((u + AHEAD) % NSLOT) * 4096);
      read_k(pk[u], ka, (u % NSLOT) * 4096);
    }
    lds_wait_nonzero(lds0 + L_QR);
    bf16x8 qf[4];
#pragma unroll
    for (int s = 0; s < 4; ++s) qf[s] = lds_ld<bf16x8>(lds0 + L_QI + (c & 3) * 256 + (4 * s + g) * 16);
    const unsigned tag = lds_ld<unsigned>(lds0 + L_TAG);

    unsigned prev = now32();
    auto landed = [&](int u, auto win) {
      const u32x2 plan = *(volatile LAS u32x2*)(size_t)(lds0 + L_TL);
      wait_vm<win.value>();
      unsigned tn = now32();
      *(volatile LAS u32x2*)(size_t)(lds0 + L_TP + wave * 8) = u32x2{(unsigned)u, tn};
      if (u == CPA) *(volatile LAS unsigned*)(size_t)(lds0 + L_TS + wave * 4) = tn | 1u;
      const unsigned tl = __builtin_amdgcn_readfirstlane(plan[0]);
      const int d = (int)(tl - prev);
      if (tl != 0 && d > 0) {
        const float n = (float)(NUNIT - AHEAD - u);
        const bool behind = (float)d + DELTA < n * __uint_as_float(plan[1]);
        const unsigned due = prev + (unsigned)__builtin_amdgcn_readfirstlane(behind ? 0 : (int)((float)d * __builtin_amdgcn_rcpf(n)));
        while ((int)(tn - due) < 0) { __builtin_amdgcn_s_sleep(1); tn = now32(); }
      }
      prev = tn + OVER;
    };
    static_assert(WIN <= 16 && WIN2 <= 16 && 2 * NPRE <= NTILE - AHEAD && CPA >= NPRE, "unit schedule");
    float m = M_INIT;
    KFrag kf;
#pragma unroll
    for (int u = NPRE; u < 2 * NPRE; ++u) {
      landed(u, Int<WIN>{}); issue_k(u + AHEAD, ((u + AHEAD) % NSLOT) * 4096);
      read_k(kf, ka, (u % NSLOT) * 4096);
      score_tile(kf, qf, m, sca + u * 128, c < 4);
      score_tile(pk[u - NPRE], qf, m, sca + (u - NPRE) * 128, c < 4);
    }
    unsigned off = ((2 * NPRE) % NSLOT) * 4096, offp = ((2 * NPRE + AHEAD) % NSLOT) * 4096;
    auto next_slot = [&]() { offp = off; off = off == (NSLOT - 1) * 4096 ? 0u : off + 4096; };
#pragma unroll 1
    for (int u = 2 * NPRE; u < NTILE - AHEAD; ++u) {
      landed(u, Int<WIN>{}); issue_k(u + AHEAD, offp);
      read_k(kf, ka, off);
      score_tile(kf, qf, m, sca + u * 128, c < 4);
      next_slot();
    }
#pragma unroll 1
    for (int u = NTILE - AHEAD; u < NTILE; ++u) {
      landed(u, Int<WIN2>{}); issue_v(u + AHEAD - NTILE, offp);
      read_k(kf, ka, off);
      score_tile(kf, qf, m, sca + u * 128, c < 4);
      next_slot();
    }
    const float M = rbf(rbf(rows_max(m)) * SCALE);
    if (lane < 4) lds_st<float>(lds0 + L_ML + lane * 16 + wave * 4, M);
    wait_lds();
    if (lane == 0) *(volatile LAS unsigned*)(size_t)(lds0 + L_MR + wave * 4) = 1u;
    f32x4 o[2] = {};
    VFrag vf;
#pragma unroll 1
    for (int u = NTILE; u < NUNIT - AHEAD; ++u) {
      landed(u, Int<WIN2>{}); issue_v(u + AHEAD - NTILE, offp);
      const s16x4 pa = probs(sca + (u - NTILE) * 128, M, 1.f);
      read_v(vf, va, off);
      value_tile(vf, pa, o);
      next_slot();
    }
    const int ks = xg * 8 + (xi >> 2), cs = xi & 3;
    if (wave < 2) {
      const rsrc_t ro = make_rsrc(wo, HQD * DM * 2);
#pragma unroll
      for (int i = 0; i < 8; ++i)
        dma_piece(ro, (lane >> 3) * (DM * 2) + sw128, (ks * 64 + 8 * i) * (DM * 2) + cs * 256 + wave * 128, lds0 + L_WO + wave * 8192 + i * 1024);
    }
    lds_wait_nonzero(lds0 + L_GR);
    const f32x2 gs = lds_ld<f32x2>(lds0 + L_GS + (c & 3) * 8);
    const float f = ex2((M - gs[0]) * L2E) * gs[1];
    {
      const float f0 = dpp<0x00>(f), f1 = dpp<0x55>(f), f2 = dpp<0xAA>(f), f3 = dpp<0xFF>(f);
#pragma unroll
      for (int h = 0; h < 2; ++h) { o[h][0] *= f0; o[h][1] *= f1; o[h][2] *= f2; o[h][3] *= f3; }
    }
    const QuadOut qo(tag, lane);
    unsigned s_p1 = WS_P1 + wg * 2048 + wave * 512, s_p2 = WS_P2 + ks * (DM * 8) + (cs * 128 + wave * 64) * 8;
    unsigned a_mw = ring + lane * 4, a_mr = lds0 + L_RING + (wave * 128 + 2 * lane) * 4;
    unsigned a_ow = lds0 + L_OS + (wave >> 1) * 128 + ((wave & 1) * 32 + 4 * (lane & 7) + g) * 2, a_or = lds0 + L_OS + (lane & 1) * 128 + (lane >> 2) * 8;
    asm volatile("" : "+s"(s_p1), "+s"(s_p2), "+v"(a_mw), "+v"(a_mr), "+v"(a_ow), "+v"(a_or));
    auto last = [&](auto j) {
      constexpr int u = NUNIT - AHEAD + j.value, younger = (NUNIT - 1 - u) * 4;
      s16x4 pa = probs(sca + (u - NTILE) * 128, M, f);
      asm volatile("" : "+v"(pa));
      if (wave < 2) wait_vm<younger + 8>(); else wait_vm<younger>();
      read_v(vf, va, (u % NSLOT) * 4096);
      value_tile(vf, pa, o);
    };
    last(Int<0>{}); last(Int<1>{}); last(Int<2>{}); last(Int<3>{}); last(Int<4>{});

#pragma unroll
    for (int h = 0; h < 2; ++h)
#pragma unroll
      for (int r = 0; r < 4; ++r) lds_st<float>(a_mw + (r * 128 + h * 64) * 4, o[h][r]);
    wg_barrier();
    {
      f32x2 ov[4];
#pragma unroll
      for (int w = 0; w < 4; ++w) ov[w] = lds_ld<f32x2>(a_mr + w * (NSLOT * 4096));
      const f32x2 sum = (ov[0] + ov[1]) + (ov[2] + ov[3]);
      qo.store(rws, sum[0], sum[1], s_p1);
    }
    s16x4 wf[16];
    if (wave < 2) {
      const unsigned wa = lds0 + L_WO + wave * 8192 + (c >> 2) * 128 + (((2 * g + ((c & 3) >> 1)) ^ ((c >> 3) << 2)) << 4) + (c & 1) * 8;
      wait_vm<1>();
#pragma unroll
      for (int a = 0; a < 16; ++a) wf[a] = tr_read(wa + a * 512);
    }
    {
      const int b = wave >> 1, half = wave & 1;
      const unsigned vo = (lane >> 3) * 2048 + (lane & 7) * 16;
      const unsigned so = WS_P1 + (b * 8 + xg) * 16 * 2048 + ((ks >> 1) & 3) * 512 + ((ks & 1) * 2 + half) * 128;
      sleep_until(__builtin_amdgcn_readfirstlane(*(volatile LAS unsigned*)(size_t)(lds0 + L_TL)) + POLL_AT);
      u32x4 gv[2] = {};
      bool h0 = false, h1 = false;
      for (unsigned spin = 0; spin < SPIN_CAP; ++spin) {
        asm volatile("" ::: "memory");
        if (!h0) gv[0] = ld_dev(rws, vo, so);
        if (!h1) gv[1] = ld_dev(rws, vo + 8 * 2048, so);
        h0 = quad_ok(gv[0], tag); h1 = quad_ok(gv[1], tag);
        if (__all(h0 & h1)) break;
      }
      float u[4];
#pragma unroll
      for (int j = 0; j < 4; ++j) u[j] = quad_val(gv[0][j]) + quad_val(gv[1][j]);
      float e = swap16_sum(swap32_sum(u[0], u[2]), swap32_sum(u[1], u[3]));
      e += dpp<ROR8>(e);
      lds_st<__bf16>(a_ow, (__bf16)e);
    }
    wg_barrier();
    if (wave < 2) {
      const s16x4 oa = lds_ld<s16x4>(a_or);
      f32x4 acc[2] = {};
      ProjReg<0>::run(oa, wf, acc);
      qo.store(rws, acc[0][0] + acc[1][0], acc[0][1] + acc[1][1], s_p2);
    }
  } else {
    __builtin_amdgcn_s_setprio(3);
    const unsigned tstart = now32();
    const int cg = xg * 32 + xi;
    const int qk = cg >> 6, qc = cg & 63;
    const bool f1 = xi < 14, f2 = xi >= 14 && xi < 28;
    const int fi = xg * 14 + (xi < 14 ? xi : xi - 14);
    const int hk = fi / 28, hc = fi % 28;
    const int gk = fi >> 3, gc = fi & 7;
    const unsigned vout = (((lane >> 4) & 1) * DM + cg * 2 + (lane >> 5)) * 2;
    const rsrc_t rx = make_rsrc(x, 2 * DM * 2);
    unsigned ev, xo, tv, to;
    u32x4 x0, x1, code;
    asm volatile(
        "s_nop 4\n\t"
        "buffer_load_dwordx4 %0, %7, %8, 0 offen\n\t"
        "buffer_load_dwordx4 %1, %7, %8, 0 offen offset:1024\n\t"
        "buffer_load_dword %2, off, %9, 0 sc1\n\t"
        "buffer_load_ushort %3, %10, %8, 0 offen\n\t"
        "buffer_load_dwordx4 %4, %7, %11, %12 offen\n\t"
        "buffer_load_dword %5, off, %13, %14 nt\n\t"
        "buffer_load_dword %6, off, %15, %16 nt"
        : "=&v"(x0), "=&v"(x1), "=&v"(ev), "=&v"(xo), "=&v"(code), "=&v"(tv), "=&v"(to)
        : "v"(lane * 16), "s"(rx), "s"(rws), "v"(vout), "s"(make_rsrc((const void*)(pc & ~255ull), CODE_KB * 1024)), "s"(xi * 1024),
          "s"(make_rsrc(vc, KV_BYTES)), "s"(wg * (2048 * 256)), "s"(make_rsrc(wo, HQD * DM * 2)), "s"((xg * 8 + (xi >> 2)) * 64 * (DM * 2))
        : "memory");
    {
      const rsrc_t rq = make_rsrc(wq, DM * HQD * 2);
#pragma unroll
      for (int i = 0; i < 16; ++i) dma_piece(rq, (lane >> 3) * (HQD * 2) + sw128, (qk * 128 + 8 * i) * (HQD * 2) + qc * 128, lds0 + L_WQ + i * 1024);
      __builtin_amdgcn_sched_barrier(0);
      const rsrc_t rf = make_rsrc(f1 ? w1 : w2, f1 || f2 ? DM * FF * 2 : 0);
      const int ld = f1 ? FF * 2 : DM * 2;
      const unsigned sf = f1 ? hk * 128 * (FF * 2) + hc * 128 : gk * 128 * (DM * 2) + gc * 128;
#pragma unroll
      for (int i = 0; i < 16; ++i) dma_piece(rf, (lane >> 3) * ld + sw128, sf + 8 * i * ld, lds0 + L_WF + i * 1024);
    }
    if (lane < 28) lds_st<unsigned>(lds0 + L_TAG + lane * 4, 0u);
    wg_barrier();
    const unsigned th0 = now32();
    asm volatile("s_waitcnt vmcnt(32)" : "+v"(x0), "+v"(x1), "+v"(ev), "+v"(xo), "+v"(code), "+v"(tv), "+v"(to)::"memory");
    const unsigned epoch = max(__builtin_amdgcn_readfirstlane(ev) + 1u, 1u), tag = epoch * 0x9E3779B1u;
    {
      float a[2][8];
#pragma unroll
      for (int j = 0; j < 4; ++j) { a[0][2 * j] = bflo(x0[j]); a[0][2 * j + 1] = bfhi(x0[j]); a[1][2 * j] = bflo(x1[j]); a[1][2 * j + 1] = bfhi(x1[j]); }
#pragma unroll
      for (int b = 0; b < 2; ++b) {
        const float mean = wave_sum(((a[b][0] + a[b][1]) + (a[b][2] + a[b][3])) + ((a[b][4] + a[b][5]) + (a[b][6] + a[b][7]))) * (1.f / DM);
        float q = 0.f;
#pragma unroll
        for (int j = 0; j < 8; ++j) q = fmaf(a[b][j] - mean, a[b][j] - mean, q);
        const float var = wave_sum(q) * (1.f / DM);
        const float mb = rbf(mean), inv = __builtin_amdgcn_rcpf(rbf(__builtin_amdgcn_sqrtf(rbf(rbf(var) + 1e-5f))));
        u32x4 xn;
#pragma unroll
        for (int j = 0; j < 4; ++j) xn[j] = pk2(rbf(a[b][2 * j] - mb) * inv, rbf(a[b][2 * j + 1] - mb) * inv);
        lds_st<u32x4>(lds0 + L_XN + b * 1024 + lane * 16, xn);
      }
    }
    if (lane == 0) lds_st<unsigned>(lds0 + L_TAG, tag);
    const QuadOut qo(tag, lane);
    asm volatile("s_waitcnt vmcnt(16) lgkmcnt(0)" ::: "memory");
    if (wg < 8 && lane == 0) st_dev(rws, u32x4{tstart, tag, tstart, tag}, 0, 1024 + wg * 128);
    {
      const f32x2 y = proj64(lds0 + L_XN + qk * 256, 1024, lds0 + L_WQ, lane);
      qo.store(rws, y[0], y[1], WS_QP + (qk * HQD + qc * 64) * 8);
    }
    if (f1) {
      wait_vm<1>();
      const f32x2 y = proj64(lds0 + L_XN + hk * 256, 1024, lds0 + L_WF, lane);
      qo.store(rws, y[0], y[1], WS_HP + (hk * FF + hc * 64) * 8);
    }
    {
      const f32x4 q = gather_quarters(rws, WS_QP + (qc * 64 + qk * 16) * 8, HQD * 8, tag, lane);
      if (lane < 8) st_dev(rws, u32x4{pk2(q[0], q[1]), tag, pk2(q[2], q[3]), tag}, lane * 16, WS_Q + (qc * 64 + qk * 16) * 8);
    }
    if (f1) {
      const f32x4 h = gather_quarters(rws, WS_HP + (hc * 64 + hk * 16) * 8, FF * 8, tag, lane);
      if (lane < 8) st_dev(rws, u32x4{pk2(silu(rbf(h[0])), silu(rbf(h[1]))), tag, pk2(silu(rbf(h[2])), silu(rbf(h[3]))), tag}, lane * 16, WS_H + (hc * 64 + hk * 16) * 8);
    }
    {
      const int b = wg >> 7, kvh = (wg >> 4) & 7;
      u32x4 gq[4];
      for (unsigned spin = 0; spin < SPIN_CAP; ++spin) {
        asm volatile("" ::: "memory");
#pragma unroll
        for (int i = 0; i < 4; ++i) gq[i] = ld_dev(rws, lane * 16 + i * 1024, WS_Q + kvh * 4096);
        if (__all(pair_ok(gq[0], tag) & pair_ok(gq[1], tag) & pair_ok(gq[2], tag) & pair_ok(gq[3], tag))) break;
        __builtin_amdgcn_s_sleep(8);
      }
      const unsigned sel = b ? 0x07060302u : 0x05040100u;
#pragma unroll
      for (int i = 0; i < 4; ++i) lds_st<unsigned>(lds0 + L_QI + i * 256 + lane * 4, __builtin_amdgcn_perm(gq[i][2], gq[i][0], sel));
    }
    wait_lds();
    if (lane == 0) *(volatile LAS unsigned*)(size_t)(lds0 + L_QR) = 1u;

    if (f2) {
      u32x4 gh;
      for (unsigned spin = 0; spin < SPIN_CAP; ++spin) {
        asm volatile("" ::: "memory");
        gh = ld_dev(rws, lane * 16, WS_H + gk * 1024);
        if (__all(pair_ok(gh, tag))) break;
        __builtin_amdgcn_s_sleep(32);
      }
      lds_st<unsigned>(lds0 + L_HS + lane * 4, __builtin_amdgcn_perm(gh[2], gh[0], 0x05040100u));
      lds_st<unsigned>(lds0 + L_HS + 256 + lane * 4, __builtin_amdgcn_perm(gh[2], gh[0], 0x07060302u));
      wait_lds();
      const f32x2 y = proj64(lds0 + L_HS, 256, lds0 + L_WF, lane);
      qo.store(rws, y[0], y[1], WS_FG + (gk * DM + gc * 64) * 8);
    }
    {
      sleep_until(th0 + TS2);
      u32x4 ta;
      for (unsigned spin = 0; spin < SPIN_CAP; ++spin) {
        ta = *(volatile LAS u32x4*)(size_t)(lds0 + L_TS);
        if (__all((ta[0] != 0) & (ta[1] != 0) & (ta[2] != 0) & (ta[3] != 0))) break;
        __builtin_amdgcn_s_sleep(8);
      }
      const u32x4 p0 = *(volatile LAS u32x4*)(size_t)(lds0 + L_TP), p1 = *(volatile LAS u32x4*)(size_t)(lds0 + L_TP + 16);
      {
        const unsigned a = th0 + (((ta[0] - th0) + (ta[1] - th0) + (ta[2] - th0) + (ta[3] - th0)) >> 2);
        const unsigned b = th0 + (((p0[1] - th0) + (p0[3] - th0) + (p1[1] - th0) + (p1[3] - th0)) >> 2);
        const unsigned c4 = p0[0] + p0[2] + p1[0] + p1[2];
        if (lane == 0) st_dev(rws, u32x4{a, tag, (b << 8) | c4, tag}, 0, WS_PC + wg * 128);
      }
      sleep_until(now32() + TSD);
      u32x4 gp;
      for (unsigned spin = 0; spin < SPIN_CAP; ++spin) {
        asm volatile("" ::: "memory");
        gp = ld_dev(rws, (lane & (NSAMP - 1)) * 128, WS_PC + (wg & ~(NSAMP - 1)) * 128);
        if (__all(pair_ok(gp, tag))) break;
        __builtin_amdgcn_s_sleep(24);
      }
      const float tb = (float)((int)((gp[2] & 0xffffff00u) - (th0 << 8)) >> 8), ta_ = (float)(int)(gp[0] - th0);
      const float cb = 0.25f * (float)(gp[2] & 0xffu);
      const float r = fmaxf(cb - (float)CPA, 0.25f) * __builtin_amdgcn_rcpf(fmaxf(tb - ta_, 25.f)) * RBOOST;
      const float inv = __builtin_amdgcn_rcpf(wave_sum(r));
      const float fin = fminf(fmaxf(wave_sum((float)(NUNIT - 4) - cb + r * tb) * inv, 0.f), 8000.f);
      if (lane == 0) *(volatile LAS u32x2*)(size_t)(lds0 + L_TL) = u32x2{(th0 + (unsigned)((int)fin - PACE_OFF)) | 1u, __float_as_uint(64.f * inv)};
    }
    {
      for (unsigned spin = 0; spin < SPIN_CAP * 4; ++spin) {
        const u32x4 mr = *(volatile LAS u32x4*)(size_t)(lds0 + L_MR);
        if (__all((mr[0] & mr[1] & mr[2] & mr[3]) != 0)) break;
        __builtin_amdgcn_s_sleep(8);
      }
      asm volatile("" ::: "memory");
      __builtin_amdgcn_s_setprio(0);
      const f32x4 ma = lds_ld<f32x4>(lds0 + L_ML + (lane & 1) * 32), mb = lds_ld<f32x4>(lds0 + L_ML + (lane & 1) * 32 + 16);
      const float Ma = fmaxf(fmaxf(ma[0], ma[1]), fmaxf(ma[2], ma[3])), Mb = fmaxf(fmaxf(mb[0], mb[1]), fmaxf(mb[2], mb[3]));
      float La = 0.f, Lb = 0.f;
#pragma unroll 4
      for (int i = 0; i < 16; ++i) {
        const u32x4 xp = lds_ld<u32x4>(lds0 + L_SC + i * 1024 + lane * 16);
        La += (ex2((bflo(xp[0]) - Ma) * L2E) + ex2((bfhi(xp[0]) - Ma) * L2E)) + (ex2((bflo(xp[1]) - Ma) * L2E) + ex2((bfhi(xp[1]) - Ma) * L2E));
        Lb += (ex2((bflo(xp[2]) - Mb) * L2E) + ex2((bfhi(xp[2]) - Mb) * L2E)) + (ex2((bflo(xp[3]) - Mb) * L2E) + ex2((bfhi(xp[3]) - Mb) * L2E));
      }
      La += dpp<XOR2>(La); Lb += dpp<XOR2>(Lb);
      La = quads_sum(La); Lb = quads_sum(Lb);
      __builtin_amdgcn_s_setprio(3);
      if (lane < 4) st_dev(rws, u32x4{__float_as_uint(lane & 2 ? Mb : Ma), tag, __float_as_uint(lane & 2 ? Lb : La), tag}, ((lane & 1) * 2 + (lane >> 1)) * 16, WS_ML + wg * 128);
      sleep_until(now32() + ML_DELAY);
      u32x4 gm;
      for (unsigned spin = 0; spin < SPIN_CAP; ++spin) {
        asm volatile("" ::: "memory");
        gm = ld_dev(rws, (lane >> 2) * 128 + (lane & 3) * 16, WS_ML + (wg & ~15) * 128);
        if (__all(pair_ok(gm, tag))) break;
        __builtin_amdgcn_s_sleep(32);
      }
      const float ms = __uint_as_float(gm[0]), Mg = quads_max(ms);
      const float inv = __builtin_amdgcn_rcpf(quads_sum(ex2((ms - Mg) * L2E) * __uint_as_float(gm[2])));
      if (lane < 4) lds_st<f32x2>(lds0 + L_GS + lane * 8, f32x2{Mg, inv});
      wait_lds();
      if (lane == 0) *(volatile LAS unsigned*)(size_t)(lds0 + L_GR) = 1u;
    }
    float gate;
    {
      u32x4 gf;
      for (unsigned spin = 0; spin < SPIN_CAP; ++spin) {
        asm volatile("" ::: "memory");
        gf = ld_dev(rws, (lane < 14 ? lane : 0) * (DM * 8), WS_FG + cg * 16);
        if (__all(quad_ok(gf, tag))) break;
      }
      if (lane >= 14) gf = u32x4{0u, 0u, 0u, 0u};
      gate = rbf(silu(rbf(quad_sum(quad_val(gf[0]), quad_val(gf[1]), quad_val(gf[2]), quad_val(gf[3])))));
      asm volatile("" : "+v"(gate));
    }
    wg_barrier();
    u32x4 gs = ld_dev(rws, lane * (DM * 8), WS_P2 + cg * 16);
    float xr = bflo(xo);
    unsigned vst = (lane & 15) == 0 ? vout : OOB;
    const rsrc_t rout = make_rsrc(out, 2 * DM * 2);
    asm volatile("" : "+v"(gs), "+v"(gate), "+v"(xr), "+v"(vst) : "s"(rout));
    wg_barrier();
    {
      bool have = false;
      for (unsigned spin = 0; spin < SPIN_CAP; ++spin) {
        asm volatile("" ::: "memory");
        if (!have) gs = ld_dev(rws, lane * (DM * 8), WS_P2 + cg * 16);
        have = quad_ok(gs, tag);
        if (__all(have)) break;
      }
      const float s = rbf(quad_sum(quad_val(gs[0]), quad_val(gs[1]), quad_val(gs[2]), quad_val(gs[3])));
      __builtin_amdgcn_raw_buffer_store_b16(__builtin_bit_cast(u16, (__bf16)(xr + rbf(s * gate))), rout, vst, 0, 0);
      if (wg == 0 && lane == 0) __builtin_amdgcn_raw_buffer_store_b32(epoch, rws, 0, WS_EPOCH, SC1);
      if (wg == 0) {
        const u32x4 r = ld_dev(rws, (lane & 7) * 128, 1024);
        const float d = (float)(int)(r[0] - tstart);
        float mx = fmaxf(d, dpp<XOR1>(d)); mx = fmaxf(mx, dpp<XOR2>(mx)); mx = quads_max(mx);
        float mn = fminf(d, dpp<XOR1>(d)); mn = fminf(mn, dpp<XOR2>(mn)); mn = -quads_max(-mn);
        const float mean = wave_sum(d) * (1.f / 64.f);
        const float val = MEAS == 1 ? mx - mn : MEAS == 2 ? mean - mn : MEAS == 3 ? (float)(int)(now32() - tstart) - mn : 0.f;
        sleep_until(now32() + (unsigned)__builtin_amdgcn_readfirstlane((int)(10.f * val)));
      }
    }
  }
}

}

extern "C" void kernel_launch(void* const* d_in, const int* in_sizes, int n_in, void* d_out, int out_size, void* d_ws, size_t ws_size,
                              hipStream_t stream) {
  (void)hipFuncSetAttribute((const void*)fused_decode_kernel, hipFuncAttributeMaxDynamicSharedMemorySize, L_END);
  fused_decode_kernel<<<NWG, NTHR, L_END, stream>>>((const u16*)d_in[0], (const u16*)d_in[1], (const u16*)d_in[2], (const u16*)d_in[3],
                                                    (const u16*)d_in[4], (const u16*)d_in[6], (const u16*)d_in[7], (u16*)d_out, (char*)d_ws);
}
```

```cpp
#include <hip/hip_runtime.h>
#include <hip/hip_bf16.h>
#include <type_traits>

namespace {

typedef unsigned short u16;
typedef short s16x4 __attribute__((ext_vector_type(4)));
typedef __bf16 bf16x8 __attribute__((ext_vector_type(8)));
typedef __bf16 bf16x2 __attribute__((ext_vector_type(2)));
typedef float f32x2 __attribute__((ext_vector_type(2)));
typedef float f32x4 __attribute__((ext_vector_type(4)));
typedef unsigned u32x2 __attribute__((ext_vector_type(2)));
typedef unsigned u32x4 __attribute__((ext_vector_type(4)));
typedef __amdgpu_buffer_rsrc_t rsrc_t;
#define LAS __attribute__((address_space(3)))

constexpr int NWG = 256, NTHR = 320;
constexpr int DM = 512, HQD = 4096, SK = 32768, DH = 128, FF = 1792;
constexpr int NSLOT = 6, AHEAD = NSLOT - 1, NTILE = 32, NUNIT = 2 * NTILE, NPRE = 11;
constexpr int WIN = 4, WIN2 = 8;
constexpr int CODE_KB = 32;
constexpr int CPA = 13, TS2 = 1430, TSD = 100, PACE_OFF = 70, OVER = 4, NSAMP = 32, POLL_AT = 170, ML_DELAY = 100;
constexpr float RBOOST = 0.99f, DELTA = 55.f;
constexpr int KV_BYTES = 2 * 8 * SK * DH * 2;
constexpr float SCALE = 0.08838834764831845f, L2E = 1.4426950408889634f;
constexpr float M_INIT = -3.0e38f;
constexpr unsigned SPIN_CAP = 1u << 18;
constexpr unsigned OOB = 0x80000000u;

constexpr int WS_EPOCH = 0;
constexpr int WS_Q = 4096;
constexpr int WS_H = WS_Q + HQD * 8;
constexpr int WS_ML = 65536;
constexpr int WS_PC = WS_ML + NWG * 128;
constexpr int WS_QP = WS_PC + NWG * 128;
constexpr int WS_HP = WS_QP + 4 * HQD * 8;
constexpr int WS_FG = WS_HP + 4 * FF * 8;
constexpr int WS_P2 = WS_FG + 14 * DM * 8;
constexpr int WS_P1 = WS_P2 + 64 * DM * 8;
constexpr int WS_END = WS_P1 + NWG * 2048;
static_assert(WS_H + FF * 8 <= WS_ML, "workspace map");

constexpr int L_RING = 0;
constexpr int L_WQ = 4 * NSLOT * 4096;
constexpr int L_SC = L_WQ;
constexpr int L_WF = L_WQ + 16384;
constexpr int L_WO = L_WF + 16384;
constexpr int L_XN = L_WO + 16384;
constexpr int L_QI = L_XN + 2048;
constexpr int L_OS = L_QI + 1024;
constexpr int L_HS = L_OS + 256;
constexpr int L_ML = L_HS + 512;
constexpr int L_TAG = L_ML + 128;
constexpr int L_QR = L_TAG + 4;
constexpr int L_TL = L_TAG + 16;
constexpr int L_TS = L_TL + 16;
constexpr int L_TP = L_TS + 16;
constexpr int L_MR = L_TP + 32;
constexpr int L_GR = L_MR + 16;
constexpr int L_WR = L_GR + 8;
constexpr int L_GS = L_GR + 16;
constexpr int L_END = L_GS + 32;

__device__ __forceinline__ float rbf(float f) { return (float)(__bf16)f; }
__device__ __forceinline__ unsigned pk2(float lo, float hi) {
  bf16x2 v = {(__bf16)lo, (__bf16)hi};
  return __builtin_bit_cast(unsigned, v);
}
__device__ __forceinline__ float bflo(unsigned u) { return __uint_as_float(u << 16); }
__device__ __forceinline__ float bfhi(unsigned u) { return __uint_as_float(u & 0xffff0000u); }
template <int CTRL> __device__ __forceinline__ float dpp(float x) {
  return __builtin_bit_cast(float, __builtin_amdgcn_mov_dpp(__builtin_bit_cast(int, x), CTRL, 0xf, 0xf, true));
}
constexpr int XOR1 = 0xB1, XOR2 = 0x4E, ROR4 = 0x124, ROR8 = 0x128;
__device__ __forceinline__ float swap16_sum(float a, float b) {
  auto s = __builtin_amdgcn_permlane16_swap(__float_as_uint(a), __float_as_uint(b), false, false);
  return __uint_as_float(s[0]) + __uint_as_float(s[1]);
}
__device__ __forceinline__ float swap32_sum(float a, float b) {
  auto s = __builtin_amdgcn_permlane32_swap(__float_as_uint(a), __float_as_uint(b), false, false);
  return __uint_as_float(s[0]) + __uint_as_float(s[1]);
}
__device__ __forceinline__ float rows_sum(float x) { x = swap16_sum(x, x); return swap32_sum(x, x); }
__device__ __forceinline__ float rows_max(float x) {
  auto s = __builtin_amdgcn_permlane16_swap(__float_as_uint(x), __float_as_uint(x), false, false);
  x = fmaxf(__uint_as_float(s[0]), __uint_as_float(s[1]));
  auto t = __builtin_amdgcn_permlane32_swap(__float_as_uint(x), __float_as_uint(x), false, false);
  return fmaxf(__uint_as_float(t[0]), __uint_as_float(t[1]));
}
__device__ __forceinline__ float quads_sum(float x) { x += dpp<ROR4>(x); x += dpp<ROR8>(x); return rows_sum(x); }
__device__ __forceinline__ float quads_max(float x) { x = fmaxf(x, dpp<ROR4>(x)); x = fmaxf(x, dpp<ROR8>(x)); return rows_max(x); }
__device__ __forceinline__ float wave_sum(float x) { x += dpp<XOR1>(x); x += dpp<XOR2>(x); return quads_sum(x); }
__device__ __forceinline__ float ex2(float x) { return __builtin_amdgcn_exp2f(x); }
__device__ __forceinline__ float silu(float v) { return v * __builtin_amdgcn_rcpf(1.f + ex2(-v * L2E)); }

template <int N> __device__ __forceinline__ void wait_vm() {
  __builtin_amdgcn_sched_barrier(0);
  asm volatile("s_waitcnt vmcnt(%0)" ::"i"(N) : "memory");
}
__device__ __forceinline__ void wait_lds() { asm volatile("s_waitcnt lgkmcnt(0)" ::: "memory"); }
__device__ __forceinline__ void wg_barrier() {
  asm volatile("s_waitcnt lgkmcnt(0)\n\ts_barrier" ::: "memory");
}
__device__ __forceinline__ void lds_wait_nonzero(unsigned addr) {
  for (unsigned spin = 0; spin < SPIN_CAP * 16 && *(volatile LAS unsigned*)(size_t)addr == 0; ++spin) __builtin_amdgcn_s_sleep(1);
  asm volatile("" ::: "memory");
}
__device__ __forceinline__ unsigned now32() { return (unsigned)__builtin_amdgcn_s_memrealtime(); }
__device__ __forceinline__ void sleep_until(unsigned due) { while ((int)(now32() - due) < 0) __builtin_amdgcn_s_sleep(2); }
__device__ __forceinline__ rsrc_t make_rsrc(const void* p, int bytes) {
  return __builtin_amdgcn_make_buffer_rsrc((void*)p, (short)0, bytes, 0x00020000);
}
constexpr int SC1 = 16;
__device__ __forceinline__ u32x4 ld_dev(rsrc_t r, unsigned voff, unsigned soff) { return __builtin_amdgcn_raw_buffer_load_b128(r, voff, soff, SC1); }
__device__ __forceinline__ void st_dev(rsrc_t r, u32x4 v, unsigned voff, unsigned soff) { __builtin_amdgcn_raw_buffer_store_b128(v, r, voff, soff, SC1); }
__device__ __forceinline__ bool pair_ok(const u32x4& g, unsigned tag) { return (g[1] == tag) & (g[3] == tag); }
__device__ __forceinline__ bool quad_ok(const u32x4& g, unsigned tag) {
  return (__builtin_amdgcn_perm(g[1], g[0], 0x0c0c0400u) | __builtin_amdgcn_perm(g[3], g[2], 0x04000c0cu)) == tag;
}
__device__ __forceinline__ float quad_val(unsigned w) { return __uint_as_float(w & 0xffffff00u); }
struct QuadOut {
  unsigned t0, t1, t2, t3, voff;
  __device__ __forceinline__ QuadOut(unsigned tag, int lane) : t0(tag & 255u), t1((tag >> 8) & 255u), t2((tag >> 16) & 255u), t3(tag >> 24), voff(lane & 1 ? OOB : lane * 8) {
    asm volatile("" : "+v"(t0), "+v"(t1), "+v"(t2), "+v"(t3), "+v"(voff));
  }
  __device__ __forceinline__ void store(rsrc_t r, float a, float b, unsigned soff) const {
    const float c = dpp<XOR1>(a), d = dpp<XOR1>(b);
    st_dev(r, u32x4{(__float_as_uint(a) & 0xffffff00u) | t0, (__float_as_uint(b) & 0xffffff00u) | t1, (__float_as_uint(c) & 0xffffff00u) | t2,
                    (__float_as_uint(d) & 0xffffff00u) | t3}, voff, soff);
  }
};
__device__ __forceinline__ float quad_sum(float v0, float v1, float v2, float v3) {
  float x = swap16_sum(swap32_sum(v0, v2), swap32_sum(v1, v3));
  x += dpp<ROR8>(x); x += dpp<ROR4>(x); x += dpp<XOR2>(x); x += dpp<XOR1>(x);
  return x;
}
__device__ __forceinline__ s16x4 tr_read(unsigned addr) { return __builtin_amdgcn_ds_read_tr16_b64_v4i16((LAS s16x4*)(size_t)addr); }
template <typename T> __device__ __forceinline__ T lds_ld(unsigned addr) { return *(const LAS T*)(size_t)addr; }
template <typename T> __device__ __forceinline__ void lds_st(unsigned addr, T v) { *(LAS T*)(size_t)addr = v; }

__device__ __forceinline__ void dma_piece(rsrc_t r, unsigned voff, unsigned soff, unsigned dst) {
  asm volatile("s_mov_b32 m0, %0\n\ts_nop 0\n\tbuffer_load_dwordx4 %1, %2, %3 offen nt lds" ::"s"(dst), "v"(voff), "s"(r), "s"(soff) : "memory");
}
__device__ __forceinline__ void dma_half(rsrc_t r, unsigned v0, unsigned v1, unsigned v2, unsigned v3, unsigned soff, unsigned dst) {
  asm volatile(
      "s_mov_b32 m0, %0\n\ts_nop 0\n\t"
      "buffer_load_dwordx4 %1, %5, %6 offen nt lds\n\t"
      "buffer_load_dwordx4 %2, %5, %6 offen offset:1024 nt lds\n\t"
      "buffer_load_dwordx4 %3, %5, %6 offen offset:2048 nt lds\n\t"
      "buffer_load_dwordx4 %4, %5, %6 offen offset:3072 nt lds"
      ::"s"(dst), "v"(v0), "v"(v1), "v"(v2), "v"(v3), "s"(r), "s"(soff)
      : "memory");
}
template <int A> __device__ __forceinline__ f32x4 mfma4(s16x4 a, s16x4 b, f32x4 c) { return __builtin_amdgcn_mfma_f32_4x4x4bf16_1k(a, b, c, 4, A, 0); }
template <int A, int N> struct Proj {
  static __device__ __forceinline__ void run(const s16x4 (&av)[2], unsigned ba, f32x4 (&c)[4]) {
    c[A & 3] = mfma4<A & 15>(av[A >> 4], tr_read(ba + A * 512), c[A & 3]);
    if constexpr (A + 1 < N) Proj<A + 1, N>::run(av, ba, c);
  }
};
template <int A> struct ProjReg {
  static __device__ __forceinline__ void run(s16x4 a, const s16x4 (&w)[16], f32x4 (&c)[2]) {
    c[A & 1] = mfma4<A>(a, w[A], c[A & 1]);
    if constexpr (A + 1 < 16) ProjReg<A + 1>::run(a, w, c);
  }
};
__device__ __forceinline__ f32x2 proj64(unsigned a_img, int a_stride, unsigned w_img, int lane) {
  const int q = (lane & 15) >> 2, p = lane & 3;
  const unsigned aa = a_img + (lane & 1) * a_stride + (lane >> 2) * 8;
  const s16x4 av[2] = {lds_ld<s16x4>(aa), lds_ld<s16x4>(aa + 128)};
  const unsigned ba = w_img + q * 128 + (((2 * (lane >> 4) + (p >> 1)) ^ ((q >> 1) << 2)) << 4) + (p & 1) * 8;
  f32x4 c[4] = {};
  Proj<0, 32>::run(av, ba, c);
  return f32x2{(c[0][0] + c[1][0]) + (c[2][0] + c[3][0]), (c[0][1] + c[1][1]) + (c[2][1] + c[3][1])};
}
__device__ __forceinline__ f32x4 gather_quarters(rsrc_t rws, unsigned base, int quarter_bytes, unsigned tag, int lane) {
  u32x4 g;
  for (unsigned spin = 0; spin < SPIN_CAP; ++spin) {
    asm volatile("" ::: "memory");
    g = ld_dev(rws, (lane >> 4) * quarter_bytes + (lane & 7) * 16, base);
    if (__all(quad_ok(g, tag))) break;
    __builtin_amdgcn_s_sleep(8);
  }
  return f32x4{rows_sum(quad_val(g[0])), rows_sum(quad_val(g[1])), rows_sum(quad_val(g[2])), rows_sum(quad_val(g[3]))};
}

template <int N> using Int = std::integral_constant<int, N>;
struct KFrag { bf16x8 k[4]; };
struct VFrag { s16x4 v[4][2]; };

__device__ __forceinline__ void read_k(KFrag& t, const unsigned (&ka)[4], unsigned off) {
#pragma unroll
  for (int s = 0; s < 4; ++s) t.k[s] = lds_ld<bf16x8>(ka[s] + off);
  asm volatile("s_waitcnt lgkmcnt(0)" : "+v"(t.k[0]), "+v"(t.k[1]), "+v"(t.k[2]), "+v"(t.k[3])::"memory");
}
__device__ __forceinline__ void read_v(VFrag& t, const unsigned (&va)[2], unsigned off) {
#pragma unroll
  for (int a = 0; a < 4; ++a)
#pragma unroll
    for (int h = 0; h < 2; ++h) t.v[a][h] = tr_read(va[h] + off + a * 1024);
  asm volatile("s_waitcnt lgkmcnt(0)"
               : "+v"(t.v[0][0]), "+v"(t.v[0][1]), "+v"(t.v[1][0]), "+v"(t.v[1][1]), "+v"(t.v[2][0]), "+v"(t.v[2][1]), "+v"(t.v[3][0]), "+v"(t.v[3][1])
               :
               : "memory");
}
__device__ __forceinline__ void score_tile(const KFrag& t, const bf16x8 (&qf)[4], float& m, unsigned dst, bool writer) {
  f32x4 sc = {0.f, 0.f, 0.f, 0.f};
#pragma unroll
  for (int s = 0; s < 4; ++s) sc = __builtin_amdgcn_mfma_f32_16x16x32_bf16(t.k[s], qf[s], sc, 0, 0, 0);
  m = fmaxf(fmaxf(m, fmaxf(sc[0], sc[1])), fmaxf(sc[2], sc[3]));
  const unsigned r0 = pk2(sc[0], sc[1]), r1 = pk2(sc[2], sc[3]);
  if (writer) lds_st<u32x2>(dst, u32x2{pk2(bflo(r0) * SCALE, bfhi(r0) * SCALE), pk2(bflo(r1) * SCALE, bfhi(r1) * SCALE)});
}
__device__ __forceinline__ s16x4 probs(unsigned src, float M, float f) {
  const u32x2 xp = lds_ld<u32x2>(src);
  const u32x2 pp = {pk2(f * ex2((bflo(xp[0]) - M) * L2E), f * ex2((bfhi(xp[0]) - M) * L2E)), pk2(f * ex2((bflo(xp[1]) - M) * L2E), f * ex2((bfhi(xp[1]) - M) * L2E))};
  return __builtin_bit_cast(s16x4, pp);
}
__device__ __forceinline__ void value_tile(const VFrag& t, s16x4 pa, f32x4 (&o)[2]) {
#pragma unroll
  for (int h = 0; h < 2; ++h) o[h] = mfma4<0>(pa, t.v[0][h], o[h]);
#pragma unroll
  for (int h = 0; h < 2; ++h) o[h] = mfma4<4>(pa, t.v[1][h], o[h]);
#pragma unroll
  for (int h = 0; h < 2; ++h) o[h] = mfma4<8>(pa, t.v[2][h], o[h]);
#pragma unroll
  for (int h = 0; h < 2; ++h) o[h] = mfma4<12>(pa, t.v[3][h], o[h]);
}

__global__ void __launch_bounds__(NTHR, 2)
fused_decode_kernel(const u16* __restrict__ x, const u16* __restrict__ kc, const u16* __restrict__ vc, const u16* __restrict__ wq,
                    const u16* __restrict__ wo, const u16* __restrict__ w1, const u16* __restrict__ w2, u16* __restrict__ out, char* ws) {
  unsigned long long pc;
  asm volatile("s_getpc_b64 %0" : "=s"(pc));
  extern __shared__ __attribute__((aligned(16))) unsigned char lds[];
  const unsigned lds0 = (unsigned)(size_t)(LAS unsigned char*)lds;
  const int lane = threadIdx.x & 63, wave = __builtin_amdgcn_readfirstlane(threadIdx.x >> 6), wg = blockIdx.x;
  const int xg = wg & 7, xi = wg >> 3;
  asm volatile("" ::"s"(x), "s"(kc), "s"(vc), "s"(wq), "s"(wo), "s"(w1), "s"(w2), "s"(out), "s"(ws));
  const rsrc_t rws = make_rsrc(ws, WS_END);
  const unsigned sw128 = ((lane & 7) ^ (((lane >> 4) & 1) << 2)) << 4;

  if (wave < 4) {
    const rsrc_t rk = make_rsrc(kc, KV_BYTES), rv = make_rsrc(vc, KV_BYTES);
    const int g = lane >> 4, c = lane & 15;
    unsigned vk[4];
#pragma unroll
    for (int u = 0; u < 4; ++u) vk[u] = g * 256 + ((c ^ (4 * u + g)) << 4);
    const unsigned vv = g * 256 + ((c ^ (g << 2)) << 4);
    const unsigned ring = lds0 + L_RING + wave * (NSLOT * 4096);
    const unsigned tile0 = (unsigned)wg * (2048 * 256) + wave * 4096;
    unsigned ka[4], va[2];
#pragma unroll
    for (int s = 0; s < 4; ++s) ka[s] = ring + c * 256 + (((4 * s + g) ^ c) << 4);
#pragma unroll
    for (int h = 0; h < 2; ++h) va[h] = ring + (c >> 2) * 256 + (((8 * h + 2 * g + ((c & 3) >> 1)) ^ ((c >> 2) << 2)) << 4) + (c & 1) * 8;
    const unsigned sca = lds0 + L_SC + wave * 4096 + (g * 4 + (c & 3)) * 8;
    auto issue_k = [&](int t, unsigned off) { dma_half(rk, vk[0], vk[1], vk[2], vk[3], tile0 + t * 16384, ring + off); };
    auto issue_v = [&](int t, unsigned off) { dma_half(rv, vv, vv, vv, vv, tile0 + t * 16384, ring + off); };

    wg_barrier();
#pragma unroll
    for (int u = 0; u < AHEAD; ++u) { wait_vm<WIN>(); issue_k(u, u * 4096); }
    KFrag pk[NPRE];
#pragma unroll
    for (int u = 0; u < NPRE; ++u) {
      wait_vm<WIN>();
      issue_k(u + AHEAD, ((u + AHEAD) % NSLOT) * 4096);
      read_k(pk[u], ka, (u % NSLOT) * 4096);
    }
    lds_wait_nonzero(lds0 + L_QR);
    bf16x8 qf[4];
#pragma unroll
    for (int s = 0; s < 4; ++s) qf[s] = lds_ld<bf16x8>(lds0 + L_QI + (c & 3) * 256 + (4 * s + g) * 16);
    const unsigned tag = lds_ld<unsigned>(lds0 + L_TAG);

    unsigned prev = now32();
    auto landed = [&](int u, auto win) {
      const u32x2 plan = *(volatile LAS u32x2*)(size_t)(lds0 + L_TL);
      wait_vm<win.value>();
      unsigned tn = now32();
      *(volatile LAS u32x2*)(size_t)(lds0 + L_TP + wave * 8) = u32x2{(unsigned)u, tn};
      if (u == CPA) *(volatile LAS unsigned*)(size_t)(lds0 + L_TS + wave * 4) = tn | 1u;
      const unsigned tl = __builtin_amdgcn_readfirstlane(plan[0]);
      const int d = (int)(tl - prev);
      if (tl != 0 && d > 0) {
        const float n = (float)(NUNIT - AHEAD - u);
        const bool behind = (float)d + DELTA < n * __uint_as_float(plan[1]);
        const unsigned due = prev + (unsigned)__builtin_amdgcn_readfirstlane(behind ? 0 : (int)((float)d * __builtin_amdgcn_rcpf(n)));
        while ((int)(tn - due) < 0) { __builtin_amdgcn_s_sleep(1); tn = now32(); }
      }
      prev = tn + OVER;
    };
    static_assert(WIN <= 16 && WIN2 <= 16 && 2 * NPRE <= NTILE - AHEAD && CPA >= NPRE, "unit schedule");
    float m = M_INIT;
    KFrag kf;
#pragma unroll
    for (int u = NPRE; u < 2 * NPRE; ++u) {
      landed(u, Int<WIN>{}); issue_k(u + AHEAD, ((u + AHEAD) % NSLOT) * 4096);
      read_k(kf, ka, (u % NSLOT) * 4096);
      score_tile(kf, qf, m, sca + u * 128, c < 4);
      score_tile(pk[u - NPRE], qf, m, sca + (u - NPRE) * 128, c < 4);
    }
    unsigned off = ((2 * NPRE) % NSLOT) * 4096, offp = ((2 * NPRE + AHEAD) % NSLOT) * 4096;
    auto next_slot = [&]() { offp = off; off = off == (NSLOT - 1) * 4096 ? 0u : off + 4096; };
#pragma unroll 1
    for (int u = 2 * NPRE; u < NTILE - AHEAD; ++u) {
      landed(u, Int<WIN>{}); issue_k(u + AHEAD, offp);
      read_k(kf, ka, off);
      score_tile(kf, qf, m, sca + u * 128, c < 4);
      next_slot();
    }
#pragma unroll 1
    for (int u = NTILE - AHEAD; u < NTILE; ++u) {
      landed(u, Int<WIN2>{}); issue_v(u + AHEAD - NTILE, offp);
      read_k(kf, ka, off);
      score_tile(kf, qf, m, sca + u * 128, c < 4);
      next_slot();
    }
    const float M = rbf(rbf(rows_max(m)) * SCALE);
    if (lane < 4) lds_st<float>(lds0 + L_ML + lane * 16 + wave * 4, M);
    wait_lds();
    if (lane == 0) *(volatile LAS unsigned*)(size_t)(lds0 + L_MR + wave * 4) = 1u;
    f32x4 o[2] = {};
    VFrag vf;
#pragma unroll 1
    for (int u = NTILE; u < NUNIT - AHEAD; ++u) {
      landed(u, Int<WIN2>{}); issue_v(u + AHEAD - NTILE, offp);
      const s16x4 pa = probs(sca + (u - NTILE) * 128, M, 1.f);
      read_v(vf, va, off);
      value_tile(vf, pa, o);
      next_slot();
    }
    const int ks = xg * 8 + (xi >> 2), cs = xi & 3;
    if (wave < 2) {
      const rsrc_t ro = make_rsrc(wo, HQD * DM * 2);
#pragma unroll
      for (int i = 0; i < 8; ++i)
        dma_piece(ro, (lane >> 3) * (DM * 2) + sw128, (ks * 64 + 8 * i) * (DM * 2) + cs * 256 + wave * 128, lds0 + L_WO + wave * 8192 + i * 1024);
    }
    const bool late_w2 = wave >= 2 && xi >= 14 && xi < 28;
    if (late_w2) {
      const int fi = xg * 14 + xi - 14;
      const rsrc_t r2 = make_rsrc(w2, DM * FF * 2);
#pragma unroll
      for (int i = 0; i < 8; ++i)
        dma_piece(r2, (lane >> 3) * (DM * 2) + sw128, ((fi >> 3) * 128 + (wave & 1) * 64 + 8 * i) * (DM * 2) + (fi & 7) * 128, lds0 + L_WF + (wave & 1) * 8192 + i * 1024);
    }
    lds_wait_nonzero(lds0 + L_GR);
    const f32x2 gs = lds_ld<f32x2>(lds0 + L_GS + (c & 3) * 8);
    const float f = ex2((M - gs[0]) * L2E) * gs[1];
    {
      const float f0 = dpp<0x00>(f), f1 = dpp<0x55>(f), f2 = dpp<0xAA>(f), f3 = dpp<0xFF>(f);
#pragma unroll
      for (int h = 0; h < 2; ++h) { o[h][0] *= f0; o[h][1] *= f1; o[h][2] *= f2; o[h][3] *= f3; }
    }
    const QuadOut qo(tag, lane);
    unsigned s_p1 = WS_P1 + wg * 2048 + wave * 512, s_p2 = WS_P2 + ks * (DM * 8) + (cs * 128 + wave * 64) * 8;
    unsigned a_mw = ring + lane * 4, a_mr = lds0 + L_RING + (wave * 128 + 2 * lane) * 4;
    unsigned a_ow = lds0 + L_OS + (wave >> 1) * 128 + ((wave & 1) * 32 + 4 * (lane & 7) + g) * 2, a_or = lds0 + L_OS + (lane & 1) * 128 + (lane >> 2) * 8;
    asm volatile("" : "+s"(s_p1), "+s"(s_p2), "+v"(a_mw), "+v"(a_mr), "+v"(a_ow), "+v"(a_or));
    auto last = [&](auto j) {
      constexpr int u = NUNIT - AHEAD + j.value, younger = (NUNIT - 1 - u) * 4;
      s16x4 pa = probs(sca + (u - NTILE) * 128, M, f);
      asm volatile("" : "+v"(pa));
      if (wave < 2 || late_w2) wait_vm<younger + 8>(); else wait_vm<younger>();
      read_v(vf, va, (u % NSLOT) * 4096);
      value_tile(vf, pa, o);
    };
    last(Int<0>{}); last(Int<1>{}); last(Int<2>{}); last(Int<3>{}); last(Int<4>{});

#pragma unroll
    for (int h = 0; h < 2; ++h)
#pragma unroll
      for (int r = 0; r < 4; ++r) lds_st<float>(a_mw + (r * 128 + h * 64) * 4, o[h][r]);
    wg_barrier();
    {
      f32x2 ov[4];
#pragma unroll
      for (int w = 0; w < 4; ++w) ov[w] = lds_ld<f32x2>(a_mr + w * (NSLOT * 4096));
      const f32x2 sum = (ov[0] + ov[1]) + (ov[2] + ov[3]);
      qo.store(rws, sum[0], sum[1], s_p1);
    }
    s16x4 wf[16];
    if (wave < 2) {
      const unsigned wa = lds0 + L_WO + wave * 8192 + (c >> 2) * 128 + (((2 * g + ((c & 3) >> 1)) ^ ((c >> 3) << 2)) << 4) + (c & 1) * 8;
      wait_vm<1>();
#pragma unroll
      for (int a = 0; a < 16; ++a) wf[a] = tr_read(wa + a * 512);
    }
    if (late_w2) {
      wait_vm<1>();
      if (lane == 0) *(volatile LAS unsigned*)(size_t)(lds0 + L_WR + (wave & 1) * 4) = 1u;
    }
    {
      const int b = wave >> 1, half = wave & 1;
      const unsigned vo = (lane >> 3) * 2048 + (lane & 7) * 16;
      const unsigned so = WS_P1 + (b * 8 + xg) * 16 * 2048 + ((ks >> 1) & 3) * 512 + ((ks & 1) * 2 + half) * 128;
      sleep_until(__builtin_amdgcn_readfirstlane(*(volatile LAS unsigned*)(size_t)(lds0 + L_TL)) + POLL_AT);
      u32x4 gv[2] = {};
      bool h0 = false, h1 = false;
      for (unsigned spin = 0; spin < SPIN_CAP; ++spin) {
        asm volatile("" ::: "memory");
        if (!h0) gv[0] = ld_dev(rws, vo, so);
        if (!h1) gv[1] = ld_dev(rws, vo + 8 * 2048, so);
        h0 = quad_ok(gv[0], tag); h1 = quad_ok(gv[1], tag);
        if (__all(h0 & h1)) break;
      }
      float u[4];
#pragma unroll
      for (int j = 0; j < 4; ++j) u[j] = quad_val(gv[0][j]) + quad_val(gv[1][j]);
      float e = swap16_sum(swap32_sum(u[0], u[2]), swap32_sum(u[1], u[3]));
      e += dpp<ROR8>(e);
      lds_st<__bf16>(a_ow, (__bf16)e);
    }
    wg_barrier();
    if (wave < 2) {
      const s16x4 oa = lds_ld<s16x4>(a_or);
      f32x4 acc[2] = {};
      ProjReg<0>::run(oa, wf, acc);
      qo.store(rws, acc[0][0] + acc[1][0], acc[0][1] + acc[1][1], s_p2);
    }
  } else {
    __builtin_amdgcn_s_setprio(3);
    const int cg = xg * 32 + xi;
    const int qk = cg >> 6, qc = cg & 63;
    const bool f1 = xi < 14, f2 = xi >= 14 && xi < 28;
    const int fi = xg * 14 + (xi < 14 ? xi : xi - 14);
    const int hk = fi / 28, hc = fi % 28;
    const int gk = fi >> 3, gc = fi & 7;
    const unsigned vout = (((lane >> 4) & 1) * DM + cg * 2 + (lane >> 5)) * 2;
    const rsrc_t rx = make_rsrc(x, 2 * DM * 2);
    unsigned ev, xo, tv, to;
    u32x4 x0, x1, code;
    asm volatile(
        "s_nop 4\n\t"
        "buffer_load_dwordx4 %0, %7, %8, 0 offen\n\t"
        "buffer_load_dwordx4 %1, %7, %8, 0 offen offset:1024\n\t"
        "buffer_load_dword %2, off, %9, 0 sc1\n\t"
        "buffer_load_ushort %3, %10, %8, 0 offen\n\t"
        "buffer_load_dwordx4 %4, %7, %11, %12 offen\n\t"
        "buffer_load_dword %5, off, %13, %14 nt\n\t"
        "buffer_load_dword %6, off, %15, %16 nt"
        : "=&v"(x0), "=&v"(x1), "=&v"(ev), "=&v"(xo), "=&v"(code), "=&v"(tv), "=&v"(to)
        : "v"(lane * 16), "s"(rx), "s"(rws), "v"(vout), "s"(make_rsrc((const void*)(pc & ~255ull), CODE_KB * 1024)), "s"(xi * 1024),
          "s"(make_rsrc(vc, KV_BYTES)), "s"(wg * (2048 * 256)), "s"(make_rsrc(wo, HQD * DM * 2)), "s"((xg * 8 + (xi >> 2)) * 64 * (DM * 2))
        : "memory");
    {
      const rsrc_t rq = make_rsrc(wq, DM * HQD * 2);
#pragma unroll
      for (int i = 0; i < 16; ++i) dma_piece(rq, (lane >> 3) * (HQD * 2) + sw128, (qk * 128 + 8 * i) * (HQD * 2) + qc * 128, lds0 + L_WQ + i * 1024);
      __builtin_amdgcn_sched_barrier(0);
      if (f1) {
        const rsrc_t r1 = make_rsrc(w1, DM * FF * 2);
#pragma unroll
        for (int i = 0; i < 16; ++i) dma_piece(r1, (lane >> 3) * (FF * 2) + sw128, (hk * 128 + 8 * i) * (FF * 2) + hc * 128, lds0 + L_WF + i * 1024);
      }
    }
    if (lane < 28) lds_st<unsigned>(lds0 + L_TAG + lane * 4, 0u);
    wg_barrier();
    const unsigned th0 = now32();
    if (f1) wait_vm<32>(); else wait_vm<16>();
    asm volatile("" : "+v"(x0), "+v"(x1), "+v"(ev), "+v"(xo), "+v"(code), "+v"(tv), "+v"(to)::"memory");
    const unsigned epoch = max(__builtin_amdgcn_readfirstlane(ev) + 1u, 1u), tag = epoch * 0x9E3779B1u;
    {
      float a[2][8];
#pragma unroll
      for (int j = 0; j < 4; ++j) { a[0][2 * j] = bflo(x0[j]); a[0][2 * j + 1] = bfhi(x0[j]); a[1][2 * j] = bflo(x1[j]); a[1][2 * j + 1] = bfhi(x1[j]); }
#pragma unroll
      for (int b = 0; b < 2; ++b) {
        const float mean = wave_sum(((a[b][0] + a[b][1]) + (a[b][2] + a[b][3])) + ((a[b][4] + a[b][5]) + (a[b][6] + a[b][7]))) * (1.f / DM);
        float q = 0.f;
#pragma unroll
        for (int j = 0; j < 8; ++j) q = fmaf(a[b][j] - mean, a[b][j] - mean, q);
        const float var = wave_sum(q) * (1.f / DM);
        const float mb = rbf(mean), inv = __builtin_amdgcn_rcpf(rbf(__builtin_amdgcn_sqrtf(rbf(rbf(var) + 1e-5f))));
        u32x4 xn;
#pragma unroll
        for (int j = 0; j < 4; ++j) xn[j] = pk2(rbf(a[b][2 * j] - mb) * inv, rbf(a[b][2 * j + 1] - mb) * inv);
        lds_st<u32x4>(lds0 + L_XN + b * 1024 + lane * 16, xn);
      }
    }
    if (lane == 0) lds_st<unsigned>(lds0 + L_TAG, tag);
    const QuadOut qo(tag, lane);
    if (f1) wait_vm<16>(); else wait_vm<0>();
    wait_lds();
    {
      const f32x2 y = proj64(lds0 + L_XN + qk * 256, 1024, lds0 + L_WQ, lane);
      qo.store(rws, y[0], y[1], WS_QP + (qk * HQD + qc * 64) * 8);
    }
    if (f1) {
      wait_vm<1>();
      const f32x2 y = proj64(lds0 + L_XN + hk * 256, 1024, lds0 + L_WF, lane);
      qo.store(rws, y[0], y[1], WS_HP + (hk * FF + hc * 64) * 8);
    }
    {
      const f32x4 q = gather_quarters(rws, WS_QP + (qc * 64 + qk * 16) * 8, HQD * 8, tag, lane);
      if (lane < 8) st_dev(rws, u32x4{pk2(q[0], q[1]), tag, pk2(q[2], q[3]), tag}, lane * 16, WS_Q + (qc * 64 + qk * 16) * 8);
    }
    if (f1) {
      const f32x4 h = gather_quarters(rws, WS_HP + (hc * 64 + hk * 16) * 8, FF * 8, tag, lane);
      if (lane < 8) st_dev(rws, u32x4{pk2(silu(rbf(h[0])), silu(rbf(h[1]))), tag, pk2(silu(rbf(h[2])), silu(rbf(h[3]))), tag}, lane * 16, WS_H + (hc * 64 + hk * 16) * 8);
    }
    {
      const int b = wg >> 7, kvh = (wg >> 4) & 7;
      u32x4 gq[4];
      for (unsigned spin = 0; spin < SPIN_CAP; ++spin) {
        asm volatile("" ::: "memory");
#pragma unroll
        for (int i = 0; i < 4; ++i) gq[i] = ld_dev(rws, lane * 16 + i * 1024, WS_Q + kvh * 4096);
        if (__all(pair_ok(gq[0], tag) & pair_ok(gq[1], tag) & pair_ok(gq[2], tag) & pair_ok(gq[3], tag))) break;
        __builtin_amdgcn_s_sleep(8);
      }
      const unsigned sel = b ? 0x07060302u : 0x05040100u;
#pragma unroll
      for (int i = 0; i < 4; ++i) lds_st<unsigned>(lds0 + L_QI + i * 256 + lane * 4, __builtin_amdgcn_perm(gq[i][2], gq[i][0], sel));
    }
    wait_lds();
    if (lane == 0) *(volatile LAS unsigned*)(size_t)(lds0 + L_QR) = 1u;

    if (f2) {
      u32x4 gh;
      for (unsigned spin = 0; spin < SPIN_CAP; ++spin) {
        asm volatile("" ::: "memory");
        gh = ld_dev(rws, lane * 16, WS_H + gk * 1024);
        if (__all(pair_ok(gh, tag))) break;
        __builtin_amdgcn_s_sleep(32);
      }
      lds_st<unsigned>(lds0 + L_HS + lane * 4, __builtin_amdgcn_perm(gh[2], gh[0], 0x05040100u));
      lds_st<unsigned>(lds0 + L_HS + 256 + lane * 4, __builtin_amdgcn_perm(gh[2], gh[0], 0x07060302u));
    }
    {
      sleep_until(th0 + TS2);
      u32x4 ta;
      for (unsigned spin = 0; spin < SPIN_CAP; ++spin) {
        ta = *(volatile LAS u32x4*)(size_t)(lds0 + L_TS);
        if (__all((ta[0] != 0) & (ta[1] != 0) & (ta[2] != 0) & (ta[3] != 0))) break;
        __builtin_amdgcn_s_sleep(8);
      }
      const u32x4 p0 = *(volatile LAS u32x4*)(size_t)(lds0 + L_TP), p1 = *(volatile LAS u32x4*)(size_t)(lds0 + L_TP + 16);
      {
        const unsigned a = th0 + (((ta[0] - th0) + (ta[1] - th0) + (ta[2] - th0) + (ta[3] - th0)) >> 2);
        const unsigned b = th0 + (((p0[1] - th0) + (p0[3] - th0) + (p1[1] - th0) + (p1[3] - th0)) >> 2);
        const unsigned c4 = p0[0] + p0[2] + p1[0] + p1[2];
        if (lane == 0) st_dev(rws, u32x4{a, tag, (b << 8) | c4, tag}, 0, WS_PC + wg * 128);
      }
      sleep_until(now32() + TSD);
      u32x4 gp;
      for (unsigned spin = 0; spin < SPIN_CAP; ++spin) {
        asm volatile("" ::: "memory");
        gp = ld_dev(rws, (lane & (NSAMP - 1)) * 128, WS_PC + (wg & ~(NSAMP - 1)) * 128);
        if (__all(pair_ok(gp, tag))) break;
        __builtin_amdgcn_s_sleep(24);
      }
      const float tb = (float)((int)((gp[2] & 0xffffff00u) - (th0 << 8)) >> 8), ta_ = (float)(int)(gp[0] - th0);
      const float cb = 0.25f * (float)(gp[2] & 0xffu);
      const float r = fmaxf(cb - (float)CPA, 0.25f) * __builtin_amdgcn_rcpf(fmaxf(tb - ta_, 25.f)) * RBOOST;
      const float inv = __builtin_amdgcn_rcpf(wave_sum(r));
      const float fin = fminf(fmaxf(wave_sum((float)(NUNIT - 4) - cb + r * tb) * inv, 0.f), 8000.f);
      if (lane == 0) *(volatile LAS u32x2*)(size_t)(lds0 + L_TL) = u32x2{(th0 + (unsigned)((int)fin - PACE_OFF)) | 1u, __float_as_uint(64.f * inv)};
    }
    {
      for (unsigned spin = 0; spin < SPIN_CAP * 4; ++spin) {
        const u32x4 mr = *(volatile LAS u32x4*)(size_t)(lds0 + L_MR);
        if (__all((mr[0] & mr[1] & mr[2] & mr[3]) != 0)) break;
        __builtin_amdgcn_s_sleep(8);
      }
      asm volatile("" ::: "memory");
      __builtin_amdgcn_s_setprio(0);
      const f32x4 ma = lds_ld<f32x4>(lds0 + L_ML + (lane & 1) * 32), mb = lds_ld<f32x4>(lds0 + L_ML + (lane & 1) * 32 + 16);
      const float Ma = fmaxf(fmaxf(ma[0], ma[1]), fmaxf(ma[2], ma[3])), Mb = fmaxf(fmaxf(mb[0], mb[1]), fmaxf(mb[2], mb[3]));
      float La = 0.f, Lb = 0.f;
#pragma unroll 4
      for (int i = 0; i < 16; ++i) {
        const u32x4 xp = lds_ld<u32x4>(lds0 + L_SC + i * 1024 + lane * 16);
        La += (ex2((bflo(xp[0]) - Ma) * L2E) + ex2((bfhi(xp[0]) - Ma) * L2E)) + (ex2((bflo(xp[1]) - Ma) * L2E) + ex2((bfhi(xp[1]) - Ma) * L2E));
        Lb += (ex2((bflo(xp[2]) - Mb) * L2E) + ex2((bfhi(xp[2]) - Mb) * L2E)) + (ex2((bflo(xp[3]) - Mb) * L2E) + ex2((bfhi(xp[3]) - Mb) * L2E));
      }
      La += dpp<XOR2>(La); Lb += dpp<XOR2>(Lb);
      La = quads_sum(La); Lb = quads_sum(Lb);
      __builtin_amdgcn_s_setprio(3);
      if (lane < 4) st_dev(rws, u32x4{__float_as_uint(lane & 2 ? Mb : Ma), tag, __float_as_uint(lane & 2 ? Lb : La), tag}, ((lane & 1) * 2 + (lane >> 1)) * 16, WS_ML + wg * 128);
      sleep_until(now32() + ML_DELAY);
      u32x4 gm;
      for (unsigned spin = 0; spin < SPIN_CAP; ++spin) {
        asm volatile("" ::: "memory");
        gm = ld_dev(rws, (lane >> 2) * 128 + (lane & 3) * 16, WS_ML + (wg & ~15) * 128);
        if (__all(pair_ok(gm, tag))) break;
        __builtin_amdgcn_s_sleep(32);
      }
      const float ms = __uint_as_float(gm[0]), Mg = quads_max(ms);
      const float inv = __builtin_amdgcn_rcpf(quads_sum(ex2((ms - Mg) * L2E) * __uint_as_float(gm[2])));
      if (lane < 4) lds_st<f32x2>(lds0 + L_GS + lane * 8, f32x2{Mg, inv});
      wait_lds();
      if (lane == 0) *(volatile LAS unsigned*)(size_t)(lds0 + L_GR) = 1u;
    }
    wg_barrier();
    u32x4 gs = ld_dev(rws, lane * (DM * 8), WS_P2 + cg * 16);
    if (f2) {
      lds_wait_nonzero(lds0 + L_WR); lds_wait_nonzero(lds0 + L_WR + 4);
      const f32x2 y = proj64(lds0 + L_HS, 256, lds0 + L_WF, lane);
      qo.store(rws, y[0], y[1], WS_FG + (gk * DM + gc * 64) * 8);
    }
    float xr = bflo(xo);
    unsigned vst = (lane & 15) == 0 ? vout : OOB;
    const rsrc_t rout = make_rsrc(out, 2 * DM * 2);
    asm volatile("" : "+v"(gs), "+v"(xr), "+v"(vst) : "s"(rout));
    wg_barrier();
    float gate;
    {
      u32x4 gf;
      for (unsigned spin = 0; spin < SPIN_CAP; ++spin) {
        asm volatile("" ::: "memory");
        gf = ld_dev(rws, (lane < 14 ? lane : 0) * (DM * 8), WS_FG + cg * 16);
        if (__all(quad_ok(gf, tag))) break;
      }
      if (lane >= 14) gf = u32x4{0u, 0u, 0u, 0u};
      gate = rbf(silu(rbf(quad_sum(quad_val(gf[0]), quad_val(gf[1]), quad_val(gf[2]), quad_val(gf[3])))));
      asm volatile("" : "+v"(gate));
    }
    {
      bool have = false;
      for (unsigned spin = 0; spin < SPIN_CAP; ++spin) {
        asm volatile("" ::: "memory");
        if (!have) gs = ld_dev(rws, lane * (DM * 8), WS_P2 + cg * 16);
        have = quad_ok(gs, tag);
        if (__all(have)) break;
      }
      const float s = rbf(quad_sum(quad_val(gs[0]), quad_val(gs[1]), quad_val(gs[2]), quad_val(gs[3])));
      __builtin_amdgcn_raw_buffer_store_b16(__builtin_bit_cast(u16, (__bf16)(xr + rbf(s * gate))), rout, vst, 0, 0);
      if (wg == 0 && lane == 0) __builtin_amdgcn_raw_buffer_store_b32(epoch, rws, 0, WS_EPOCH, SC1);
    }
  }
}

}

extern "C" void kernel_launch(void* const* d_in, const int* in_sizes, int n_in, void* d_out, int out_size, void* d_ws, size_t ws_size,
                              hipStream_t stream) {
  (void)hipFuncSetAttribute((const void*)fused_decode_kernel, hipFuncAttributeMaxDynamicSharedMemorySize, L_END);
  fused_decode_kernel<<<NWG, NTHR, L_END, stream>>>((const u16*)d_in[0], (const u16*)d_in[1], (const u16*)d_in[2], (const u16*)d_in[3],
                                                    (const u16*)d_in[4], (const u16*)d_in[6], (const u16*)d_in[7], (u16*)d_out, (char*)d_ws);
}
```

```cpp
#include <hip/hip_runtime.h>
#include <hip/hip_bf16.h>
#include <type_traits>

namespace {

typedef unsigned short u16;
typedef short s16x4 __attribute__((ext_vector_type(4)));
typedef __bf16 bf16x8 __attribute__((ext_vector_type(8)));
typedef __bf16 bf16x2 __attribute__((ext_vector_type(2)));
typedef float f32x2 __attribute__((ext_vector_type(2)));
typedef float f32x4 __attribute__((ext_vector_type(4)));
typedef unsigned u32x2 __attribute__((ext_vector_type(2)));
typedef unsigned u32x4 __attribute__((ext_vector_type(4)));
typedef __amdgpu_buffer_rsrc_t rsrc_t;
#define LAS __attribute__((address_space(3)))

constexpr int NWG = 256, NTHR = 320;
constexpr int DM = 512, HQD = 4096, SK = 32768, DH = 128, FF = 1792;
constexpr int NSLOT = 6, AHEAD = NSLOT - 1, NTILE = 32, NUNIT = 2 * NTILE, NPRE = 11;
constexpr int WIN = 4, WIN2 = 8;
constexpr int CODE_KB = 32;
constexpr int CPA = 13, TS2 = 1430, TSD = 100, PACE_OFF = 78, OVER = 4, NSAMP = 32, POLL_AT = 170, ML_DELAY = 100;
constexpr float RBOOST = 0.99f, DELTA = 55.f;
constexpr int KV_BYTES = 2 * 8 * SK * DH * 2;
constexpr float SCALE = 0.08838834764831845f, L2E = 1.4426950408889634f;
constexpr float M_INIT = -3.0e38f;
constexpr unsigned SPIN_CAP = 1u << 18;
constexpr unsigned OOB = 0x80000000u;

constexpr int WS_EPOCH = 0;
constexpr int WS_Q = 4096;
constexpr int WS_H = WS_Q + HQD * 8;
constexpr int WS_ML = 65536;
constexpr int WS_PC = WS_ML + NWG * 128;
constexpr int WS_QP = WS_PC + NWG * 128;
constexpr int WS_HP = WS_QP + 4 * HQD * 8;
constexpr int WS_FG = WS_HP + 4 * FF * 8;
constexpr int WS_P2 = WS_FG + 14 * DM * 8;
constexpr int WS_P1 = WS_P2 + 64 * DM * 8;
constexpr int WS_END = WS_P1 + NWG * 2048;
static_assert(WS_H + FF * 8 <= WS_ML, "workspace map");

constexpr int L_RING = 0;
constexpr int L_WQ = 4 * NSLOT * 4096;
constexpr int L_SC = L_WQ;
constexpr int L_WF = L_WQ + 16384;
constexpr int L_WO = L_WF + 16384;
constexpr int L_XN = L_WO + 16384;
constexpr int L_QI = L_XN + 2048;
constexpr int L_OS = L_QI + 1024;
constexpr int L_HS = L_OS + 256;
constexpr int L_ML = L_HS + 512;
constexpr int L_TAG = L_ML + 128;
constexpr int L_QR = L_TAG + 4;
constexpr int L_TL = L_TAG + 16;
constexpr int L_TS = L_TL + 16;
constexpr int L_TP = L_TS + 16;
constexpr int L_MR = L_TP + 32;
constexpr int L_GR = L_MR + 16;
constexpr int L_WR = L_GR + 8;
constexpr int L_GS = L_GR + 16;
constexpr int L_END = L_GS + 32;
static_assert(L_GS - L_TAG == 28 * 4 && L_END <= 160 * 1024, "LDS map");

__device__ __forceinline__ float rbf(float f) { return (float)(__bf16)f; }
__device__ __forceinline__ unsigned pk2(float lo, float hi) {
  bf16x2 v = {(__bf16)lo, (__bf16)hi};
  return __builtin_bit_cast(unsigned, v);
}
__device__ __forceinline__ float bflo(unsigned u) { return __uint_as_float(u << 16); }
__device__ __forceinline__ float bfhi(unsigned u) { return __uint_as_float(u & 0xffff0000u); }
template <int CTRL> __device__ __forceinline__ float dpp(float x) {
  return __builtin_bit_cast(float, __builtin_amdgcn_mov_dpp(__builtin_bit_cast(int, x), CTRL, 0xf, 0xf, true));
}
constexpr int XOR1 = 0xB1, XOR2 = 0x4E, ROR4 = 0x124, ROR8 = 0x128;
__device__ __forceinline__ float swap16_sum(float a, float b) {
  auto s = __builtin_amdgcn_permlane16_swap(__float_as_uint(a), __float_as_uint(b), false, false);
  return __uint_as_float(s[0]) + __uint_as_float(s[1]);
}
__device__ __forceinline__ float swap32_sum(float a, float b) {
  auto s = __builtin_amdgcn_permlane32_swap(__float_as_uint(a), __float_as_uint(b), false, false);
  return __uint_as_float(s[0]) + __uint_as_float(s[1]);
}
__device__ __forceinline__ float rows_sum(float x) { x = swap16_sum(x, x); return swap32_sum(x, x); }
__device__ __forceinline__ float rows_max(float x) {
  auto s = __builtin_amdgcn_permlane16_swap(__float_as_uint(x), __float_as_uint(x), false, false);
  x = fmaxf(__uint_as_float(s[0]), __uint_as_float(s[1]));
  auto t = __builtin_amdgcn_permlane32_swap(__float_as_uint(x), __float_as_uint(x), false, false);
  return fmaxf(__uint_as_float(t[0]), __uint_as_float(t[1]));
}
__device__ __forceinline__ float quads_sum(float x) { x += dpp<ROR4>(x); x += dpp<ROR8>(x); return rows_sum(x); }
__device__ __forceinline__ float quads_max(float x) { x = fmaxf(x, dpp<ROR4>(x)); x = fmaxf(x, dpp<ROR8>(x)); return rows_max(x); }
__device__ __forceinline__ float wave_sum(float x) { x += dpp<XOR1>(x); x += dpp<XOR2>(x); return quads_sum(x); }
__device__ __forceinline__ float ex2(float x) { return __builtin_amdgcn_exp2f(x); }
__device__ __forceinline__ float silu(float v) { return v * __builtin_amdgcn_rcpf(1.f + ex2(-v * L2E)); }

template <int N> __device__ __forceinline__ void wait_vm() {
  __builtin_amdgcn_sched_barrier(0);
  asm volatile("s_waitcnt vmcnt(%0)" ::"i"(N) : "memory");
}
__device__ __forceinline__ void wait_lds() { asm volatile("s_waitcnt lgkmcnt(0)" ::: "memory"); }
__device__ __forceinline__ void wg_barrier() {
  asm volatile("s_waitcnt lgkmcnt(0)\n\ts_barrier" ::: "memory");
}
__device__ __forceinline__ void lds_wait_nonzero(unsigned addr) {
  for (unsigned spin = 0; spin < SPIN_CAP * 16 && *(volatile LAS unsigned*)(size_t)addr == 0; ++spin) __builtin_amdgcn_s_sleep(1);
  asm volatile("" ::: "memory");
}
__device__ __forceinline__ unsigned now32() { return (unsigned)__builtin_amdgcn_s_memrealtime(); }
__device__ __forceinline__ void sleep_until(unsigned due) { while ((int)(now32() - due) < 0) __builtin_amdgcn_s_sleep(2); }
__device__ __forceinline__ rsrc_t make_rsrc(const void* p, int bytes) {
  return __builtin_amdgcn_make_buffer_rsrc((void*)p, (short)0, bytes, 0x00020000);
}
constexpr int SC1 = 16;
__device__ __forceinline__ u32x4 ld_dev(rsrc_t r, unsigned voff, unsigned soff) { return __builtin_amdgcn_raw_buffer_load_b128(r, voff, soff, SC1); }
__device__ __forceinline__ void st_dev(rsrc_t r, u32x4 v, unsigned voff, unsigned soff) { __builtin_amdgcn_raw_buffer_store_b128(v, r, voff, soff, SC1); }
__device__ __forceinline__ bool pair_ok(const u32x4& g, unsigned tag) { return (g[1] == tag) & (g[3] == tag); }
__device__ __forceinline__ bool quad_ok(const u32x4& g, unsigned tag) {
  return (__builtin_amdgcn_perm(g[1], g[0], 0x0c0c0400u) | __builtin_amdgcn_perm(g[3], g[2], 0x04000c0cu)) == tag;
}
__device__ __forceinline__ float quad_val(unsigned w) { return __uint_as_float(w & 0xffffff00u); }
struct QuadOut {
  unsigned t0, t1, t2, t3, voff;
  __device__ __forceinline__ QuadOut(unsigned tag, int lane) : t0(tag & 255u), t1((tag >> 8) & 255u), t2((tag >> 16) & 255u), t3(tag >> 24), voff(lane & 1 ? OOB : lane * 8) {
    asm volatile("" : "+v"(t0), "+v"(t1), "+v"(t2), "+v"(t3), "+v"(voff));
  }
  __device__ __forceinline__ void store(rsrc_t r, float a, float b, unsigned soff) const {
    const float c = dpp<XOR1>(a), d = dpp<XOR1>(b);
    st_dev(r, u32x4{(__float_as_uint(a) & 0xffffff00u) | t0, (__float_as_uint(b) & 0xffffff00u) | t1, (__float_as_uint(c) & 0xffffff00u) | t2,
                    (__float_as_uint(d) & 0xffffff00u) | t3}, voff, soff);
  }
};
__device__ __forceinline__ float quad_sum(float v0, float v1, float v2, float v3) {
  float x = swap16_sum(swap32_sum(v0, v2), swap32_sum(v1, v3));
  x += dpp<ROR8>(x); x += dpp<ROR4>(x); x += dpp<XOR2>(x); x += dpp<XOR1>(x);
  return x;
}
__device__ __forceinline__ s16x4 tr_read(unsigned addr) { return __builtin_amdgcn_ds_read_tr16_b64_v4i16((LAS s16x4*)(size_t)addr); }
template <typename T> __device__ __forceinline__ T lds_ld(unsigned addr) { return *(const LAS T*)(size_t)addr; }
template <typename T> __device__ __forceinline__ void lds_st(unsigned addr, T v) { *(LAS T*)(size_t)addr = v; }

__device__ __forceinline__ void dma_piece(rsrc_t r, unsigned voff, unsigned soff, unsigned dst) {
  asm volatile("s_mov_b32 m0, %0\n\ts_nop 0\n\tbuffer_load_dwordx4 %1, %2, %3 offen nt lds" ::"s"(dst), "v"(voff), "s"(r), "s"(soff) : "memory");
}
__device__ __forceinline__ void dma_half(rsrc_t r, unsigned v0, unsigned v1, unsigned v2, unsigned v3, unsigned soff, unsigned dst) {
  asm volatile(
      "s_mov_b32 m0, %0\n\ts_nop 0\n\t"
      "buffer_load_dwordx4 %1, %5, %6 offen nt lds\n\t"
      "buffer_load_dwordx4 %2, %5, %6 offen offset:1024 nt lds\n\t"
      "buffer_load_dwordx4 %3, %5, %6 offen offset:2048 nt lds\n\t"
      "buffer_load_dwordx4 %4, %5, %6 offen offset:3072 nt lds"
      ::"s"(dst), "v"(v0), "v"(v1), "v"(v2), "v"(v3), "s"(r), "s"(soff)
      : "memory");
}
template <int A> __device__ __forceinline__ f32x4 mfma4(s16x4 a, s16x4 b, f32x4 c) { return __builtin_amdgcn_mfma_f32_4x4x4bf16_1k(a, b, c, 4, A, 0); }
template <int A, int N> struct Proj {
  static __device__ __forceinline__ void run(const s16x4 (&av)[2], unsigned ba, f32x4 (&c)[4]) {
    c[A & 3] = mfma4<A & 15>(av[A >> 4], tr_read(ba + A * 512), c[A & 3]);
    if constexpr (A + 1 < N) Proj<A + 1, N>::run(av, ba, c);
  }
};
template <int A> struct ProjReg {
  static __device__ __forceinline__ void run(s16x4 a, const s16x4 (&w)[16], f32x4 (&c)[2]) {
    c[A & 1] = mfma4<A>(a, w[A], c[A & 1]);
    if constexpr (A + 1 < 16) ProjReg<A + 1>::run(a, w, c);
  }
};
__device__ __forceinline__ f32x2 proj64(unsigned a_img, int a_stride, unsigned w_img, int lane) {
  const int q = (lane & 15) >> 2, p = lane & 3;
  const unsigned aa = a_img + (lane & 1) * a_stride + (lane >> 2) * 8;
  const s16x4 av[2] = {lds_ld<s16x4>(aa), lds_ld<s16x4>(aa + 128)};
  const unsigned ba = w_img + q * 128 + (((2 * (lane >> 4) + (p >> 1)) ^ ((q >> 1) << 2)) << 4) + (p & 1) * 8;
  f32x4 c[4] = {};
  Proj<0, 32>::run(av, ba, c);
  return f32x2{(c[0][0] + c[1][0]) + (c[2][0] + c[3][0]), (c[0][1] + c[1][1]) + (c[2][1] + c[3][1])};
}
__device__ __forceinline__ f32x4 gather_quarters(rsrc_t rws, unsigned base, int quarter_bytes, unsigned tag, int lane) {
  u32x4 g;
  for (unsigned spin = 0; spin < SPIN_CAP; ++spin) {
    asm volatile("" ::: "memory");
    g = ld_dev(rws, (lane >> 4) * quarter_bytes + (lane & 7) * 16, base);
    if (__all(quad_ok(g, tag))) break;
    __builtin_amdgcn_s_sleep(8);
  }
  return f32x4{rows_sum(quad_val(g[0])), rows_sum(quad_val(g[1])), rows_sum(quad_val(g[2])), rows_sum(quad_val(g[3]))};
}

template <int N> using Int = std::integral_constant<int, N>;
struct KFrag { bf16x8 k[4]; };
struct VFrag { s16x4 v[4][2]; };

__device__ __forceinline__ void read_k(KFrag& t, const unsigned (&ka)[4], unsigned off) {
#pragma unroll
  for (int s = 0; s < 4; ++s) t.k[s] = lds_ld<bf16x8>(ka[s] + off);
  asm volatile("s_waitcnt lgkmcnt(0)" : "+v"(t.k[0]), "+v"(t.k[1]), "+v"(t.k[2]), "+v"(t.k[3])::"memory");
}
__device__ __forceinline__ void read_v(VFrag& t, const unsigned (&va)[2], unsigned off) {
#pragma unroll
  for (int a = 0; a < 4; ++a)
#pragma unroll
    for (int h = 0; h < 2; ++h) t.v[a][h] = tr_read(va[h] + off + a * 1024);
  asm volatile("s_waitcnt lgkmcnt(0)"
               : "+v"(t.v[0][0]), "+v"(t.v[0][1]), "+v"(t.v[1][0]), "+v"(t.v[1][1]), "+v"(t.v[2][0]), "+v"(t.v[2][1]), "+v"(t.v[3][0]), "+v"(t.v[3][1])
               :
               : "memory");
}
__device__ __forceinline__ void score_tile(const KFrag& t, const bf16x8 (&qf)[4], float& m, unsigned dst, bool writer) {
  f32x4 sc = {0.f, 0.f, 0.f, 0.f};
#pragma unroll
  for (int s = 0; s < 4; ++s) sc = __builtin_amdgcn_mfma_f32_16x16x32_bf16(t.k[s], qf[s], sc, 0, 0, 0);
  m = fmaxf(fmaxf(m, fmaxf(sc[0], sc[1])), fmaxf(sc[2], sc[3]));
  const unsigned r0 = pk2(sc[0], sc[1]), r1 = pk2(sc[2], sc[3]);
  if (writer) lds_st<u32x2>(dst, u32x2{pk2(bflo(r0) * SCALE, bfhi(r0) * SCALE), pk2(bflo(r1) * SCALE, bfhi(r1) * SCALE)});
}
__device__ __forceinline__ s16x4 probs(unsigned src, float M, float f) {
  const u32x2 xp = lds_ld<u32x2>(src);
  const u32x2 pp = {pk2(f * ex2((bflo(xp[0]) - M) * L2E), f * ex2((bfhi(xp[0]) - M) * L2E)), pk2(f * ex2((bflo(xp[1]) - M) * L2E), f * ex2((bfhi(xp[1]) - M) * L2E))};
  return __builtin_bit_cast(s16x4, pp);
}
__device__ __forceinline__ void value_tile(const VFrag& t, s16x4 pa, f32x4 (&o)[2]) {
#pragma unroll
  for (int h = 0; h < 2; ++h) o[h] = mfma4<0>(pa, t.v[0][h], o[h]);
#pragma unroll
  for (int h = 0; h < 2; ++h) o[h] = mfma4<4>(pa, t.v[1][h], o[h]);
#pragma unroll
  for (int h = 0; h < 2; ++h) o[h] = mfma4<8>(pa, t.v[2][h], o[h]);
#pragma unroll
  for (int h = 0; h < 2; ++h) o[h] = mfma4<12>(pa, t.v[3][h], o[h]);
}

__global__ void __launch_bounds__(NTHR, 2)
fused_decode_kernel(const u16* __restrict__ x, const u16* __restrict__ kc, const u16* __restrict__ vc, const u16* __restrict__ wq,
                    const u16* __restrict__ wo, const u16* __restrict__ w1, const u16* __restrict__ w2, u16* __restrict__ out, char* ws) {
  unsigned long long pc;
  asm volatile("s_getpc_b64 %0" : "=s"(pc));
  extern __shared__ __attribute__((aligned(16))) unsigned char lds[];
  const unsigned lds0 = (unsigned)(size_t)(LAS unsigned char*)lds;
  const int lane = threadIdx.x & 63, wave = __builtin_amdgcn_readfirstlane(threadIdx.x >> 6), wg = blockIdx.x;
  const int xg = wg & 7, xi = wg >> 3;
  asm volatile("" ::"s"(x), "s"(kc), "s"(vc), "s"(wq), "s"(wo), "s"(w1), "s"(w2), "s"(out), "s"(ws));
  const rsrc_t rws = make_rsrc(ws, WS_END);
  const unsigned sw128 = ((lane & 7) ^ (((lane >> 4) & 1) << 2)) << 4;

  if (wave < 4) {
    const rsrc_t rk = make_rsrc(kc, KV_BYTES), rv = make_rsrc(vc, KV_BYTES);
    const int g = lane >> 4, c = lane & 15;
    unsigned vk[4];
#pragma unroll
    for (int u = 0; u < 4; ++u) vk[u] = g * 256 + ((c ^ (4 * u + g)) << 4);
    const unsigned vv = g * 256 + ((c ^ (g << 2)) << 4);
    const unsigned ring = lds0 + L_RING + wave * (NSLOT * 4096);
    const unsigned tile0 = (unsigned)wg * (2048 * 256) + wave * 4096;
    unsigned ka[4], va[2];
#pragma unroll
    for (int s = 0; s < 4; ++s) ka[s] = ring + c * 256 + (((4 * s + g) ^ c) << 4);
#pragma unroll
    for (int h = 0; h < 2; ++h) va[h] = ring + (c >> 2) * 256 + (((8 * h + 2 * g + ((c & 3) >> 1)) ^ ((c >> 2) << 2)) << 4) + (c & 1) * 8;
    const unsigned sca = lds0 + L_SC + wave * 4096 + (g * 4 + (c & 3)) * 8;
    auto issue_k = [&](int t, unsigned off) { dma_half(rk, vk[0], vk[1], vk[2], vk[3], tile0 + t * 16384, ring + off); };
    auto issue_v = [&](int t, unsigned off) { dma_half(rv, vv, vv, vv, vv, tile0 + t * 16384, ring + off); };

    wg_barrier();
#pragma unroll
    for (int u = 0; u < AHEAD; ++u) { wait_vm<WIN>(); issue_k(u, u * 4096); }
    KFrag pk[NPRE];
#pragma unroll
    for (int u = 0; u < NPRE; ++u) {
      wait_vm<WIN>();
      issue_k(u + AHEAD, ((u + AHEAD) % NSLOT) * 4096);
      read_k(pk[u], ka, (u % NSLOT) * 4096);
    }
    lds_wait_nonzero(lds0 + L_QR);
    bf16x8 qf[4];
#pragma unroll
    for (int s = 0; s < 4; ++s) qf[s] = lds_ld<bf16x8>(lds0 + L_QI + (c & 3) * 256 + (4 * s + g) * 16);
    const unsigned tag = lds_ld<unsigned>(lds0 + L_TAG);

    unsigned prev = now32();
    auto landed = [&](int u, auto win) {
      const u32x2 plan = *(volatile LAS u32x2*)(size_t)(lds0 + L_TL);
      wait_vm<win.value>();
      unsigned tn = now32();
      *(volatile LAS u32x2*)(size_t)(lds0 + L_TP + wave * 8) = u32x2{(unsigned)u, tn};
      if (u == CPA) *(volatile LAS unsigned*)(size_t)(lds0 + L_TS + wave * 4) = tn | 1u;
      const unsigned tl = __builtin_amdgcn_readfirstlane(plan[0]);
      const int d = (int)(tl - prev);
      if (tl != 0 && d > 0) {
        const float n = (float)(NUNIT - AHEAD - u);
        const bool behind = (float)d + DELTA < n * __uint_as_float(plan[1]);
        const unsigned due = prev + (unsigned)__builtin_amdgcn_readfirstlane(behind ? 0 : (int)((float)d * __builtin_amdgcn_rcpf(n)));
        while ((int)(tn - due) < 0) { __builtin_amdgcn_s_sleep(1); tn = now32(); }
      }
      prev = tn + OVER;
    };
    static_assert(WIN <= 16 && WIN2 <= 16 && 2 * NPRE <= NTILE - AHEAD && CPA >= NPRE, "unit schedule");
    float m = M_INIT;
    KFrag kf;
#pragma unroll
    for (int u = NPRE; u < 2 * NPRE; ++u) {
      landed(u, Int<WIN>{}); issue_k(u + AHEAD, ((u + AHEAD) % NSLOT) * 4096);
      read_k(kf, ka, (u % NSLOT) * 4096);
      score_tile(kf, qf, m, sca + u * 128, c < 4);
      score_tile(pk[u - NPRE], qf, m, sca + (u - NPRE) * 128, c < 4);
    }
    unsigned off = ((2 * NPRE) % NSLOT) * 4096, offp = ((2 * NPRE + AHEAD) % NSLOT) * 4096;
    auto next_slot = [&]() { offp = off; off = off == (NSLOT - 1) * 4096 ? 0u : off + 4096; };
#pragma unroll 1
    for (int u = 2 * NPRE; u < NTILE - AHEAD; ++u) {
      landed(u, Int<WIN>{}); issue_k(u + AHEAD, offp);
      read_k(kf, ka, off);
      score_tile(kf, qf, m, sca + u * 128, c < 4);
      next_slot();
    }
#pragma unroll 1
    for (int u = NTILE - AHEAD; u < NTILE; ++u) {
      landed(u, Int<WIN2>{}); issue_v(u + AHEAD - NTILE, offp);
      read_k(kf, ka, off);
      score_tile(kf, qf, m, sca + u * 128, c < 4);
      next_slot();
    }
    const float M = rbf(rbf(rows_max(m)) * SCALE);
    if (lane < 4) lds_st<float>(lds0 + L_ML + lane * 16 + wave * 4, M);
    wait_lds();
    if (lane == 0) *(volatile LAS unsigned*)(size_t)(lds0 + L_MR + wave * 4) = 1u;
    f32x4 o[2] = {};
    VFrag vf;
#pragma unroll 1
    for (int u = NTILE; u < NUNIT - AHEAD; ++u) {
      landed(u, Int<WIN2>{}); issue_v(u + AHEAD - NTILE, offp);
      const s16x4 pa = probs(sca + (u - NTILE) * 128, M, 1.f);
      read_v(vf, va, off);
      value_tile(vf, pa, o);
      next_slot();
    }
    const int ks = xg * 8 + (xi >> 2), cs = xi & 3;
    if (wave < 2) {
      const rsrc_t ro = make_rsrc(wo, HQD * DM * 2);
#pragma unroll
      for (int i = 0; i < 8; ++i)
        dma_piece(ro, (lane >> 3) * (DM * 2) + sw128, (ks * 64 + 8 * i) * (DM * 2) + cs * 256 + wave * 128, lds0 + L_WO + wave * 8192 + i * 1024);
    }
    const bool late_w2 = wave >= 2 && xi >= 14 && xi < 28;
    if (late_w2) {
      const int fi = xg * 14 + xi - 14;
      const rsrc_t r2 = make_rsrc(w2, DM * FF * 2);
#pragma unroll
      for (int i = 0; i < 8; ++i)
        dma_piece(r2, (lane >> 3) * (DM * 2) + sw128, ((fi >> 3) * 128 + (wave & 1) * 64 + 8 * i) * (DM * 2) + (fi & 7) * 128, lds0 + L_WF + (wave & 1) * 8192 + i * 1024);
    }
    lds_wait_nonzero(lds0 + L_GR);
    const f32x2 gs = lds_ld<f32x2>(lds0 + L_GS + (c & 3) * 8);
    const float f = ex2((M - gs[0]) * L2E) * gs[1];
    {
      const float f0 = dpp<0x00>(f), f1 = dpp<0x55>(f), f2 = dpp<0xAA>(f), f3 = dpp<0xFF>(f);
#pragma unroll
      for (int h = 0; h < 2; ++h) { o[h][0] *= f0; o[h][1] *= f1; o[h][2] *= f2; o[h][3] *= f3; }
    }
    const QuadOut qo(tag, lane);
    unsigned s_p1 = WS_P1 + wg * 2048 + wave * 512, s_p2 = WS_P2 + ks * (DM * 8) + (cs * 128 + wave * 64) * 8;
    unsigned a_mw = ring + lane * 4, a_mr = lds0 + L_RING + (wave * 128 + 2 * lane) * 4;
    unsigned a_ow = lds0 + L_OS + (wave >> 1) * 128 + ((wave & 1) * 32 + 4 * (lane & 7) + g) * 2, a_or = lds0 + L_OS + (lane & 1) * 128 + (lane >> 2) * 8;
    asm volatile("" : "+s"(s_p1), "+s"(s_p2), "+v"(a_mw), "+v"(a_mr), "+v"(a_ow), "+v"(a_or));
    auto last = [&](auto j) {
      constexpr int u = NUNIT - AHEAD + j.value, younger = (NUNIT - 1 - u) * 4;
      s16x4 pa = probs(sca + (u - NTILE) * 128, M, f);
      asm volatile("" : "+v"(pa));
      if (wave < 2 || late_w2) wait_vm<younger + 8>(); else wait_vm<younger>();
      read_v(vf, va, (u % NSLOT) * 4096);
      value_tile(vf, pa, o);
    };
    last(Int<0>{}); last(Int<1>{}); last(Int<2>{}); last(Int<3>{}); last(Int<4>{});

#pragma unroll
    for (int h = 0; h < 2; ++h)
#pragma unroll
      for (int r = 0; r < 4; ++r) lds_st<float>(a_mw + (r * 128 + h * 64) * 4, o[h][r]);
    wg_barrier();
    {
      f32x2 ov[4];
#pragma unroll
      for (int w = 0; w < 4; ++w) ov[w] = lds_ld<f32x2>(a_mr + w * (NSLOT * 4096));
      const f32x2 sum = (ov[0] + ov[1]) + (ov[2] + ov[3]);
      qo.store(rws, sum[0], sum[1], s_p1);
    }
    s16x4 wf[16];
    if (wave < 2) {
      const unsigned wa = lds0 + L_WO + wave * 8192 + (c >> 2) * 128 + (((2 * g + ((c & 3) >> 1)) ^ ((c >> 3) << 2)) << 4) + (c & 1) * 8;
      wait_vm<1>();
#pragma unroll
      for (int a = 0; a < 16; ++a) wf[a] = tr_read(wa + a * 512);
    }
    if (late_w2) {
      wait_vm<1>();
      if (lane == 0) *(volatile LAS unsigned*)(size_t)(lds0 + L_WR + (wave & 1) * 4) = 1u;
    }
    {
      const int b = wave >> 1, half = wave & 1;
      const unsigned vo = (lane >> 3) * 2048 + (lane & 7) * 16;
      const unsigned so = WS_P1 + (b * 8 + xg) * 16 * 2048 + ((ks >> 1) & 3) * 512 + ((ks & 1) * 2 + half) * 128;
      sleep_until(__builtin_amdgcn_readfirstlane(*(volatile LAS unsigned*)(size_t)(lds0 + L_TL)) + POLL_AT);
      u32x4 gv[2] = {};
      bool h0 = false, h1 = false;
      for (unsigned spin = 0; spin < SPIN_CAP; ++spin) {
        asm volatile("" ::: "memory");
        if (!h0) gv[0] = ld_dev(rws, vo, so);
        if (!h1) gv[1] = ld_dev(rws, vo + 8 * 2048, so);
        h0 = quad_ok(gv[0], tag); h1 = quad_ok(gv[1], tag);
        if (__all(h0 & h1)) break;
      }
      float u[4];
#pragma unroll
      for (int j = 0; j < 4; ++j) u[j] = quad_val(gv[0][j]) + quad_val(gv[1][j]);
      float e = swap16_sum(swap32_sum(u[0], u[2]), swap32_sum(u[1], u[3]));
      e += dpp<ROR8>(e);
      lds_st<__bf16>(a_ow, (__bf16)e);
    }
    wg_barrier();
    if (wave < 2) {
      const s16x4 oa = lds_ld<s16x4>(a_or);
      f32x4 acc[2] = {};
      ProjReg<0>::run(oa, wf, acc);
      qo.store(rws, acc[0][0] + acc[1][0], acc[0][1] + acc[1][1], s_p2);
    }
  } else {
    __builtin_amdgcn_s_setprio(3);
    const int cg = xg * 32 + xi;
    const int qk = cg >> 6, qc = cg & 63;
    const bool f1 = xi < 14, f2 = xi >= 14 && xi < 28;
    const int fi = xg * 14 + (xi < 14 ? xi : xi - 14);
    const int hk = fi / 28, hc = fi % 28;
    const int gk = fi >> 3, gc = fi & 7;
    const unsigned vout = (((lane >> 4) & 1) * DM + cg * 2 + (lane >> 5)) * 2;
    const rsrc_t rx = make_rsrc(x, 2 * DM * 2);
    const rsrc_t rout = make_rsrc(out, 2 * DM * 2);
    unsigned ev, xo;
    u32x4 x0, x1, code;
    asm volatile(
        "s_nop 4\n\t"
        "buffer_load_dwordx4 %0, %5, %6, 0 offen\n\t"
        "buffer_load_dwordx4 %1, %5, %6, 0 offen offset:1024\n\t"
        "buffer_load_dword %2, off, %7, 0 sc1\n\t"
        "buffer_load_ushort %3, %8, %6, 0 offen\n\t"
        "buffer_load_dwordx4 %4, %5, %9, %10 offen"
        : "=&v"(x0), "=&v"(x1), "=&v"(ev), "=&v"(xo), "=&v"(code)
        : "v"(lane * 16), "s"(rx), "s"(rws), "v"(vout), "s"(make_rsrc((const void*)(pc & ~255ull), CODE_KB * 1024)), "s"(xi * 1024)
        : "memory");
    {
      const rsrc_t rq = make_rsrc(wq, DM * HQD * 2);
#pragma unroll
      for (int i = 0; i < 16; ++i) dma_piece(rq, (lane >> 3) * (HQD * 2) + sw128, (qk * 128 + 8 * i) * (HQD * 2) + qc * 128, lds0 + L_WQ + i * 1024);
      __builtin_amdgcn_sched_barrier(0);
      if (f1) {
        const rsrc_t r1 = make_rsrc(w1, DM * FF * 2);
#pragma unroll
        for (int i = 0; i < 16; ++i) dma_piece(r1, (lane >> 3) * (FF * 2) + sw128, (hk * 128 + 8 * i) * (FF * 2) + hc * 128, lds0 + L_WF + i * 1024);
      }
    }
    if (lane < 28) lds_st<unsigned>(lds0 + L_TAG + lane * 4, 0u);
    wg_barrier();
    const unsigned th0 = now32();
    if (f1) wait_vm<32>(); else wait_vm<16>();
    asm volatile("" : "+v"(x0), "+v"(x1), "+v"(ev), "+v"(xo), "+v"(code)::"memory");
    const unsigned epoch = max(__builtin_amdgcn_readfirstlane(ev) + 1u, 1u), tag = epoch * 0x9E3779B1u;
    {
      float a[2][8];
#pragma unroll
      for (int j = 0; j < 4; ++j) { a[0][2 * j] = bflo(x0[j]); a[0][2 * j + 1] = bfhi(x0[j]); a[1][2 * j] = bflo(x1[j]); a[1][2 * j + 1] = bfhi(x1[j]); }
#pragma unroll
      for (int b = 0; b < 2; ++b) {
        const float mean = wave_sum(((a[b][0] + a[b][1]) + (a[b][2] + a[b][3])) + ((a[b][4] + a[b][5]) + (a[b][6] + a[b][7]))) * (1.f / DM);
        float q = 0.f;
#pragma unroll
        for (int j = 0; j < 8; ++j) q = fmaf(a[b][j] - mean, a[b][j] - mean, q);
        const float var = wave_sum(q) * (1.f / DM);
        const float mb = rbf(mean), inv = __builtin_amdgcn_rcpf(rbf(__builtin_amdgcn_sqrtf(rbf(rbf(var) + 1e-5f))));
        u32x4 xn;
#pragma unroll
        for (int j = 0; j < 4; ++j) xn[j] = pk2(rbf(a[b][2 * j] - mb) * inv, rbf(a[b][2 * j + 1] - mb) * inv);
        lds_st<u32x4>(lds0 + L_XN + b * 1024 + lane * 16, xn);
      }
    }
    if (lane == 0) lds_st<unsigned>(lds0 + L_TAG, tag);
    const QuadOut qo(tag, lane);
    if (f1) wait_vm<16>(); else wait_vm<0>();
    wait_lds();
    {
      const f32x2 y = proj64(lds0 + L_XN + qk * 256, 1024, lds0 + L_WQ, lane);
      qo.store(rws, y[0], y[1], WS_QP + (qk * HQD + qc * 64) * 8);
    }
    if (f1) {
      wait_vm<1>();
      const f32x2 y = proj64(lds0 + L_XN + hk * 256, 1024, lds0 + L_WF, lane);
      qo.store(rws, y[0], y[1], WS_HP + (hk * FF + hc * 64) * 8);
    }
    {
      const f32x4 q = gather_quarters(rws, WS_QP + (qc * 64 + qk * 16) * 8, HQD * 8, tag, lane);
      if (lane < 8) st_dev(rws, u32x4{pk2(q[0], q[1]), tag, pk2(q[2], q[3]), tag}, lane * 16, WS_Q + (qc * 64 + qk * 16) * 8);
    }
    if (f1) {
      const f32x4 h = gather_quarters(rws, WS_HP + (hc * 64 + hk * 16) * 8, FF * 8, tag, lane);
      if (lane < 8) st_dev(rws, u32x4{pk2(silu(rbf(h[0])), silu(rbf(h[1]))), tag, pk2(silu(rbf(h[2])), silu(rbf(h[3]))), tag}, lane * 16, WS_H + (hc * 64 + hk * 16) * 8);
    }
    {
      const int b = wg >> 7, kvh = (wg >> 4) & 7;
      u32x4 gq[4];
      for (unsigned spin = 0; spin < SPIN_CAP; ++spin) {
        asm volatile("" ::: "memory");
#pragma unroll
        for (int i = 0; i < 4; ++i) gq[i] = ld_dev(rws, lane * 16 + i * 1024, WS_Q + kvh * 4096);
        if (__all(pair_ok(gq[0], tag) & pair_ok(gq[1], tag) & pair_ok(gq[2], tag) & pair_ok(gq[3], tag))) break;
        __builtin_amdgcn_s_sleep(8);
      }
      const unsigned sel = b ? 0x07060302u : 0x05040100u;
#pragma unroll
      for (int i = 0; i < 4; ++i) lds_st<unsigned>(lds0 + L_QI + i * 256 + lane * 4, __builtin_amdgcn_perm(gq[i][2], gq[i][0], sel));
    }
    wait_lds();
    if (lane == 0) *(volatile LAS unsigned*)(size_t)(lds0 + L_QR) = 1u;

    if (f2) {
      u32x4 gh;
      for (unsigned spin = 0; spin < SPIN_CAP; ++spin) {
        asm volatile("" ::: "memory");
        gh = ld_dev(rws, lane * 16, WS_H + gk * 1024);
        if (__all(pair_ok(gh, tag))) break;
        __builtin_amdgcn_s_sleep(32);
      }
      lds_st<unsigned>(lds0 + L_HS + lane * 4, __builtin_amdgcn_perm(gh[2], gh[0], 0x05040100u));
      lds_st<unsigned>(lds0 + L_HS + 256 + lane * 4, __builtin_amdgcn_perm(gh[2], gh[0], 0x07060302u));
    }
    {
      sleep_until(th0 + TS2);
      u32x4 ta;
      for (unsigned spin = 0; spin < SPIN_CAP; ++spin) {
        ta = *(volatile LAS u32x4*)(size_t)(lds0 + L_TS);
        if (__all((ta[0] != 0) & (ta[1] != 0) & (ta[2] != 0) & (ta[3] != 0))) break;
        __builtin_amdgcn_s_sleep(8);
      }
      const u32x4 p0 = *(volatile LAS u32x4*)(size_t)(lds0 + L_TP), p1 = *(volatile LAS u32x4*)(size_t)(lds0 + L_TP + 16);
      {
        const unsigned a = th0 + (((ta[0] - th0) + (ta[1] - th0) + (ta[2] - th0) + (ta[3] - th0)) >> 2);
        const unsigned b = th0 + (((p0[1] - th0) + (p0[3] - th0) + (p1[1] - th0) + (p1[3] - th0)) >> 2);
        const unsigned c4 = p0[0] + p0[2] + p1[0] + p1[2];
        if (lane == 0) st_dev(rws, u32x4{a, tag, (b << 8) | c4, tag}, 0, WS_PC + wg * 128);
      }
      sleep_until(now32() + TSD);
      u32x4 gp;
      for (unsigned spin = 0; spin < SPIN_CAP; ++spin) {
        asm volatile("" ::: "memory");
        gp = ld_dev(rws, (lane & (NSAMP - 1)) * 128, WS_PC + (wg & ~(NSAMP - 1)) * 128);
        if (__all(pair_ok(gp, tag))) break;
        __builtin_amdgcn_s_sleep(24);
      }
      const float tb = (float)((int)((gp[2] & 0xffffff00u) - (th0 << 8)) >> 8), ta_ = (float)(int)(gp[0] - th0);
      const float cb = 0.25f * (float)(gp[2] & 0xffu);
      const float r = fmaxf(cb - (float)CPA, 0.25f) * __builtin_amdgcn_rcpf(fmaxf(tb - ta_, 25.f)) * RBOOST;
      const float inv = __builtin_amdgcn_rcpf(wave_sum(r));
      const float fin = fminf(fmaxf(wave_sum((float)(NUNIT - 4) - cb + r * tb) * inv, 0.f), 8000.f);
      if (lane == 0) *(volatile LAS u32x2*)(size_t)(lds0 + L_TL) = u32x2{(th0 + (unsigned)((int)fin - PACE_OFF)) | 1u, __float_as_uint(64.f * inv)};
    }
    {
      for (unsigned spin = 0; spin < SPIN_CAP * 4; ++spin) {
        const u32x4 mr = *(volatile LAS u32x4*)(size_t)(lds0 + L_MR);
        if (__all((mr[0] & mr[1] & mr[2] & mr[3]) != 0)) break;
        __builtin_amdgcn_s_sleep(8);
      }
      asm volatile("" ::: "memory");
      __builtin_amdgcn_s_setprio(0);
      const f32x4 ma = lds_ld<f32x4>(lds0 + L_ML + (lane & 1) * 32), mb = lds_ld<f32x4>(lds0 + L_ML + (lane & 1) * 32 + 16);
      const float Ma = fmaxf(fmaxf(ma[0], ma[1]), fmaxf(ma[2], ma[3])), Mb = fmaxf(fmaxf(mb[0], mb[1]), fmaxf(mb[2], mb[3]));
      float La = 0.f, Lb = 0.f;
#pragma unroll 4
      for (int i = 0; i < 16; ++i) {
        const u32x4 xp = lds_ld<u32x4>(lds0 + L_SC + i * 1024 + lane * 16);
        La += (ex2((bflo(xp[0]) - Ma) * L2E) + ex2((bfhi(xp[0]) - Ma) * L2E)) + (ex2((bflo(xp[1]) - Ma) * L2E) + ex2((bfhi(xp[1]) - Ma) * L2E));
        Lb += (ex2((bflo(xp[2]) - Mb) * L2E) + ex2((bfhi(xp[2]) - Mb) * L2E)) + (ex2((bflo(xp[3]) - Mb) * L2E) + ex2((bfhi(xp[3]) - Mb) * L2E));
      }
      La += dpp<XOR2>(La); Lb += dpp<XOR2>(Lb);
      La = quads_sum(La); Lb = quads_sum(Lb);
      __builtin_amdgcn_s_setprio(3);
      if (lane < 4) st_dev(rws, u32x4{__float_as_uint(lane & 2 ? Mb : Ma), tag, __float_as_uint(lane & 2 ? Lb : La), tag}, ((lane & 1) * 2 + (lane >> 1)) * 16, WS_ML + wg * 128);
      sleep_until(now32() + ML_DELAY);
      u32x4 gm;
      for (unsigned spin = 0; spin < SPIN_CAP; ++spin) {
        asm volatile("" ::: "memory");
        gm = ld_dev(rws, (lane >> 2) * 128 + (lane & 3) * 16, WS_ML + (wg & ~15) * 128);
        if (__all(pair_ok(gm, tag))) break;
        __builtin_amdgcn_s_sleep(32);
      }
      const float ms = __uint_as_float(gm[0]), Mg = quads_max(ms);
      const float inv = __builtin_amdgcn_rcpf(quads_sum(ex2((ms - Mg) * L2E) * __uint_as_float(gm[2])));
      if (lane < 4) lds_st<f32x2>(lds0 + L_GS + lane * 8, f32x2{Mg, inv});
      wait_lds();
      if (lane == 0) *(volatile LAS unsigned*)(size_t)(lds0 + L_GR) = 1u;
    }
    wg_barrier();
    u32x4 gs = ld_dev(rws, lane * (DM * 8), WS_P2 + cg * 16);
    if (f2) {
      lds_wait_nonzero(lds0 + L_WR); lds_wait_nonzero(lds0 + L_WR + 4);
      const f32x2 y = proj64(lds0 + L_HS, 256, lds0 + L_WF, lane);
      qo.store(rws, y[0], y[1], WS_FG + (gk * DM + gc * 64) * 8);
    }
    float xr = bflo(xo);
    unsigned vst = (lane & 15) == 0 ? vout : OOB;
    asm volatile("" : "+v"(gs), "+v"(xr), "+v"(vst) : "s"(rout));
    wg_barrier();
    float gate;
    {
      u32x4 gf;
      for (unsigned spin = 0; spin < SPIN_CAP; ++spin) {
        asm volatile("" ::: "memory");
        gf = ld_dev(rws, (lane < 14 ? lane : 0) * (DM * 8), WS_FG + cg * 16);
        if (__all(quad_ok(gf, tag))) break;
      }
      if (lane >= 14) gf = u32x4{0u, 0u, 0u, 0u};
      gate = rbf(silu(rbf(quad_sum(quad_val(gf[0]), quad_val(gf[1]), quad_val(gf[2]), quad_val(gf[3])))));
      asm volatile("" : "+v"(gate));
    }
    {
      bool have = false;
      for (unsigned spin = 0; spin < SPIN_CAP; ++spin) {
        asm volatile("" ::: "memory");
        if (!have) gs = ld_dev(rws, lane * (DM * 8), WS_P2 + cg * 16);
        have = quad_ok(gs, tag);
        if (__all(have)) break;
      }
      const float s = rbf(quad_sum(quad_val(gs[0]), quad_val(gs[1]), quad_val(gs[2]), quad_val(gs[3])));
      __builtin_amdgcn_raw_buffer_store_b16(__builtin_bit_cast(u16, (__bf16)(xr + rbf(s * gate))), rout, vst, 0, 0);
      if (wg == 0 && lane == 0) __builtin_amdgcn_raw_buffer_store_b32(epoch, rws, 0, WS_EPOCH, SC1);
    }
  }
}

}

extern "C" void kernel_launch(void* const* d_in, const int* in_sizes, int n_in, void* d_out, int out_size, void* d_ws, size_t ws_size,
                              hipStream_t stream) {
  (void)hipFuncSetAttribute((const void*)fused_decode_kernel, hipFuncAttributeMaxDynamicSharedMemorySize, L_END);
  fused_decode_kernel<<<NWG, NTHR, L_END, stream>>>((const u16*)d_in[0], (const u16*)d_in[1], (const u16*)d_in[2], (const u16*)d_in[3],
                                                    (const u16*)d_in[4], (const u16*)d_in[6], (const u16*)d_in[7], (u16*)d_out, (char*)d_ws);
}
```

```cpp
#include <hip/hip_runtime.h>
#include <hip/hip_bf16.h>
#include <type_traits>

namespace {

typedef unsigned short u16;
typedef short s16x4 __attribute__((ext_vector_type(4)));
typedef __bf16 bf16x8 __attribute__((ext_vector_type(8)));
typedef __bf16 bf16x2 __attribute__((ext_vector_type(2)));
typedef float f32x2 __attribute__((ext_vector_type(2)));
typedef float f32x4 __attribute__((ext_vector_type(4)));
typedef unsigned u32x2 __attribute__((ext_vector_type(2)));
typedef unsigned u32x4 __attribute__((ext_vector_type(4)));
typedef __amdgpu_buffer_rsrc_t rsrc_t;
#define LAS __attribute__((address_space(3)))

constexpr int NWG = 256, NTHR = 320;
constexpr int DM = 512, HQD = 4096, SK = 32768, DH = 128, FF = 1792;
constexpr int NSLOT = 6, AHEAD = NSLOT - 1, NTILE = 32, NUNIT = 2 * NTILE, NPRE = 11;
constexpr int WIN = 4, WIN2 = 8;
constexpr int CODE_KB = 29;
constexpr int CPA = 13, TS2 = 1430, TSD = 100, PACE_OFF = 64, OVER = 4, NSAMP = 32, POLL_AT = 170, ML_DELAY = 100;
constexpr float RBOOST = 0.99f, DELTA = 55.f;
constexpr int KV_BYTES = 2 * 8 * SK * DH * 2;
constexpr float SCALE = 0.08838834764831845f, L2E = 1.4426950408889634f;
constexpr float M_INIT = -3.0e38f;
constexpr unsigned SPIN_CAP = 1u << 18;
constexpr unsigned OOB = 0x80000000u;

constexpr int WS_EPOCH = 0;
constexpr int WS_Q = 4096;
constexpr int WS_H = WS_Q + HQD * 8;
constexpr int WS_ML = 65536;
constexpr int WS_PC = WS_ML + NWG * 128;
constexpr int WS_QP = WS_PC + NWG * 128;
constexpr int WS_HP = WS_QP + 4 * HQD * 8;
constexpr int WS_FG = WS_HP + 4 * FF * 8;
constexpr int WS_P2 = WS_FG + 14 * DM * 8;
constexpr int WS_P1 = WS_P2 + 64 * DM * 8;
constexpr int WS_END = WS_P1 + NWG * 2048;
static_assert(WS_H + FF * 8 <= WS_ML, "workspace map");

constexpr int L_RING = 0;
constexpr int L_WQ = 4 * NSLOT * 4096;
constexpr int L_SC = L_WQ;
constexpr int L_WF = L_WQ + 16384;
constexpr int L_WO = L_WF + 16384;
constexpr int L_XN = L_WO + 16384;
constexpr int L_QI = L_XN + 2048;
constexpr int L_OS = L_QI + 1024;
constexpr int L_HS = L_OS + 256;
constexpr int L_ML = L_HS + 512;
constexpr int L_TAG = L_ML + 128;
constexpr int L_QR = L_TAG + 4;
constexpr int L_TL = L_TAG + 16;
constexpr int L_TS = L_TL + 16;
constexpr int L_TP = L_TS + 16;
constexpr int L_MR = L_TP + 32;
constexpr int L_GR = L_MR + 16;
constexpr int L_WR = L_GR + 8;
constexpr int L_GS = L_GR + 16;
constexpr int L_END = L_GS + 32;
static_assert(L_GS - L_TAG == 28 * 4 && L_END <= 160 * 1024, "LDS map");

__device__ __forceinline__ float rbf(float f) { return (float)(__bf16)f; }
__device__ __forceinline__ unsigned pk2(float lo, float hi) {
  bf16x2 v = {(__bf16)lo, (__bf16)hi};
  return __builtin_bit_cast(unsigned, v);
}
__device__ __forceinline__ float bflo(unsigned u) { return __uint_as_float(u << 16); }
__device__ __forceinline__ float bfhi(unsigned u) { return __uint_as_float(u & 0xffff0000u); }
template <int CTRL> __device__ __forceinline__ float dpp(float x) {
  return __builtin_bit_cast(float, __builtin_amdgcn_mov_dpp(__builtin_bit_cast(int, x), CTRL, 0xf, 0xf, true));
}
constexpr int XOR1 = 0xB1, XOR2 = 0x4E, ROR4 = 0x124, ROR8 = 0x128;
__device__ __forceinline__ float swap16_sum(float a, float b) {
  auto s = __builtin_amdgcn_permlane16_swap(__float_as_uint(a), __float_as_uint(b), false, false);
  return __uint_as_float(s[0]) + __uint_as_float(s[1]);
}
__device__ __forceinline__ float swap32_sum(float a, float b) {
  auto s = __builtin_amdgcn_permlane32_swap(__float_as_uint(a), __float_as_uint(b), false, false);
  return __uint_as_float(s[0]) + __uint_as_float(s[1]);
}
__device__ __forceinline__ float rows_sum(float x) { x = swap16_sum(x, x); return swap32_sum(x, x); }
__device__ __forceinline__ float rows_max(float x) {
  auto s = __builtin_amdgcn_permlane16_swap(__float_as_uint(x), __float_as_uint(x), false, false);
  x = fmaxf(__uint_as_float(s[0]), __uint_as_float(s[1]));
  auto t = __builtin_amdgcn_permlane32_swap(__float_as_uint(x), __float_as_uint(x), false, false);
  return fmaxf(__uint_as_float(t[0]), __uint_as_float(t[1]));
}
__device__ __forceinline__ float quads_sum(float x) { x += dpp<ROR4>(x); x += dpp<ROR8>(x); return rows_sum(x); }
__device__ __forceinline__ float quads_max(float x) { x = fmaxf(x, dpp<ROR4>(x)); x = fmaxf(x, dpp<ROR8>(x)); return rows_max(x); }
__device__ __forceinline__ float wave_sum(float x) { x += dpp<XOR1>(x); x += dpp<XOR2>(x); return quads_sum(x); }
__device__ __forceinline__ float ex2(float x) { return __builtin_amdgcn_exp2f(x); }
__device__ __forceinline__ float silu(float v) { return v * __builtin_amdgcn_rcpf(1.f + ex2(-v * L2E)); }

template <int N> __device__ __forceinline__ void wait_vm() {
  __builtin_amdgcn_sched_barrier(0);
  asm volatile("s_waitcnt vmcnt(%0)" ::"i"(N) : "memory");
}
__device__ __forceinline__ void wait_lds() { asm volatile("s_waitcnt lgkmcnt(0)" ::: "memory"); }
__device__ __forceinline__ void wg_barrier() {
  asm volatile("s_waitcnt lgkmcnt(0)\n\ts_barrier" ::: "memory");
}
__device__ __forceinline__ void lds_wait_nonzero(unsigned addr) {
#pragma unroll 1
  for (unsigned spin = 0; spin < SPIN_CAP * 16 && *(volatile LAS unsigned*)(size_t)addr == 0; ++spin) __builtin_amdgcn_s_sleep(1);
  asm volatile("" ::: "memory");
}
__device__ __forceinline__ unsigned now32() { return (unsigned)__builtin_amdgcn_s_memrealtime(); }
__device__ __forceinline__ void sleep_until(unsigned due) { while ((int)(now32() - due) < 0) __builtin_amdgcn_s_sleep(2); }
__device__ __forceinline__ rsrc_t make_rsrc(const void* p, int bytes) {
  return __builtin_amdgcn_make_buffer_rsrc((void*)p, (short)0, bytes, 0x00020000);
}
constexpr int SC1 = 16;
__device__ __forceinline__ u32x4 ld_dev(rsrc_t r, unsigned voff, unsigned soff) { return __builtin_amdgcn_raw_buffer_load_b128(r, voff, soff, SC1); }
__device__ __forceinline__ void st_dev(rsrc_t r, u32x4 v, unsigned voff, unsigned soff) { __builtin_amdgcn_raw_buffer_store_b128(v, r, voff, soff, SC1); }
__device__ __forceinline__ bool pair_ok(const u32x4& g, unsigned tag) { return (g[1] == tag) & (g[3] == tag); }
__device__ __forceinline__ bool quad_ok(const u32x4& g, unsigned tag) {
  return (__builtin_amdgcn_perm(g[1], g[0], 0x0c0c0400u) | __builtin_amdgcn_perm(g[3], g[2], 0x04000c0cu)) == tag;
}
__device__ __forceinline__ float quad_val(unsigned w) { return __uint_as_float(w & 0xffffff00u); }
struct QuadOut {
  unsigned t0, t1, t2, t3, voff;
  __device__ __forceinline__ QuadOut(unsigned tag, int lane) : t0(tag & 255u), t1((tag >> 8) & 255u), t2((tag >> 16) & 255u), t3(tag >> 24), voff(lane & 1 ? OOB : lane * 8) {
    asm volatile("" : "+v"(t0), "+v"(t1), "+v"(t2), "+v"(t3), "+v"(voff));
  }
  __device__ __forceinline__ void store(rsrc_t r, float a, float b, unsigned soff) const {
    const float c = dpp<XOR1>(a), d = dpp<XOR1>(b);
    st_dev(r, u32x4{(__float_as_uint(a) & 0xffffff00u) | t0, (__float_as_uint(b) & 0xffffff00u) | t1, (__float_as_uint(c) & 0xffffff00u) | t2,
                    (__float_as_uint(d) & 0xffffff00u) | t3}, voff, soff);
  }
};
__device__ __forceinline__ float quad_sum(float v0, float v1, float v2, float v3) {
  float x = swap16_sum(swap32_sum(v0, v2), swap32_sum(v1, v3));
  x += dpp<ROR8>(x); x += dpp<ROR4>(x); x += dpp<XOR2>(x); x += dpp<XOR1>(x);
  return x;
}
__device__ __forceinline__ s16x4 tr_read(unsigned addr) { return __builtin_amdgcn_ds_read_tr16_b64_v4i16((LAS s16x4*)(size_t)addr); }
template <typename T> __device__ __forceinline__ T lds_ld(unsigned addr) { return *(const LAS T*)(size_t)addr; }
template <typename T> __device__ __forceinline__ void lds_st(unsigned addr, T v) { *(LAS T*)(size_t)addr = v; }

__device__ __forceinline__ void dma_piece(rsrc_t r, unsigned voff, unsigned soff, unsigned dst) {
  asm volatile("s_mov_b32 m0, %0\n\ts_nop 0\n\tbuffer_load_dwordx4 %1, %2, %3 offen nt lds" ::"s"(dst), "v"(voff), "s"(r), "s"(soff) : "memory");
}
__device__ __forceinline__ void dma_half(rsrc_t r, unsigned v0, unsigned v1, unsigned v2, unsigned v3, unsigned soff, unsigned dst) {
  asm volatile(
      "s_mov_b32 m0, %0\n\ts_nop 0\n\t"
      "buffer_load_dwordx4 %1, %5, %6 offen nt lds\n\t"
      "buffer_load_dwordx4 %2, %5, %6 offen offset:1024 nt lds\n\t"
      "buffer_load_dwordx4 %3, %5, %6 offen offset:2048 nt lds\n\t"
      "buffer_load_dwordx4 %4, %5, %6 offen offset:3072 nt lds"
      ::"s"(dst), "v"(v0), "v"(v1), "v"(v2), "v"(v3), "s"(r), "s"(soff)
      : "memory");
}
template <int A> __device__ __forceinline__ f32x4 mfma4(s16x4 a, s16x4 b, f32x4 c) { return __builtin_amdgcn_mfma_f32_4x4x4bf16_1k(a, b, c, 4, A, 0); }
template <int A, int N> struct Proj {
  static __device__ __forceinline__ void run(const s16x4 (&av)[2], unsigned ba, f32x4 (&c)[4]) {
    c[A & 3] = mfma4<A & 15>(av[A >> 4], tr_read(ba + A * 512), c[A & 3]);
    if constexpr (A + 1 < N) Proj<A + 1, N>::run(av, ba, c);
  }
};
template <int A> struct ProjReg {
  static __device__ __forceinline__ void run(s16x4 a, const s16x4 (&w)[16], f32x4 (&c)[2]) {
    c[A & 1] = mfma4<A>(a, w[A], c[A & 1]);
    if constexpr (A + 1 < 16) ProjReg<A + 1>::run(a, w, c);
  }
};
__device__ __forceinline__ f32x2 proj64(unsigned a_img, int a_stride, unsigned w_img, int lane) {
  const int q = (lane & 15) >> 2, p = lane & 3;
  const unsigned aa = a_img + (lane & 1) * a_stride + (lane >> 2) * 8;
  const s16x4 av[2] = {lds_ld<s16x4>(aa), lds_ld<s16x4>(aa + 128)};
  const unsigned ba = w_img + q * 128 + (((2 * (lane >> 4) + (p >> 1)) ^ ((q >> 1) << 2)) << 4) + (p & 1) * 8;
  f32x4 c[4] = {};
  Proj<0, 32>::run(av, ba, c);
  return f32x2{(c[0][0] + c[1][0]) + (c[2][0] + c[3][0]), (c[0][1] + c[1][1]) + (c[2][1] + c[3][1])};
}
__device__ __forceinline__ f32x4 gather_quarters(rsrc_t rws, unsigned base, int quarter_bytes, unsigned tag, int lane) {
  u32x4 g;
#pragma unroll 1
  for (unsigned spin = 0; spin < SPIN_CAP; ++spin) {
    asm volatile("" ::: "memory");
    g = ld_dev(rws, (lane >> 4) * quarter_bytes + (lane & 7) * 16, base);
    if (__all(quad_ok(g, tag))) break;
    __builtin_amdgcn_s_sleep(8);
  }
  return f32x4{rows_sum(quad_val(g[0])), rows_sum(quad_val(g[1])), rows_sum(quad_val(g[2])), rows_sum(quad_val(g[3]))};
}

template <int N> using Int = std::integral_constant<int, N>;
struct KFrag { bf16x8 k[4]; };
struct VFrag { s16x4 v[4][2]; };

__device__ __forceinline__ void read_k(KFrag& t, const unsigned (&ka)[4], unsigned off) {
#pragma unroll
  for (int s = 0; s < 4; ++s) t.k[s] = lds_ld<bf16x8>(ka[s] + off);
  asm volatile("s_waitcnt lgkmcnt(0)" : "+v"(t.k[0]), "+v"(t.k[1]), "+v"(t.k[2]), "+v"(t.k[3])::"memory");
}
__device__ __forceinline__ void read_v(VFrag& t, const unsigned (&va)[2], unsigned off) {
#pragma unroll
  for (int a = 0; a < 4; ++a)
#pragma unroll
    for (int h = 0; h < 2; ++h) t.v[a][h] = tr_read(va[h] + off + a * 1024);
  asm volatile("s_waitcnt lgkmcnt(0)"
               : "+v"(t.v[0][0]), "+v"(t.v[0][1]), "+v"(t.v[1][0]), "+v"(t.v[1][1]), "+v"(t.v[2][0]), "+v"(t.v[2][1]), "+v"(t.v[3][0]), "+v"(t.v[3][1])
               :
               : "memory");
}
__device__ __forceinline__ void score_tile(const KFrag& t, const bf16x8 (&qf)[4], float& m, unsigned dst, bool writer) {
  f32x4 sc = {0.f, 0.f, 0.f, 0.f};
#pragma unroll
  for (int s = 0; s < 4; ++s) sc = __builtin_amdgcn_mfma_f32_16x16x32_bf16(t.k[s], qf[s], sc, 0, 0, 0);
  m = fmaxf(fmaxf(m, fmaxf(sc[0], sc[1])), fmaxf(sc[2], sc[3]));
  const unsigned r0 = pk2(sc[0], sc[1]), r1 = pk2(sc[2], sc[3]);
  if (writer) lds_st<u32x2>(dst, u32x2{pk2(bflo(r0) * SCALE, bfhi(r0) * SCALE), pk2(bflo(r1) * SCALE, bfhi(r1) * SCALE)});
}
__device__ __forceinline__ s16x4 probs(unsigned src, float M, float f) {
  const u32x2 xp = lds_ld<u32x2>(src);
  const u32x2 pp = {pk2(f * ex2((bflo(xp[0]) - M) * L2E), f * ex2((bfhi(xp[0]) - M) * L2E)), pk2(f * ex2((bflo(xp[1]) - M) * L2E), f * ex2((bfhi(xp[1]) - M) * L2E))};
  return __builtin_bit_cast(s16x4, pp);
}
__device__ __forceinline__ void value_tile(const VFrag& t, s16x4 pa, f32x4 (&o)[2]) {
#pragma unroll
  for (int h = 0; h < 2; ++h) o[h] = mfma4<0>(pa, t.v[0][h], o[h]);
#pragma unroll
  for (int h = 0; h < 2; ++h) o[h] = mfma4<4>(pa, t.v[1][h], o[h]);
#pragma unroll
  for (int h = 0; h < 2; ++h) o[h] = mfma4<8>(pa, t.v[2][h], o[h]);
#pragma unroll
  for (int h = 0; h < 2; ++h) o[h] = mfma4<12>(pa, t.v[3][h], o[h]);
}

__global__ void __launch_bounds__(NTHR, 2)
fused_decode_kernel(const u16* __restrict__ x, const u16* __restrict__ kc, const u16* __restrict__ vc, const u16* __restrict__ wq,
                    const u16* __restrict__ wo, const u16* __restrict__ w1, const u16* __restrict__ w2, u16* __restrict__ out, char* ws) {
  unsigned long long pc;
  asm volatile("s_getpc_b64 %0" : "=s"(pc));
  extern __shared__ __attribute__((aligned(16))) unsigned char lds[];
  const unsigned lds0 = (unsigned)(size_t)(LAS unsigned char*)lds;
  const int lane = threadIdx.x & 63, wave = __builtin_amdgcn_readfirstlane(threadIdx.x >> 6), wg = blockIdx.x;
  const int xg = wg & 7, xi = wg >> 3;
  asm volatile("" ::"s"(x), "s"(kc), "s"(vc), "s"(wq), "s"(wo), "s"(w1), "s"(w2), "s"(out), "s"(ws));
  const rsrc_t rws = make_rsrc(ws, WS_END);
  const unsigned sw128 = ((lane & 7) ^ (((lane >> 4) & 1) << 2)) << 4;

  if (wave < 4) {
    const rsrc_t rk = make_rsrc(kc, KV_BYTES), rv = make_rsrc(vc, KV_BYTES);
    const int g = lane >> 4, c = lane & 15;
    unsigned vk[4];
#pragma unroll
    for (int u = 0; u < 4; ++u) vk[u] = g * 256 + ((c ^ (4 * u + g)) << 4);
    const unsigned vv = g * 256 + ((c ^ (g << 2)) << 4);
    const unsigned ring = lds0 + L_RING + wave * (NSLOT * 4096);
    const unsigned tile0 = (unsigned)wg * (2048 * 256) + wave * 4096;
    unsigned ka[4], va[2];
#pragma unroll
    for (int s = 0; s < 4; ++s) ka[s] = ring + c * 256 + (((4 * s + g) ^ c) << 4);
#pragma unroll
    for (int h = 0; h < 2; ++h) va[h] = ring + (c >> 2) * 256 + (((8 * h + 2 * g + ((c & 3) >> 1)) ^ ((c >> 2) << 2)) << 4) + (c & 1) * 8;
    const unsigned sca = lds0 + L_SC + wave * 4096 + (g * 4 + (c & 3)) * 8;
    auto issue_k = [&](int t, unsigned off) { dma_half(rk, vk[0], vk[1], vk[2], vk[3], tile0 + t * 16384, ring + off); };
    auto issue_v = [&](int t, unsigned off) { dma_half(rv, vv, vv, vv, vv, tile0 + t * 16384, ring + off); };

    wg_barrier();
#pragma unroll
    for (int u = 0; u < AHEAD; ++u) { wait_vm<WIN>(); issue_k(u, u * 4096); }
    KFrag pk[NPRE];
#pragma unroll
    for (int u = 0; u < NPRE; ++u) {
      wait_vm<WIN>();
      issue_k(u + AHEAD, ((u + AHEAD) % NSLOT) * 4096);
      read_k(pk[u], ka, (u % NSLOT) * 4096);
    }
    lds_wait_nonzero(lds0 + L_QR);
    bf16x8 qf[4];
#pragma unroll
    for (int s = 0; s < 4; ++s) qf[s] = lds_ld<bf16x8>(lds0 + L_QI + (c & 3) * 256 + (4 * s + g) * 16);
    const unsigned tag = lds_ld<unsigned>(lds0 + L_TAG);

    unsigned prev = now32();
    auto landed = [&](int u, auto win) {
      const u32x2 plan = *(volatile LAS u32x2*)(size_t)(lds0 + L_TL);
      wait_vm<win.value>();
      unsigned tn = now32();
      *(volatile LAS u32x2*)(size_t)(lds0 + L_TP + wave * 8) = u32x2{(unsigned)u, tn};
      if (u == CPA) *(volatile LAS unsigned*)(size_t)(lds0 + L_TS + wave * 4) = tn | 1u;
      const unsigned tl = __builtin_amdgcn_readfirstlane(plan[0]);
      const int d = (int)(tl - prev);
      if (tl != 0 && d > 0) {
        const float n = (float)(NUNIT - AHEAD - u);
        const bool behind = (float)d + DELTA < n * __uint_as_float(plan[1]);
        const unsigned due = prev + (unsigned)__builtin_amdgcn_readfirstlane(behind ? 0 : (int)((float)d * __builtin_amdgcn_rcpf(n)));
        while ((int)(tn - due) < 0) { __builtin_amdgcn_s_sleep(1); tn = now32(); }
      }
      prev = tn + OVER;
    };
    static_assert(WIN <= 16 && WIN2 <= 16 && 2 * NPRE <= NTILE - AHEAD && CPA >= NPRE, "unit schedule");
    float m = M_INIT;
    KFrag kf;
#pragma unroll
    for (int u = NPRE; u < 2 * NPRE; ++u) {
      landed(u, Int<WIN>{}); issue_k(u + AHEAD, ((u + AHEAD) % NSLOT) * 4096);
      read_k(kf, ka, (u % NSLOT) * 4096);
      score_tile(kf, qf, m, sca + u * 128, c < 4);
      score_tile(pk[u - NPRE], qf, m, sca + (u - NPRE) * 128, c < 4);
    }
    unsigned off = ((2 * NPRE) % NSLOT) * 4096, offp = ((2 * NPRE + AHEAD) % NSLOT) * 4096;
    auto next_slot = [&]() { offp = off; off = off == (NSLOT - 1) * 4096 ? 0u : off + 4096; };
#pragma unroll 1
    for (int u = 2 * NPRE; u < NTILE - AHEAD; ++u) {
      landed(u, Int<WIN>{}); issue_k(u + AHEAD, offp);
      read_k(kf, ka, off);
      score_tile(kf, qf, m, sca + u * 128, c < 4);
      next_slot();
    }
#pragma unroll 1
    for (int u = NTILE - AHEAD; u < NTILE; ++u) {
      landed(u, Int<WIN2>{}); issue_v(u + AHEAD - NTILE, offp);
      read_k(kf, ka, off);
      score_tile(kf, qf, m, sca + u * 128, c < 4);
      next_slot();
    }
    const float M = rbf(rbf(rows_max(m)) * SCALE);
    if (lane < 4) lds_st<float>(lds0 + L_ML + lane * 16 + wave * 4, M);
    wait_lds();
    if (lane == 0) *(volatile LAS unsigned*)(size_t)(lds0 + L_MR + wave * 4) = 1u;
    f32x4 o[2] = {};
    VFrag vf;
#pragma unroll 1
    for (int u = NTILE; u < NUNIT - AHEAD; ++u) {
      landed(u, Int<WIN2>{}); issue_v(u + AHEAD - NTILE, offp);
      const s16x4 pa = probs(sca + (u - NTILE) * 128, M, 1.f);
      read_v(vf, va, off);
      value_tile(vf, pa, o);
      next_slot();
    }
    const int ks = xg * 8 + (xi >> 2), cs = xi & 3;
    if (wave < 2) {
      const rsrc_t ro = make_rsrc(wo, HQD * DM * 2);
#pragma unroll
      for (int i = 0; i < 8; ++i)
        dma_piece(ro, (lane >> 3) * (DM * 2) + sw128, (ks * 64 + 8 * i) * (DM * 2) + cs * 256 + wave * 128, lds0 + L_WO + wave * 8192 + i * 1024);
    }
    const bool late_w2 = wave >= 2 && xi >= 14 && xi < 28;
    if (late_w2) {
      const int fi = xg * 14 + xi - 14;
      const rsrc_t r2 = make_rsrc(w2, DM * FF * 2);
#pragma unroll
      for (int i = 0; i < 8; ++i)
        dma_piece(r2, (lane >> 3) * (DM * 2) + sw128, ((fi >> 3) * 128 + (wave & 1) * 64 + 8 * i) * (DM * 2) + (fi & 7) * 128, lds0 + L_WF + (wave & 1) * 8192 + i * 1024);
    }
    lds_wait_nonzero(lds0 + L_GR);
    const f32x2 gs = lds_ld<f32x2>(lds0 + L_GS + (c & 3) * 8);
    const float f = ex2((M - gs[0]) * L2E) * gs[1];
    {
      const float f0 = dpp<0x00>(f), f1 = dpp<0x55>(f), f2 = dpp<0xAA>(f), f3 = dpp<0xFF>(f);
#pragma unroll
      for (int h = 0; h < 2; ++h) { o[h][0] *= f0; o[h][1] *= f1; o[h][2] *= f2; o[h][3] *= f3; }
    }
    const QuadOut qo(tag, lane);
    unsigned s_p1 = WS_P1 + wg * 2048 + wave * 512, s_p2 = WS_P2 + ks * (DM * 8) + (cs * 128 + wave * 64) * 8;
    unsigned a_mw = ring + lane * 4, a_mr = lds0 + L_RING + (wave * 128 + 2 * lane) * 4;
    unsigned a_ow = lds0 + L_OS + (wave >> 1) * 128 + ((wave & 1) * 32 + 4 * (lane & 7) + g) * 2, a_or = lds0 + L_OS + (lane & 1) * 128 + (lane >> 2) * 8;
    asm volatile("" : "+s"(s_p1), "+s"(s_p2), "+v"(a_mw), "+v"(a_mr), "+v"(a_ow), "+v"(a_or));
    auto last = [&](auto j) {
      constexpr int u = NUNIT - AHEAD + j.value, younger = (NUNIT - 1 - u) * 4;
      s16x4 pa = probs(sca + (u - NTILE) * 128, M, f);
      asm volatile("" : "+v"(pa));
      if (wave < 2 || late_w2) wait_vm<younger + 8>(); else wait_vm<younger>();
      read_v(vf, va, (u % NSLOT) * 4096);
      value_tile(vf, pa, o);
    };
    last(Int<0>{}); last(Int<1>{}); last(Int<2>{}); last(Int<3>{}); last(Int<4>{});

#pragma unroll
    for (int h = 0; h < 2; ++h)
#pragma unroll
      for (int r = 0; r < 4; ++r) lds_st<float>(a_mw + (r * 128 + h * 64) * 4, o[h][r]);
    wg_barrier();
    {
      f32x2 ov[4];
#pragma unroll
      for (int w = 0; w < 4; ++w) ov[w] = lds_ld<f32x2>(a_mr + w * (NSLOT * 4096));
      const f32x2 sum = (ov[0] + ov[1]) + (ov[2] + ov[3]);
      qo.store(rws, sum[0], sum[1], s_p1);
    }
    s16x4 wf[16];
    if (wave < 2) {
      const unsigned wa = lds0 + L_WO + wave * 8192 + (c >> 2) * 128 + (((2 * g + ((c & 3) >> 1)) ^ ((c >> 3) << 2)) << 4) + (c & 1) * 8;
      wait_vm<1>();
#pragma unroll
      for (int a = 0; a < 16; ++a) wf[a] = tr_read(wa + a * 512);
    }
    if (late_w2) {
      wait_vm<1>();
      if (lane == 0) *(volatile LAS unsigned*)(size_t)(lds0 + L_WR + (wave & 1) * 4) = 1u;
    }
    {
      const int b = wave >> 1, half = wave & 1;
      const unsigned vo = (lane >> 3) * 2048 + (lane & 7) * 16;
      const unsigned so = WS_P1 + (b * 8 + xg) * 16 * 2048 + ((ks >> 1) & 3) * 512 + ((ks & 1) * 2 + half) * 128;
      sleep_until(__builtin_amdgcn_readfirstlane(*(volatile LAS unsigned*)(size_t)(lds0 + L_TL)) + POLL_AT);
      u32x4 gv[2] = {};
      bool h0 = false, h1 = false;
#pragma unroll 1
      for (unsigned spin = 0; spin < SPIN_CAP; ++spin) {
        asm volatile("" ::: "memory");
        if (!h0) gv[0] = ld_dev(rws, vo, so);
        if (!h1) gv[1] = ld_dev(rws, vo + 8 * 2048, so);
        h0 = quad_ok(gv[0], tag); h1 = quad_ok(gv[1], tag);
        if (__all(h0 & h1)) break;
      }
      float u[4];
#pragma unroll
      for (int j = 0; j < 4; ++j) u[j] = quad_val(gv[0][j]) + quad_val(gv[1][j]);
      float e = swap16_sum(swap32_sum(u[0], u[2]), swap32_sum(u[1], u[3]));
      e += dpp<ROR8>(e);
      lds_st<__bf16>(a_ow, (__bf16)e);
    }
    wg_barrier();
    if (wave < 2) {
      const s16x4 oa = lds_ld<s16x4>(a_or);
      f32x4 acc[2] = {};
      ProjReg<0>::run(oa, wf, acc);
      qo.store(rws, acc[0][0] + acc[1][0], acc[0][1] + acc[1][1], s_p2);
    }
  } else {
    __builtin_amdgcn_s_setprio(3);
    const int cg = xg * 32 + xi;
    const int qk = cg >> 6, qc = cg & 63;
    const bool f1 = xi < 14, f2 = xi >= 14 && xi < 28;
    const int fi = xg * 14 + (xi < 14 ? xi : xi - 14);
    const int hk = fi / 28, hc = fi % 28;
    const int gk = fi >> 3, gc = fi & 7;
    const unsigned vout = (((lane >> 4) & 1) * DM + cg * 2 + (lane >> 5)) * 2;
    const rsrc_t rx = make_rsrc(x, 2 * DM * 2);
    const rsrc_t rout = make_rsrc(out, 2 * DM * 2);
    unsigned ev, xo;
    u32x4 x0, x1, code;
    asm volatile(
        "s_nop 4\n\t"
        "buffer_load_dwordx4 %0, %5, %6, 0 offen\n\t"
        "buffer_load_dwordx4 %1, %5, %6, 0 offen offset:1024\n\t"
        "buffer_load_dword %2, off, %7, 0 sc1\n\t"
        "buffer_load_ushort %3, %8, %6, 0 offen\n\t"
        "buffer_load_dwordx4 %4, %5, %9, %10 offen"
        : "=&v"(x0), "=&v"(x1), "=&v"(ev), "=&v"(xo), "=&v"(code)
        : "v"(lane * 16), "s"(rx), "s"(rws), "v"(vout), "s"(make_rsrc((const void*)(pc & ~255ull), CODE_KB * 1024)), "s"(xi * 1024)
        : "memory");
    {
      const rsrc_t rq = make_rsrc(wq, DM * HQD * 2);
#pragma unroll
      for (int i = 0; i < 16; ++i) dma_piece(rq, (lane >> 3) * (HQD * 2) + sw128, (qk * 128 + 8 * i) * (HQD * 2) + qc * 128, lds0 + L_WQ + i * 1024);
      __builtin_amdgcn_sched_barrier(0);
      if (f1) {
        const rsrc_t r1 = make_rsrc(w1, DM * FF * 2);
#pragma unroll
        for (int i = 0; i < 16; ++i) dma_piece(r1, (lane >> 3) * (FF * 2) + sw128, (hk * 128 + 8 * i) * (FF * 2) + hc * 128, lds0 + L_WF + i * 1024);
      }
    }
    if (lane < 28) lds_st<unsigned>(lds0 + L_TAG + lane * 4, 0u);
    wg_barrier();
    const unsigned th0 = now32();
    if (f1) wait_vm<32>(); else wait_vm<16>();
    asm volatile("" : "+v"(x0), "+v"(x1), "+v"(ev), "+v"(xo), "+v"(code)::"memory");
    const unsigned epoch = max(__builtin_amdgcn_readfirstlane(ev) + 1u, 1u), tag = epoch * 0x9E3779B1u;
    {
      float a[2][8];
#pragma unroll
      for (int j = 0; j < 4; ++j) { a[0][2 * j] = bflo(x0[j]); a[0][2 * j + 1] = bfhi(x0[j]); a[1][2 * j] = bflo(x1[j]); a[1][2 * j + 1] = bfhi(x1[j]); }
#pragma unroll
      for (int b = 0; b < 2; ++b) {
        const float mean = wave_sum(((a[b][0] + a[b][1]) + (a[b][2] + a[b][3])) + ((a[b][4] + a[b][5]) + (a[b][6] + a[b][7]))) * (1.f / DM);
        float q = 0.f;
#pragma unroll
        for (int j = 0; j < 8; ++j) q = fmaf(a[b][j] - mean, a[b][j] - mean, q);
        const float var = wave_sum(q) * (1.f / DM);
        const float mb = rbf(mean), inv = __builtin_amdgcn_rcpf(rbf(__builtin_amdgcn_sqrtf(rbf(rbf(var) + 1e-5f))));
        u32x4 xn;
#pragma unroll
        for (int j = 0; j < 4; ++j) xn[j] = pk2(rbf(a[b][2 * j] - mb) * inv, rbf(a[b][2 * j + 1] - mb) * inv);
        lds_st<u32x4>(lds0 + L_XN + b * 1024 + lane * 16, xn);
      }
    }
    if (lane == 0) lds_st<unsigned>(lds0 + L_TAG, tag);
    const QuadOut qo(tag, lane);
    if (f1) wait_vm<16>(); else wait_vm<0>();
    wait_lds();
    {
      const f32x2 y = proj64(lds0 + L_XN + qk * 256, 1024, lds0 + L_WQ, lane);
      qo.store(rws, y[0], y[1], WS_QP + (qk * HQD + qc * 64) * 8);
    }
    if (f1) {
      wait_vm<1>();
      const f32x2 y = proj64(lds0 + L_XN + hk * 256, 1024, lds0 + L_WF, lane);
      qo.store(rws, y[0], y[1], WS_HP + (hk * FF + hc * 64) * 8);
    }
    {
      const f32x4 q = gather_quarters(rws, WS_QP + (qc * 64 + qk * 16) * 8, HQD * 8, tag, lane);
      if (lane < 8) st_dev(rws, u32x4{pk2(q[0], q[1]), tag, pk2(q[2], q[3]), tag}, lane * 16, WS_Q + (qc * 64 + qk * 16) * 8);
    }
    if (f1) {
      const f32x4 h = gather_quarters(rws, WS_HP + (hc * 64 + hk * 16) * 8, FF * 8, tag, lane);
      if (lane < 8) st_dev(rws, u32x4{pk2(silu(rbf(h[0])), silu(rbf(h[1]))), tag, pk2(silu(rbf(h[2])), silu(rbf(h[3]))), tag}, lane * 16, WS_H + (hc * 64 + hk * 16) * 8);
    }
    {
      const int b = wg >> 7, kvh = (wg >> 4) & 7;
      u32x4 gq[4];
#pragma unroll 1
      for (unsigned spin = 0; spin < SPIN_CAP; ++spin) {
        asm volatile("" ::: "memory");
#pragma unroll
        for (int i = 0; i < 4; ++i) gq[i] = ld_dev(rws, lane * 16 + i * 1024, WS_Q + kvh * 4096);
        if (__all(pair_ok(gq[0], tag) & pair_ok(gq[1], tag) & pair_ok(gq[2], tag) & pair_ok(gq[3], tag))) break;
        __builtin_amdgcn_s_sleep(8);
      }
      const unsigned sel = b ? 0x07060302u : 0x05040100u;
#pragma unroll
      for (int i = 0; i < 4; ++i) lds_st<unsigned>(lds0 + L_QI + i * 256 + lane * 4, __builtin_amdgcn_perm(gq[i][2], gq[i][0], sel));
    }
    wait_lds();
    if (lane == 0) *(volatile LAS unsigned*)(size_t)(lds0 + L_QR) = 1u;

    if (f2) {
      u32x4 gh;
#pragma unroll 1
      for (unsigned spin = 0; spin < SPIN_CAP; ++spin) {
        asm volatile("" ::: "memory");
        gh = ld_dev(rws, lane * 16, WS_H + gk * 1024);
        if (__all(pair_ok(gh, tag))) break;
        __builtin_amdgcn_s_sleep(32);
      }
      lds_st<unsigned>(lds0 + L_HS + lane * 4, __builtin_amdgcn_perm(gh[2], gh[0], 0x05040100u));
      lds_st<unsigned>(lds0 + L_HS + 256 + lane * 4, __builtin_amdgcn_perm(gh[2], gh[0], 0x07060302u));
    }
    {
      sleep_until(th0 + TS2);
      u32x4 ta;
#pragma unroll 1
      for (unsigned spin = 0; spin < SPIN_CAP; ++spin) {
        ta = *(volatile LAS u32x4*)(size_t)(lds0 + L_TS);
        if (__all((ta[0] != 0) & (ta[1] != 0) & (ta[2] != 0) & (ta[3] != 0))) break;
        __builtin_amdgcn_s_sleep(8);
      }
      const u32x4 p0 = *(volatile LAS u32x4*)(size_t)(lds0 + L_TP), p1 = *(volatile LAS u32x4*)(size_t)(lds0 + L_TP + 16);
      {
        const unsigned a = th0 + (((ta[0] - th0) + (ta[1] - th0) + (ta[2] - th0) + (ta[3] - th0)) >> 2);
        const unsigned b = th0 + (((p0[1] - th0) + (p0[3] - th0) + (p1[1] - th0) + (p1[3] - th0)) >> 2);
        const unsigned c4 = p0[0] + p0[2] + p1[0] + p1[2];
        if (lane == 0) st_dev(rws, u32x4{a, tag, (b << 8) | c4, tag}, 0, WS_PC + wg * 128);
      }
      sleep_until(now32() + TSD);
      u32x4 gp;
#pragma unroll 1
      for (unsigned spin = 0; spin < SPIN_CAP; ++spin) {
        asm volatile("" ::: "memory");
        gp = ld_dev(rws, (lane & (NSAMP - 1)) * 128, WS_PC + (wg & ~(NSAMP - 1)) * 128);
        if (__all(pair_ok(gp, tag))) break;
        __builtin_amdgcn_s_sleep(24);
      }
      const float tb = (float)((int)((gp[2] & 0xffffff00u) - (th0 << 8)) >> 8), ta_ = (float)(int)(gp[0] - th0);
      const float cb = 0.25f * (float)(gp[2] & 0xffu);
      const float r = fmaxf(cb - (float)CPA, 0.25f) * __builtin_amdgcn_rcpf(fmaxf(tb - ta_, 25.f)) * RBOOST;
      const float inv = __builtin_amdgcn_rcpf(wave_sum(r));
      const float fin = fminf(fmaxf(wave_sum((float)(NUNIT - 4) - cb + r * tb) * inv, 0.f), 8000.f);
      if (lane == 0) *(volatile LAS u32x2*)(size_t)(lds0 + L_TL) = u32x2{(th0 + (unsigned)((int)fin - PACE_OFF)) | 1u, __float_as_uint(64.f * inv)};
    }
    {
#pragma unroll 1
      for (unsigned spin = 0; spin < SPIN_CAP * 4; ++spin) {
        const u32x4 mr = *(volatile LAS u32x4*)(size_t)(lds0 + L_MR);
        if (__all((mr[0] & mr[1] & mr[2] & mr[3]) != 0)) break;
        __builtin_amdgcn_s_sleep(8);
      }
      asm volatile("" ::: "memory");
      __builtin_amdgcn_s_setprio(0);
      const f32x4 ma = lds_ld<f32x4>(lds0 + L_ML + (lane & 1) * 32), mb = lds_ld<f32x4>(lds0 + L_ML + (lane & 1) * 32 + 16);
      const float Ma = fmaxf(fmaxf(ma[0], ma[1]), fmaxf(ma[2], ma[3])), Mb = fmaxf(fmaxf(mb[0], mb[1]), fmaxf(mb[2], mb[3]));
      float La = 0.f, Lb = 0.f;
#pragma unroll 4
      for (int i = 0; i < 16; ++i) {
        const u32x4 xp = lds_ld<u32x4>(lds0 + L_SC + i * 1024 + lane * 16);
        La += (ex2((bflo(xp[0]) - Ma) * L2E) + ex2((bfhi(xp[0]) - Ma) * L2E)) + (ex2((bflo(xp[1]) - Ma) * L2E) + ex2((bfhi(xp[1]) - Ma) * L2E));
        Lb += (ex2((bflo(xp[2]) - Mb) * L2E) + ex2((bfhi(xp[2]) - Mb) * L2E)) + (ex2((bflo(xp[3]) - Mb) * L2E) + ex2((bfhi(xp[3]) - Mb) * L2E));
      }
      La += dpp<XOR2>(La); Lb += dpp<XOR2>(Lb);
      La = quads_sum(La); Lb = quads_sum(Lb);
      __builtin_amdgcn_s_setprio(3);
      if (lane < 4) st_dev(rws, u32x4{__float_as_uint(lane & 2 ? Mb : Ma), tag, __float_as_uint(lane & 2 ? Lb : La), tag}, ((lane & 1) * 2 + (lane >> 1)) * 16, WS_ML + wg * 128);
      sleep_until(now32() + ML_DELAY);
      u32x4 gm;
#pragma unroll 1
      for (unsigned spin = 0; spin < SPIN_CAP; ++spin) {
        asm volatile("" ::: "memory");
        gm = ld_dev(rws, (lane >> 2) * 128 + (lane & 3) * 16, WS_ML + (wg & ~15) * 128);
        if (__all(pair_ok(gm, tag))) break;
        __builtin_amdgcn_s_sleep(32);
      }
      const float ms = __uint_as_float(gm[0]), Mg = quads_max(ms);
      const float inv = __builtin_amdgcn_rcpf(quads_sum(ex2((ms - Mg) * L2E) * __uint_as_float(gm[2])));
      if (lane < 4) lds_st<f32x2>(lds0 + L_GS + lane * 8, f32x2{Mg, inv});
      wait_lds();
      if (lane == 0) *(volatile LAS unsigned*)(size_t)(lds0 + L_GR) = 1u;
    }
    wg_barrier();
    if (f2) {
      lds_wait_nonzero(lds0 + L_WR); lds_wait_nonzero(lds0 + L_WR + 4);
      const f32x2 y = proj64(lds0 + L_HS, 256, lds0 + L_WF, lane);
      qo.store(rws, y[0], y[1], WS_FG + (gk * DM + gc * 64) * 8);
    }
    float xr = bflo(xo);
    unsigned vst = (lane & 15) == 0 ? vout : OOB;
    asm volatile("" : "+v"(xr), "+v"(vst) : "s"(rout));
    wg_barrier();
    float gate;
    {
      u32x4 gf;
#pragma unroll 1
      for (unsigned spin = 0; spin < SPIN_CAP; ++spin) {
        asm volatile("" ::: "memory");
        gf = ld_dev(rws, (lane < 14 ? lane : 0) * (DM * 8), WS_FG + cg * 16);
        if (__all(quad_ok(gf, tag))) break;
      }
      if (lane >= 14) gf = u32x4{0u, 0u, 0u, 0u};
      gate = rbf(silu(rbf(quad_sum(quad_val(gf[0]), quad_val(gf[1]), quad_val(gf[2]), quad_val(gf[3])))));
      asm volatile("" : "+v"(gate));
    }
    {
      u32x4 gs = {};
      bool have = false;
#pragma unroll 1
      for (unsigned spin = 0; spin < SPIN_CAP; ++spin) {
        asm volatile("" ::: "memory");
        if (!have) gs = ld_dev(rws, lane * (DM * 8), WS_P2 + cg * 16);
        have = quad_ok(gs, tag);
        if (__all(have)) break;
      }
      const float s = rbf(quad_sum(quad_val(gs[0]), quad_val(gs[1]), quad_val(gs[2]), quad_val(gs[3])));
      __builtin_amdgcn_raw_buffer_store_b16(__builtin_bit_cast(u16, (__bf16)(xr + rbf(s * gate))), rout, vst, 0, 2);
      if (wg == 0 && lane == 0) __builtin_amdgcn_raw_buffer_store_b32(epoch, rws, 0, WS_EPOCH, SC1);
    }
  }
}

}

extern "C" void kernel_launch(void* const* d_in, const int* in_sizes, int n_in, void* d_out, int out_size, void* d_ws, size_t ws_size,
                              hipStream_t stream) {
  (void)hipFuncSetAttribute((const void*)fused_decode_kernel, hipFuncAttributeMaxDynamicSharedMemorySize, L_END);
  fused_decode_kernel<<<NWG, NTHR, L_END, stream>>>((const u16*)d_in[0], (const u16*)d_in[1], (const u16*)d_in[2], (const u16*)d_in[3],
                                                    (const u16*)d_in[4], (const u16*)d_in[6], (const u16*)d_in[7], (u16*)d_out, (char*)d_ws);
}
```

```cpp
#include <hip/hip_runtime.h>
#include <hip/hip_bf16.h>
#include <type_traits>

namespace {

typedef unsigned short u16;
typedef short s16x4 __attribute__((ext_vector_type(4)));
typedef __bf16 bf16x8 __attribute__((ext_vector_type(8)));
typedef __bf16 bf16x2 __attribute__((ext_vector_type(2)));
typedef float f32x2 __attribute__((ext_vector_type(2)));
typedef float f32x4 __attribute__((ext_vector_type(4)));
typedef unsigned u32x2 __attribute__((ext_vector_type(2)));
typedef unsigned u32x4 __attribute__((ext_vector_type(4)));
typedef __amdgpu_buffer_rsrc_t rsrc_t;
#define LAS __attribute__((address_space(3)))

constexpr int NWG = 256, NTHR = 320;
constexpr int DM = 512, HQD = 4096, SK = 32768, DH = 128, FF = 1792;
constexpr int NSLOT = 6, AHEAD = NSLOT - 1, NTILE = 32, NUNIT = 2 * NTILE, NPRE = 11;
constexpr int WIN = 4, WIN2 = 8;
constexpr int CODE_KB = 29;
constexpr int CPA = 13, TS2 = 1430, TSD = 100, PACE_OFF = 79, OVER = 4, NSAMP = 32, POLL_AT = 200, ML_DELAY = 100;
constexpr float RBOOST = 0.99f, DELTA = 55.f;
constexpr int KV_BYTES = 2 * 8 * SK * DH * 2;
constexpr float SCALE = 0.08838834764831845f, L2E = 1.4426950408889634f;
constexpr float M_INIT = -3.0e38f;
constexpr unsigned SPIN_CAP = 1u << 18;
constexpr unsigned OOB = 0x80000000u;

constexpr int WS_EPOCH = 0;
constexpr int WS_Q = 4096;
constexpr int WS_H = WS_Q + HQD * 8;
constexpr int WS_ML = 65536;
constexpr int WS_PC = WS_ML + NWG * 128;
constexpr int WS_QP = WS_PC + NWG * 128;
constexpr int WS_HP = WS_QP + 4 * HQD * 8;
constexpr int WS_FG = WS_HP + 4 * FF * 8;
constexpr int WS_P2 = WS_FG + 14 * DM * 8;
constexpr int WS_P1 = WS_P2 + 64 * DM * 8;
constexpr int WS_END = WS_P1 + NWG * 2048;
static_assert(WS_H + FF * 8 <= WS_ML, "workspace map");

constexpr int L_RING = 0;
constexpr int L_WQ = 4 * NSLOT * 4096;
constexpr int L_SC = L_WQ;
constexpr int L_WF = L_WQ + 16384;
constexpr int L_WO = L_WF + 16384;
constexpr int L_XN = L_WO + 16384;
constexpr int L_QI = L_XN + 2048;
constexpr int L_OS = L_QI + 1024;
constexpr int L_HS = L_OS + 256;
constexpr int L_ML = L_HS + 512;
constexpr int L_TAG = L_ML + 128;
constexpr int L_QR = L_TAG + 4;
constexpr int L_TL = L_TAG + 16;
constexpr int L_TS = L_TL + 16;
constexpr int L_TP = L_TS + 16;
constexpr int L_MR = L_TP + 32;
constexpr int L_GR = L_MR + 16;
constexpr int L_WR = L_GR + 8;
constexpr int L_GS = L_GR + 16;
constexpr int L_END = L_GS + 32;
static_assert(L_GS - L_TAG == 28 * 4 && L_END <= 160 * 1024, "LDS map");

__device__ __forceinline__ float rbf(float f) { return (float)(__bf16)f; }
__device__ __forceinline__ unsigned pk2(float lo, float hi) {
  bf16x2 v = {(__bf16)lo, (__bf16)hi};
  return __builtin_bit_cast(unsigned, v);
}
__device__ __forceinline__ float bflo(unsigned u) { return __uint_as_float(u << 16); }
__device__ __forceinline__ float bfhi(unsigned u) { return __uint_as_float(u & 0xffff0000u); }
template <int CTRL> __device__ __forceinline__ float dpp(float x) {
  return __builtin_bit_cast(float, __builtin_amdgcn_mov_dpp(__builtin_bit_cast(int, x), CTRL, 0xf, 0xf, true));
}
constexpr int XOR1 = 0xB1, XOR2 = 0x4E, ROR4 = 0x124, ROR8 = 0x128;
__device__ __forceinline__ float swap16_sum(float a, float b) {
  auto s = __builtin_amdgcn_permlane16_swap(__float_as_uint(a), __float_as_uint(b), false, false);
  return __uint_as_float(s[0]) + __uint_as_float(s[1]);
}
__device__ __forceinline__ float swap32_sum(float a, float b) {
  auto s = __builtin_amdgcn_permlane32_swap(__float_as_uint(a), __float_as_uint(b), false, false);
  return __uint_as_float(s[0]) + __uint_as_float(s[1]);
}
__device__ __forceinline__ float rows_sum(float x) { x = swap16_sum(x, x); return swap32_sum(x, x); }
__device__ __forceinline__ float rows_max(float x) {
  auto s = __builtin_amdgcn_permlane16_swap(__float_as_uint(x), __float_as_uint(x), false, false);
  x = fmaxf(__uint_as_float(s[0]), __uint_as_float(s[1]));
  auto t = __builtin_amdgcn_permlane32_swap(__float_as_uint(x), __float_as_uint(x), false, false);
  return fmaxf(__uint_as_float(t[0]), __uint_as_float(t[1]));
}
__device__ __forceinline__ float quads_sum(float x) { x += dpp<ROR4>(x); x += dpp<ROR8>(x); return rows_sum(x); }
__device__ __forceinline__ float quads_max(float x) { x = fmaxf(x, dpp<ROR4>(x)); x = fmaxf(x, dpp<ROR8>(x)); return rows_max(x); }
__device__ __forceinline__ float wave_sum(float x) { x += dpp<XOR1>(x); x += dpp<XOR2>(x); return quads_sum(x); }
__device__ __forceinline__ float ex2(float x) { return __builtin_amdgcn_exp2f(x); }
__device__ __forceinline__ float silu(float v) { return v * __builtin_amdgcn_rcpf(1.f + ex2(-v * L2E)); }

template <int N> __device__ __forceinline__ void wait_vm() {
  __builtin_amdgcn_sched_barrier(0);
  asm volatile("s_waitcnt vmcnt(%0)" ::"i"(N) : "memory");
}
__device__ __forceinline__ void wait_lds() { asm volatile("s_waitcnt lgkmcnt(0)" ::: "memory"); }
__device__ __forceinline__ void wg_barrier() {
  asm volatile("s_waitcnt lgkmcnt(0)\n\ts_barrier" ::: "memory");
}
__device__ __forceinline__ void lds_wait_nonzero(unsigned addr) {
#pragma unroll 1
  for (unsigned spin = 0; spin < SPIN_CAP * 16 && *(volatile LAS unsigned*)(size_t)addr == 0; ++spin) __builtin_amdgcn_s_sleep(1);
  asm volatile("" ::: "memory");
}
__device__ __forceinline__ unsigned now32() { return (unsigned)__builtin_amdgcn_s_memrealtime(); }
__device__ __forceinline__ void sleep_until(unsigned due) { while ((int)(now32() - due) < 0) __builtin_amdgcn_s_sleep(2); }
__device__ __forceinline__ rsrc_t make_rsrc(const void* p, int bytes) {
  return __builtin_amdgcn_make_buffer_rsrc((void*)p, (short)0, bytes, 0x00020000);
}
constexpr int SC1 = 16;
__device__ __forceinline__ u32x4 ld_dev(rsrc_t r, unsigned voff, unsigned soff) { return __builtin_amdgcn_raw_buffer_load_b128(r, voff, soff, SC1); }
__device__ __forceinline__ void st_dev(rsrc_t r, u32x4 v, unsigned voff, unsigned soff) { __builtin_amdgcn_raw_buffer_store_b128(v, r, voff, soff, SC1); }
__device__ __forceinline__ bool pair_ok(const u32x4& g, unsigned tag) { return (g[1] == tag) & (g[3] == tag); }
__device__ __forceinline__ bool quad_ok(const u32x4& g, unsigned tag) {
  return (__builtin_amdgcn_perm(g[1], g[0], 0x0c0c0400u) | __builtin_amdgcn_perm(g[3], g[2], 0x04000c0cu)) == tag;
}
__device__ __forceinline__ float quad_val(unsigned w) { return __uint_as_float(w & 0xffffff00u); }
struct QuadOut {
  unsigned t0, t1, t2, t3, voff;
  __device__ __forceinline__ QuadOut(unsigned tag, int lane) : t0(tag & 255u), t1((tag >> 8) & 255u), t2((tag >> 16) & 255u), t3(tag >> 24), voff(lane & 1 ? OOB : lane * 8) {
    asm volatile("" : "+v"(t0), "+v"(t1), "+v"(t2), "+v"(t3), "+v"(voff));
  }
  __device__ __forceinline__ void store(rsrc_t r, float fa, float fb, unsigned soff) const {
    const unsigned a = __float_as_uint(fa) + 0x80u, b = __float_as_uint(fb) + 0x80u;
    const unsigned c = __float_as_uint(dpp<XOR1>(__uint_as_float(a))), d = __float_as_uint(dpp<XOR1>(__uint_as_float(b)));
    st_dev(r, u32x4{(a & 0xffffff00u) | t0, (b & 0xffffff00u) | t1, (c & 0xffffff00u) | t2, (d & 0xffffff00u) | t3}, voff, soff);
  }
};
__device__ __forceinline__ float quad_sum(float v0, float v1, float v2, float v3) {
  float x = swap16_sum(swap32_sum(v0, v2), swap32_sum(v1, v3));
  x += dpp<ROR8>(x); x += dpp<ROR4>(x); x += dpp<XOR2>(x); x += dpp<XOR1>(x);
  return x;
}
__device__ __forceinline__ s16x4 tr_read(unsigned addr) { return __builtin_amdgcn_ds_read_tr16_b64_v4i16((LAS s16x4*)(size_t)addr); }
template <typename T> __device__ __forceinline__ T lds_ld(unsigned addr) { return *(const LAS T*)(size_t)addr; }
template <typename T> __device__ __forceinline__ void lds_st(unsigned addr, T v) { *(LAS T*)(size_t)addr = v; }

__device__ __forceinline__ void dma_piece(rsrc_t r, unsigned voff, unsigned soff, unsigned dst) {
  asm volatile("s_mov_b32 m0, %0\n\ts_nop 0\n\tbuffer_load_dwordx4 %1, %2, %3 offen nt lds" ::"s"(dst), "v"(voff), "s"(r), "s"(soff) : "memory");
}
__device__ __forceinline__ void dma_half(rsrc_t r, unsigned v0, unsigned v1, unsigned v2, unsigned v3, unsigned soff, unsigned dst) {
  asm volatile(
      "s_mov_b32 m0, %0\n\ts_nop 0\n\t"
      "buffer_load_dwordx4 %1, %5, %6 offen nt lds\n\t"
      "buffer_load_dwordx4 %2, %5, %6 offen offset:1024 nt lds\n\t"
      "buffer_load_dwordx4 %3, %5, %6 offen offset:2048 nt lds\n\t"
      "buffer_load_dwordx4 %4, %5, %6 offen offset:3072 nt lds"
      ::"s"(dst), "v"(v0), "v"(v1), "v"(v2), "v"(v3), "s"(r), "s"(soff)
      : "memory");
}
template <int A> __device__ __forceinline__ f32x4 mfma4(s16x4 a, s16x4 b, f32x4 c) { return __builtin_amdgcn_mfma_f32_4x4x4bf16_1k(a, b, c, 4, A, 0); }
template <int A, int N> struct Proj {
  static __device__ __forceinline__ void run(const s16x4 (&av)[2], unsigned ba, f32x4 (&c)[4]) {
    c[A & 3] = mfma4<A & 15>(av[A >> 4], tr_read(ba + A * 512), c[A & 3]);
    if constexpr (A + 1 < N) Proj<A + 1, N>::run(av, ba, c);
  }
};
template <int A> struct ProjReg {
  static __device__ __forceinline__ void run(s16x4 a, const s16x4 (&w)[16], f32x4 (&c)[2]) {
    c[A & 1] = mfma4<A>(a, w[A], c[A & 1]);
    if constexpr (A + 1 < 16) ProjReg<A + 1>::run(a, w, c);
  }
};
__device__ __forceinline__ f32x2 proj64(unsigned a_img, int a_stride, unsigned w_img, int lane) {
  const int q = (lane & 15) >> 2, p = lane & 3;
  const unsigned aa = a_img + (lane & 1) * a_stride + (lane >> 2) * 8;
  const s16x4 av[2] = {lds_ld<s16x4>(aa), lds_ld<s16x4>(aa + 128)};
  const unsigned ba = w_img + q * 128 + (((2 * (lane >> 4) + (p >> 1)) ^ ((q >> 1) << 2)) << 4) + (p & 1) * 8;
  f32x4 c[4] = {};
  Proj<0, 32>::run(av, ba, c);
  return f32x2{(c[0][0] + c[1][0]) + (c[2][0] + c[3][0]), (c[0][1] + c[1][1]) + (c[2][1] + c[3][1])};
}
__device__ __forceinline__ f32x4 gather_quarters(rsrc_t rws, unsigned base, int quarter_bytes, unsigned tag, int lane) {
  u32x4 g;
#pragma unroll 1
  for (unsigned spin = 0; spin < SPIN_CAP; ++spin) {
    asm volatile("" ::: "memory");
    g = ld_dev(rws, (lane >> 4) * quarter_bytes + (lane & 7) * 16, base);
    if (__all(quad_ok(g, tag))) break;
    __builtin_amdgcn_s_sleep(8);
  }
  return f32x4{rows_sum(quad_val(g[0])), rows_sum(quad_val(g[1])), rows_sum(quad_val(g[2])), rows_sum(quad_val(g[3]))};
}

template <int N> using Int = std::integral_constant<int, N>;
struct KFrag { bf16x8 k[4]; };
struct VFrag { s16x4 v[4][2]; };

__device__ __forceinline__ void read_k(KFrag& t, const unsigned (&ka)[4], unsigned off) {
#pragma unroll
  for (int s = 0; s < 4; ++s) t.k[s] = lds_ld<bf16x8>(ka[s] + off);
  asm volatile("s_waitcnt lgkmcnt(0)" : "+v"(t.k[0]), "+v"(t.k[1]), "+v"(t.k[2]), "+v"(t.k[3])::"memory");
}
__device__ __forceinline__ void read_v(VFrag& t, const unsigned (&va)[2], unsigned off) {
#pragma unroll
  for (int a = 0; a < 4; ++a)
#pragma unroll
    for (int h = 0; h < 2; ++h) t.v[a][h] = tr_read(va[h] + off + a * 1024);
  asm volatile("s_waitcnt lgkmcnt(0)"
               : "+v"(t.v[0][0]), "+v"(t.v[0][1]), "+v"(t.v[1][0]), "+v"(t.v[1][1]), "+v"(t.v[2][0]), "+v"(t.v[2][1]), "+v"(t.v[3][0]), "+v"(t.v[3][1])
               :
               : "memory");
}
__device__ __forceinline__ void score_tile(const KFrag& t, const bf16x8 (&qf)[4], float& m, unsigned dst, bool writer) {
  f32x4 sc = {0.f, 0.f, 0.f, 0.f};
#pragma unroll
  for (int s = 0; s < 4; ++s) sc = __builtin_amdgcn_mfma_f32_16x16x32_bf16(t.k[s], qf[s], sc, 0, 0, 0);
  m = fmaxf(fmaxf(m, fmaxf(sc[0], sc[1])), fmaxf(sc[2], sc[3]));
  const unsigned r0 = pk2(sc[0], sc[1]), r1 = pk2(sc[2], sc[3]);
  if (writer) lds_st<u32x2>(dst, u32x2{pk2(bflo(r0) * SCALE, bfhi(r0) * SCALE), pk2(bflo(r1) * SCALE, bfhi(r1) * SCALE)});
}
__device__ __forceinline__ s16x4 probs(unsigned src, float M, float f) {
  const u32x2 xp = lds_ld<u32x2>(src);
  const u32x2 pp = {pk2(f * ex2((bflo(xp[0]) - M) * L2E), f * ex2((bfhi(xp[0]) - M) * L2E)), pk2(f * ex2((bflo(xp[1]) - M) * L2E), f * ex2((bfhi(xp[1]) - M) * L2E))};
  return __builtin_bit_cast(s16x4, pp);
}
__device__ __forceinline__ void value_tile(const VFrag& t, s16x4 pa, f32x4 (&o)[2]) {
#pragma unroll
  for (int h = 0; h < 2; ++h) o[h] = mfma4<0>(pa, t.v[0][h], o[h]);
#pragma unroll
  for (int h = 0; h < 2; ++h) o[h] = mfma4<4>(pa, t.v[1][h], o[h]);
#pragma unroll
  for (int h = 0; h < 2; ++h) o[h] = mfma4<8>(pa, t.v[2][h], o[h]);
#pragma unroll
  for (int h = 0; h < 2; ++h) o[h] = mfma4<12>(pa, t.v[3][h], o[h]);
}

__global__ void __launch_bounds__(NTHR, 2)
fused_decode_kernel(const u16* __restrict__ x, const u16* __restrict__ kc, const u16* __restrict__ vc, const u16* __restrict__ wq,
                    const u16* __restrict__ wo, const u16* __restrict__ w1, const u16* __restrict__ w2, u16* __restrict__ out, char* ws) {
  unsigned long long pc;
  asm volatile("s_getpc_b64 %0" : "=s"(pc));
  extern __shared__ __attribute__((aligned(16))) unsigned char lds[];
  const unsigned lds0 = (unsigned)(size_t)(LAS unsigned char*)lds;
  const int lane = threadIdx.x & 63, wave = __builtin_amdgcn_readfirstlane(threadIdx.x >> 6), wg = blockIdx.x;
  const int xg = wg & 7, xi = wg >> 3;
  asm volatile("" ::"s"(x), "s"(kc), "s"(vc), "s"(wq), "s"(wo), "s"(w1), "s"(w2), "s"(out), "s"(ws));
  const rsrc_t rws = make_rsrc(ws, WS_END);
  const unsigned sw128 = ((lane & 7) ^ (((lane >> 4) & 1) << 2)) << 4;

  if (wave < 4) {
    const rsrc_t rk = make_rsrc(kc, KV_BYTES), rv = make_rsrc(vc, KV_BYTES);
    const int g = lane >> 4, c = lane & 15;
    unsigned vk[4];
#pragma unroll
    for (int u = 0; u < 4; ++u) vk[u] = g * 256 + ((c ^ (4 * u + g)) << 4);
    const unsigned vv = g * 256 + ((c ^ (g << 2)) << 4);
    const unsigned ring = lds0 + L_RING + wave * (NSLOT * 4096);
    const unsigned tile0 = (unsigned)wg * (2048 * 256) + wave * 4096;
    unsigned ka[4], va[2];
#pragma unroll
    for (int s = 0; s < 4; ++s) ka[s] = ring + c * 256 + (((4 * s + g) ^ c) << 4);
#pragma unroll
    for (int h = 0; h < 2; ++h) va[h] = ring + (c >> 2) * 256 + (((8 * h + 2 * g + ((c & 3) >> 1)) ^ ((c >> 2) << 2)) << 4) + (c & 1) * 8;
    const unsigned sca = lds0 + L_SC + wave * 4096 + (g * 4 + (c & 3)) * 8;
    auto issue_k = [&](int t, unsigned off) { dma_half(rk, vk[0], vk[1], vk[2], vk[3], tile0 + t * 16384, ring + off); };
    auto issue_v = [&](int t, unsigned off) { dma_half(rv, vv, vv, vv, vv, tile0 + t * 16384, ring + off); };

    wg_barrier();
#pragma unroll
    for (int u = 0; u < AHEAD; ++u) { wait_vm<WIN>(); issue_k(u, u * 4096); }
    KFrag pk[NPRE];
#pragma unroll
    for (int u = 0; u < NPRE; ++u) {
      wait_vm<WIN>();
      issue_k(u + AHEAD, ((u + AHEAD) % NSLOT) * 4096);
      read_k(pk[u], ka, (u % NSLOT) * 4096);
    }
    lds_wait_nonzero(lds0 + L_QR);
    bf16x8 qf[4];
#pragma unroll
    for (int s = 0; s < 4; ++s) qf[s] = lds_ld<bf16x8>(lds0 + L_QI + (c & 3) * 256 + (4 * s + g) * 16);
    const unsigned tag = lds_ld<unsigned>(lds0 + L_TAG);

    unsigned prev = now32();
    auto landed = [&](int u, auto win) {
      const u32x2 plan = *(volatile LAS u32x2*)(size_t)(lds0 + L_TL);
      wait_vm<win.value>();
      unsigned tn = now32();
      *(volatile LAS u32x2*)(size_t)(lds0 + L_TP + wave * 8) = u32x2{(unsigned)u, tn};
      if (u == CPA) *(volatile LAS unsigned*)(size_t)(lds0 + L_TS + wave * 4) = tn | 1u;
      const unsigned tl = __builtin_amdgcn_readfirstlane(plan[0]);
      const int d = (int)(tl - prev);
      if (tl != 0 && d > 0) {
        const float n = (float)(NUNIT - AHEAD - u);
        const bool behind = (float)d + DELTA < n * __uint_as_float(plan[1]);
        const unsigned due = prev + (unsigned)__builtin_amdgcn_readfirstlane(behind ? 0 : (int)((float)d * __builtin_amdgcn_rcpf(n)));
        while ((int)(tn - due) < 0) { __builtin_amdgcn_s_sleep(1); tn = now32(); }
      }
      prev = tn + OVER;
    };
    static_assert(WIN <= 16 && WIN2 <= 16 && 2 * NPRE <= NTILE - AHEAD && CPA >= NPRE, "unit schedule");
    float m = M_INIT;
    KFrag kf;
#pragma unroll
    for (int u = NPRE; u < 2 * NPRE; ++u) {
      landed(u, Int<WIN>{}); issue_k(u + AHEAD, ((u + AHEAD) % NSLOT) * 4096);
      read_k(kf, ka, (u % NSLOT) * 4096);
      score_tile(kf, qf, m, sca + u * 128, c < 4);
      score_tile(pk[u - NPRE], qf, m, sca + (u - NPRE) * 128, c < 4);
    }
    unsigned off = ((2 * NPRE) % NSLOT) * 4096, offp = ((2 * NPRE + AHEAD) % NSLOT) * 4096;
    auto next_slot = [&]() { offp = off; off = off == (NSLOT - 1) * 4096 ? 0u : off + 4096; };
#pragma unroll 1
    for (int u = 2 * NPRE; u < NTILE - AHEAD; ++u) {
      landed(u, Int<WIN>{}); issue_k(u + AHEAD, offp);
      read_k(kf, ka, off);
      score_tile(kf, qf, m, sca + u * 128, c < 4);
      next_slot();
    }
#pragma unroll 1
    for (int u = NTILE - AHEAD; u < NTILE; ++u) {
      landed(u, Int<WIN2>{}); issue_v(u + AHEAD - NTILE, offp);
      read_k(kf, ka, off);
      score_tile(kf, qf, m, sca + u * 128, c < 4);
      next_slot();
    }
    const float M = rbf(rbf(rows_max(m)) * SCALE);
    if (lane < 4) lds_st<float>(lds0 + L_ML + lane * 16 + wave * 4, M);
    wait_lds();
    if (lane == 0) *(volatile LAS unsigned*)(size_t)(lds0 + L_MR + wave * 4) = 1u;
    f32x4 o[2] = {};
    VFrag vf;
#pragma unroll 1
    for (int u = NTILE; u < NUNIT - AHEAD; ++u) {
      landed(u, Int<WIN2>{}); issue_v(u + AHEAD - NTILE, offp);
      const s16x4 pa = probs(sca + (u - NTILE) * 128, M, 1.f);
      read_v(vf, va, off);
      value_tile(vf, pa, o);
      next_slot();
    }
    const int ks = xg * 8 + (xi >> 2), cs = xi & 3;
    if (wave < 2) {
      const rsrc_t ro = make_rsrc(wo, HQD * DM * 2);
#pragma unroll
      for (int i = 0; i < 8; ++i)
        dma_piece(ro, (lane >> 3) * (DM * 2) + sw128, (ks * 64 + 8 * i) * (DM * 2) + cs * 256 + wave * 128, lds0 + L_WO + wave * 8192 + i * 1024);
    }
    const bool late_w2 = wave >= 2 && xi >= 14 && xi < 28;
    if (late_w2) {
      const int fi = xg * 14 + xi - 14;
      const rsrc_t r2 = make_rsrc(w2, DM * FF * 2);
#pragma unroll
      for (int i = 0; i < 8; ++i)
        dma_piece(r2, (lane >> 3) * (DM * 2) + sw128, ((fi >> 3) * 128 + (wave & 1) * 64 + 8 * i) * (DM * 2) + (fi & 7) * 128, lds0 + L_WF + (wave & 1) * 8192 + i * 1024);
    }
    lds_wait_nonzero(lds0 + L_GR);
    const f32x2 gs = lds_ld<f32x2>(lds0 + L_GS + (c & 3) * 8);
    const float f = ex2((M - gs[0]) * L2E) * gs[1];
    {
      const float f0 = dpp<0x00>(f), f1 = dpp<0x55>(f), f2 = dpp<0xAA>(f), f3 = dpp<0xFF>(f);
#pragma unroll
      for (int h = 0; h < 2; ++h) { o[h][0] *= f0; o[h][1] *= f1; o[h][2] *= f2; o[h][3] *= f3; }
    }
    const QuadOut qo(tag, lane);
    unsigned s_p1 = WS_P1 + wg * 2048 + wave * 512, s_p2 = WS_P2 + ks * (DM * 8) + (cs * 128 + wave * 64) * 8;
    unsigned a_mw = ring + lane * 4, a_mr = lds0 + L_RING + (wave * 128 + 2 * lane) * 4;
    unsigned a_ow = lds0 + L_OS + (wave >> 1) * 128 + ((wave & 1) * 32 + 4 * (lane & 7) + g) * 2, a_or = lds0 + L_OS + (lane & 1) * 128 + (lane >> 2) * 8;
    asm volatile("" : "+s"(s_p1), "+s"(s_p2), "+v"(a_mw), "+v"(a_mr), "+v"(a_ow), "+v"(a_or));
    auto last = [&](auto j) {
      constexpr int u = NUNIT - AHEAD + j.value, younger = (NUNIT - 1 - u) * 4;
      s16x4 pa = probs(sca + (u - NTILE) * 128, M, f);
      asm volatile("" : "+v"(pa));
      if (wave < 2 || late_w2) wait_vm<younger + 8>(); else wait_vm<younger>();
      read_v(vf, va, (u % NSLOT) * 4096);
      value_tile(vf, pa, o);
    };
    last(Int<0>{}); last(Int<1>{}); last(Int<2>{}); last(Int<3>{}); last(Int<4>{});

#pragma unroll
    for (int h = 0; h < 2; ++h)
#pragma unroll
      for (int r = 0; r < 4; ++r) lds_st<float>(a_mw + (r * 128 + h * 64) * 4, o[h][r]);
    wg_barrier();
    {
      f32x2 ov[4];
#pragma unroll
      for (int w = 0; w < 4; ++w) ov[w] = lds_ld<f32x2>(a_mr + w * (NSLOT * 4096));
      const f32x2 sum = (ov[0] + ov[1]) + (ov[2] + ov[3]);
      qo.store(rws, sum[0], sum[1], s_p1);
    }
    s16x4 wf[16];
    if (wave < 2) {
      const unsigned wa = lds0 + L_WO + wave * 8192 + (c >> 2) * 128 + (((2 * g + ((c & 3) >> 1)) ^ ((c >> 3) << 2)) << 4) + (c & 1) * 8;
      wait_vm<1>();
#pragma unroll
      for (int a = 0; a < 16; ++a) wf[a] = tr_read(wa + a * 512);
    }
    if (late_w2) {
      wait_vm<1>();
      if (lane == 0) *(volatile LAS unsigned*)(size_t)(lds0 + L_WR + (wave & 1) * 4) = 1u;
    }
    {
      const int b = wave >> 1, half = wave & 1;
      const unsigned vo = (lane >> 3) * 2048 + (lane & 7) * 16;
      const unsigned so = WS_P1 + (b * 8 + xg) * 16 * 2048 + ((ks >> 1) & 3) * 512 + ((ks & 1) * 2 + half) * 128;
      sleep_until(__builtin_amdgcn_readfirstlane(*(volatile LAS unsigned*)(size_t)(lds0 + L_TL)) + POLL_AT);
      u32x4 gv[2] = {};
      bool h0 = false, h1 = false;
#pragma unroll 1
      for (unsigned spin = 0; spin < SPIN_CAP; ++spin) {
        asm volatile("" ::: "memory");
        if (!h0) gv[0] = ld_dev(rws, vo, so);
        if (!h1) gv[1] = ld_dev(rws, vo + 8 * 2048, so);
        h0 = quad_ok(gv[0], tag); h1 = quad_ok(gv[1], tag);
        if (__all(h0 & h1)) break;
      }
      float u[4];
#pragma unroll
      for (int j = 0; j < 4; ++j) u[j] = quad_val(gv[0][j]) + quad_val(gv[1][j]);
      float e = swap16_sum(swap32_sum(u[0], u[2]), swap32_sum(u[1], u[3]));
      e += dpp<ROR8>(e);
      lds_st<__bf16>(a_ow, (__bf16)e);
    }
    wg_barrier();
    if (wave < 2) {
      const s16x4 oa = lds_ld<s16x4>(a_or);
      f32x4 acc[2] = {};
      ProjReg<0>::run(oa, wf, acc);
      qo.store(rws, acc[0][0] + acc[1][0], acc[0][1] + acc[1][1], s_p2);
    }
  } else {
    __builtin_amdgcn_s_setprio(3);
    const int cg = xg * 32 + xi;
    const int qk = cg >> 6, qc = cg & 63;
    const bool f1 = xi < 14, f2 = xi >= 14 && xi < 28;
    const int fi = xg * 14 + (xi < 14 ? xi : xi - 14);
    const int hk = fi / 28, hc = fi % 28;
    const int gk = fi >> 3, gc = fi & 7;
    const unsigned vout = (((lane >> 4) & 1) * DM + cg * 2 + (lane >> 5)) * 2;
    const rsrc_t rx = make_rsrc(x, 2 * DM * 2);
    const rsrc_t rout = make_rsrc(out, 2 * DM * 2);
    unsigned ev, xo;
    u32x4 x0, x1, code;
    asm volatile(
        "s_nop 4\n\t"
        "buffer_load_dwordx4 %0, %5, %6, 0 offen\n\t"
        "buffer_load_dwordx4 %1, %5, %6, 0 offen offset:1024\n\t"
        "buffer_load_dword %2, off, %7, 0 sc1\n\t"
        "buffer_load_ushort %3, %8, %6, 0 offen\n\t"
        "buffer_load_dwordx4 %4, %5, %9, %10 offen"
        : "=&v"(x0), "=&v"(x1), "=&v"(ev), "=&v"(xo), "=&v"(code)
        : "v"(lane * 16), "s"(rx), "s"(rws), "v"(vout), "s"(make_rsrc((const void*)(pc & ~255ull), CODE_KB * 1024)), "s"(xi * 1024)
        : "memory");
    {
      const rsrc_t rq = make_rsrc(wq, DM * HQD * 2);
#pragma unroll
      for (int i = 0; i < 16; ++i) dma_piece(rq, (lane >> 3) * (HQD * 2) + sw128, (qk * 128 + 8 * i) * (HQD * 2) + qc * 128, lds0 + L_WQ + i * 1024);
      __builtin_amdgcn_sched_barrier(0);
      if (f1) {
        const rsrc_t r1 = make_rsrc(w1, DM * FF * 2);
#pragma unroll
        for (int i = 0; i < 16; ++i) dma_piece(r1, (lane >> 3) * (FF * 2) + sw128, (hk * 128 + 8 * i) * (FF * 2) + hc * 128, lds0 + L_WF + i * 1024);
      }
    }
    if (lane < 28) lds_st<unsigned>(lds0 + L_TAG + lane * 4, 0u);
    wg_barrier();
    const unsigned th0 = now32();
    if (f1) wait_vm<32>(); else wait_vm<16>();
    asm volatile("" : "+v"(x0), "+v"(x1), "+v"(ev), "+v"(xo), "+v"(code)::"memory");
    const unsigned epoch = max(__builtin_amdgcn_readfirstlane(ev) + 1u, 1u), tag = epoch * 0x9E3779B1u;
    {
      float a[2][8];
#pragma unroll
      for (int j = 0; j < 4; ++j) { a[0][2 * j] = bflo(x0[j]); a[0][2 * j + 1] = bfhi(x0[j]); a[1][2 * j] = bflo(x1[j]); a[1][2 * j + 1] = bfhi(x1[j]); }
#pragma unroll
      for (int b = 0; b < 2; ++b) {
        const float mean = wave_sum(((a[b][0] + a[b][1]) + (a[b][2] + a[b][3])) + ((a[b][4] + a[b][5]) + (a[b][6] + a[b][7]))) * (1.f / DM);
        float q = 0.f;
#pragma unroll
        for (int j = 0; j < 8; ++j) q = fmaf(a[b][j] - mean, a[b][j] - mean, q);
        const float var = wave_sum(q) * (1.f / DM);
        const float mb = rbf(mean), inv = __builtin_amdgcn_rcpf(rbf(__builtin_amdgcn_sqrtf(rbf(rbf(var) + 1e-5f))));
        u32x4 xn;
#pragma unroll
        for (int j = 0; j < 4; ++j) xn[j] = pk2(rbf(a[b][2 * j] - mb) * inv, rbf(a[b][2 * j + 1] - mb) * inv);
        lds_st<u32x4>(lds0 + L_XN + b * 1024 + lane * 16, xn);
      }
    }
    if (lane == 0) lds_st<unsigned>(lds0 + L_TAG, tag);
    const QuadOut qo(tag, lane);
    if (f1) wait_vm<16>(); else wait_vm<0>();
    wait_lds();
    {
      const f32x2 y = proj64(lds0 + L_XN + qk * 256, 1024, lds0 + L_WQ, lane);
      qo.store(rws, y[0], y[1], WS_QP + (qk * HQD + qc * 64) * 8);
    }
    if (f1) {
      wait_vm<1>();
      const f32x2 y = proj64(lds0 + L_XN + hk * 256, 1024, lds0 + L_WF, lane);
      qo.store(rws, y[0], y[1], WS_HP + (hk * FF + hc * 64) * 8);
    }
    {
      const f32x4 q = gather_quarters(rws, WS_QP + (qc * 64 + qk * 16) * 8, HQD * 8, tag, lane);
      if (lane < 8) st_dev(rws, u32x4{pk2(q[0], q[1]), tag, pk2(q[2], q[3]), tag}, lane * 16, WS_Q + (qc * 64 + qk * 16) * 8);
    }
    if (f1) {
      const f32x4 h = gather_quarters(rws, WS_HP + (hc * 64 + hk * 16) * 8, FF * 8, tag, lane);
      if (lane < 8) st_dev(rws, u32x4{pk2(silu(rbf(h[0])), silu(rbf(h[1]))), tag, pk2(silu(rbf(h[2])), silu(rbf(h[3]))), tag}, lane * 16, WS_H + (hc * 64 + hk * 16) * 8);
    }
    {
      const int b = wg >> 7, kvh = (wg >> 4) & 7;
      u32x4 gq[4];
#pragma unroll 1
      for (unsigned spin = 0; spin < SPIN_CAP; ++spin) {
        asm volatile("" ::: "memory");
#pragma unroll
        for (int i = 0; i < 4; ++i) gq[i] = ld_dev(rws, lane * 16 + i * 1024, WS_Q + kvh * 4096);
        if (__all(pair_ok(gq[0], tag) & pair_ok(gq[1], tag) & pair_ok(gq[2], tag) & pair_ok(gq[3], tag))) break;
        __builtin_amdgcn_s_sleep(8);
      }
      const unsigned sel = b ? 0x07060302u : 0x05040100u;
#pragma unroll
      for (int i = 0; i < 4; ++i) lds_st<unsigned>(lds0 + L_QI + i * 256 + lane * 4, __builtin_amdgcn_perm(gq[i][2], gq[i][0], sel));
    }
    wait_lds();
    if (lane == 0) *(volatile LAS unsigned*)(size_t)(lds0 + L_QR) = 1u;

    if (f2) {
      u32x4 gh;
#pragma unroll 1
      for (unsigned spin = 0; spin < SPIN_CAP; ++spin) {
        asm volatile("" ::: "memory");
        gh = ld_dev(rws, lane * 16, WS_H + gk * 1024);
        if (__all(pair_ok(gh, tag))) break;
        __builtin_amdgcn_s_sleep(32);
      }
      lds_st<unsigned>(lds0 + L_HS + lane * 4, __builtin_amdgcn_perm(gh[2], gh[0], 0x05040100u));
      lds_st<unsigned>(lds0 + L_HS + 256 + lane * 4, __builtin_amdgcn_perm(gh[2], gh[0], 0x07060302u));
    }
    {
      sleep_until(th0 + TS2);
      u32x4 ta;
#pragma unroll 1
      for (unsigned spin = 0; spin < SPIN_CAP; ++spin) {
        ta = *(volatile LAS u32x4*)(size_t)(lds0 + L_TS);
        if (__all((ta[0] != 0) & (ta[1] != 0) & (ta[2] != 0) & (ta[3] != 0))) break;
        __builtin_amdgcn_s_sleep(8);
      }
      const u32x4 p0 = *(volatile LAS u32x4*)(size_t)(lds0 + L_TP), p1 = *(volatile LAS u32x4*)(size_t)(lds0 + L_TP + 16);
      {
        const unsigned a = th0 + (((ta[0] - th0) + (ta[1] - th0) + (ta[2] - th0) + (ta[3] - th0)) >> 2);
        const unsigned b = th0 + (((p0[1] - th0) + (p0[3] - th0) + (p1[1] - th0) + (p1[3] - th0)) >> 2);
        const unsigned c4 = p0[0] + p0[2] + p1[0] + p1[2];
        if (lane == 0) st_dev(rws, u32x4{a, tag, (b << 8) | c4, tag}, 0, WS_PC + wg * 128);
      }
      sleep_until(now32() + TSD);
      u32x4 gp;
#pragma unroll 1
      for (unsigned spin = 0; spin < SPIN_CAP; ++spin) {
        asm volatile("" ::: "memory");
        gp = ld_dev(rws, (lane & (NSAMP - 1)) * 128, WS_PC + (wg & ~(NSAMP - 1)) * 128);
        if (__all(pair_ok(gp, tag))) break;
        __builtin_amdgcn_s_sleep(24);
      }
      const float tb = (float)((int)((gp[2] & 0xffffff00u) - (th0 << 8)) >> 8), ta_ = (float)(int)(gp[0] - th0);
      const float cb = 0.25f * (float)(gp[2] & 0xffu);
      const float r = fmaxf(cb - (float)CPA, 0.25f) * __builtin_amdgcn_rcpf(fmaxf(tb - ta_, 25.f)) * RBOOST;
      const float inv = __builtin_amdgcn_rcpf(wave_sum(r));
      const float fin = fminf(fmaxf(wave_sum((float)(NUNIT - 4) - cb + r * tb) * inv, 0.f), 8000.f);
      if (lane == 0) *(volatile LAS u32x2*)(size_t)(lds0 + L_TL) = u32x2{(th0 + (unsigned)((int)fin - PACE_OFF)) | 1u, __float_as_uint(64.f * inv)};
    }
    {
#pragma unroll 1
      for (unsigned spin = 0; spin < SPIN_CAP * 4; ++spin) {
        const u32x4 mr = *(volatile LAS u32x4*)(size_t)(lds0 + L_MR);
        if (__all((mr[0] & mr[1] & mr[2] & mr[3]) != 0)) break;
        __builtin_amdgcn_s_sleep(8);
      }
      asm volatile("" ::: "memory");
      __builtin_amdgcn_s_setprio(0);
      const f32x4 ma = lds_ld<f32x4>(lds0 + L_ML + (lane & 1) * 32), mb = lds_ld<f32x4>(lds0 + L_ML + (lane & 1) * 32 + 16);
      const float Ma = fmaxf(fmaxf(ma[0], ma[1]), fmaxf(ma[2], ma[3])), Mb = fmaxf(fmaxf(mb[0], mb[1]), fmaxf(mb[2], mb[3]));
      float La = 0.f, Lb = 0.f;
#pragma unroll 4
      for (int i = 0; i < 16; ++i) {
        const u32x4 xp = lds_ld<u32x4>(lds0 + L_SC + i * 1024 + lane * 16);
        La += (ex2((bflo(xp[0]) - Ma) * L2E) + ex2((bfhi(xp[0]) - Ma) * L2E)) + (ex2((bflo(xp[1]) - Ma) * L2E) + ex2((bfhi(xp[1]) - Ma) * L2E));
        Lb += (ex2((bflo(xp[2]) - Mb) * L2E) + ex2((bfhi(xp[2]) - Mb) * L2E)) + (ex2((bflo(xp[3]) - Mb) * L2E) + ex2((bfhi(xp[3]) - Mb) * L2E));
      }
      La += dpp<XOR2>(La); Lb += dpp<XOR2>(Lb);
      La = quads_sum(La); Lb = quads_sum(Lb);
      __builtin_amdgcn_s_setprio(3);
      if (lane < 4) st_dev(rws, u32x4{__float_as_uint(lane & 2 ? Mb : Ma), tag, __float_as_uint(lane & 2 ? Lb : La), tag}, ((lane & 1) * 2 + (lane >> 1)) * 16, WS_ML + wg * 128);
      sleep_until(now32() + ML_DELAY);
      u32x4 gm;
#pragma unroll 1
      for (unsigned spin = 0; spin < SPIN_CAP; ++spin) {
        asm volatile("" ::: "memory");
        gm = ld_dev(rws, (lane >> 2) * 128 + (lane & 3) * 16, WS_ML + (wg & ~15) * 128);
        if (__all(pair_ok(gm, tag))) break;
        __builtin_amdgcn_s_sleep(32);
      }
      const float ms = __uint_as_float(gm[0]), Mg = quads_max(ms);
      const float inv = __builtin_amdgcn_rcpf(quads_sum(ex2((ms - Mg) * L2E) * __uint_as_float(gm[2])));
      if (lane < 4) lds_st<f32x2>(lds0 + L_GS + lane * 8, f32x2{Mg, inv});
      wait_lds();
      if (lane == 0) *(volatile LAS unsigned*)(size_t)(lds0 + L_GR) = 1u;
    }
    wg_barrier();
    if (f2) {
      lds_wait_nonzero(lds0 + L_WR); lds_wait_nonzero(lds0 + L_WR + 4);
      const f32x2 y = proj64(lds0 + L_HS, 256, lds0 + L_WF, lane);
      qo.store(rws, y[0], y[1], WS_FG + (gk * DM + gc * 64) * 8);
    }
    float xr = bflo(xo);
    unsigned vst = (lane & 15) == 0 ? vout : OOB;
    asm volatile("" : "+v"(xr), "+v"(vst) : "s"(rout));
    wg_barrier();
    float gate;
    {
      u32x4 gf;
#pragma unroll 1
      for (unsigned spin = 0; spin < SPIN_CAP; ++spin) {
        asm volatile("" ::: "memory");
        gf = ld_dev(rws, (lane < 14 ? lane : 0) * (DM * 8), WS_FG + cg * 16);
        if (__all(quad_ok(gf, tag))) break;
      }
      if (lane >= 14) gf = u32x4{0u, 0u, 0u, 0u};
      gate = rbf(silu(rbf(quad_sum(quad_val(gf[0]), quad_val(gf[1]), quad_val(gf[2]), quad_val(gf[3])))));
      asm volatile("" : "+v"(gate));
    }
    {
      u32x4 gs = {};
      bool have = false;
#pragma unroll 1
      for (unsigned spin = 0; spin < SPIN_CAP; ++spin) {
        asm volatile("" ::: "memory");
        if (!have) gs = ld_dev(rws, lane * (DM * 8), WS_P2 + cg * 16);
        have = quad_ok(gs, tag);
        if (__all(have)) break;
      }
      const float s = rbf(quad_sum(quad_val(gs[0]), quad_val(gs[1]), quad_val(gs[2]), quad_val(gs[3])));
      __builtin_amdgcn_raw_buffer_store_b16(__builtin_bit_cast(u16, (__bf16)(xr + rbf(s * gate))), rout, vst, 0, 0);
      if (wg == 0 && lane == 0) __builtin_amdgcn_raw_buffer_store_b32(epoch, rws, 0, WS_EPOCH, SC1);
    }
  }
}

}

extern "C" void kernel_launch(void* const* d_in, const int* in_sizes, int n_in, void* d_out, int out_size, void* d_ws, size_t ws_size,
                              hipStream_t stream) {
  (void)hipFuncSetAttribute((const void*)fused_decode_kernel, hipFuncAttributeMaxDynamicSharedMemorySize, L_END);
  fused_decode_kernel<<<NWG, NTHR, L_END, stream>>>((const u16*)d_in[0], (const u16*)d_in[1], (const u16*)d_in[2], (const u16*)d_in[3],
                                                    (const u16*)d_in[4], (const u16*)d_in[6], (const u16*)d_in[7], (u16*)d_out, (char*)d_ws);
}
```

```cpp
#include <hip/hip_runtime.h>
#include <hip/hip_bf16.h>
#include <type_traits>

namespace {

typedef unsigned short u16;
typedef short s16x4 __attribute__((ext_vector_type(4)));
typedef __bf16 bf16x8 __attribute__((ext_vector_type(8)));
typedef __bf16 bf16x2 __attribute__((ext_vector_type(2)));
typedef float f32x2 __attribute__((ext_vector_type(2)));
typedef float f32x4 __attribute__((ext_vector_type(4)));
typedef unsigned u32x2 __attribute__((ext_vector_type(2)));
typedef unsigned u32x4 __attribute__((ext_vector_type(4)));
typedef __amdgpu_buffer_rsrc_t rsrc_t;
#define LAS __attribute__((address_space(3)))

constexpr int NWG = 256, NTHR = 320;
constexpr int DM = 512, HQD = 4096, SK = 32768, DH = 128, FF = 1792;
constexpr int NSLOT = 6, AHEAD = NSLOT - 1, NTILE = 32, NUNIT = 2 * NTILE, NPRE = 11;
constexpr int WIN = 4, WIN_FAR = 8;
constexpr int CODE_KB = 29;
constexpr int CPA = 13, TS2 = 1430, TSD = 100, PACE_OFF = 90, OVER = 4, NSAMP = 32, POLL_AT = 210, ML_DELAY = 100;
constexpr float RBOOST = 0.99f, BEHIND = 55.f, FAR_BEHIND = 120.f;
constexpr int KV_BYTES = 2 * 8 * SK * DH * 2;
constexpr float SCALE = 0.08838834764831845f, L2E = 1.4426950408889634f;
constexpr float M_INIT = -3.0e38f;
constexpr unsigned SPIN_CAP = 1u << 18;
constexpr unsigned OOB = 0x80000000u;

constexpr int WS_EPOCH = 0;
constexpr int WS_Q = 4096;
constexpr int WS_H = WS_Q + HQD * 8;
constexpr int WS_ML = 65536;
constexpr int WS_PC = WS_ML + NWG * 128;
constexpr int WS_QP = WS_PC + NWG * 128;
constexpr int WS_HP = WS_QP + 4 * HQD * 16;
constexpr int WS_FG = WS_HP + 4 * FF * 8;
constexpr int WS_P2 = WS_FG + 14 * DM * 8;
constexpr int WS_P1 = WS_P2 + 64 * DM * 8;
constexpr int WS_END = WS_P1 + NWG * 2048;
static_assert(WS_H + FF * 8 <= WS_ML, "workspace map");

constexpr int L_RING = 0;
constexpr int L_WQ = 4 * NSLOT * 4096;
constexpr int L_SC = L_WQ;
constexpr int L_WF = L_WQ + 16384;
constexpr int L_WO = L_WF + 16384;
constexpr int L_XN = L_WO + 16384;
constexpr int L_QI = L_XN + 2048;
constexpr int L_OS = L_QI + 1024;
constexpr int L_HS = L_OS + 256;
constexpr int L_ML = L_HS + 512;
constexpr int L_TAG = L_ML + 128;
constexpr int L_QR = L_TAG + 4;
constexpr int L_TL = L_TAG + 16;
constexpr int L_TS = L_TL + 16;
constexpr int L_TP = L_TS + 16;
constexpr int L_MR = L_TP + 32;
constexpr int L_GR = L_MR + 16;
constexpr int L_WR = L_GR + 8;
constexpr int L_GS = L_GR + 16;
constexpr int L_END = L_GS + 32;
static_assert(L_GS - L_TAG == 28 * 4 && L_END <= 160 * 1024, "LDS map");

__device__ __forceinline__ float rbf(float f) { return (float)(__bf16)f; }
__device__ __forceinline__ unsigned pk2(float lo, float hi) {
  bf16x2 v = {(__bf16)lo, (__bf16)hi};
  return __builtin_bit_cast(unsigned, v);
}
__device__ __forceinline__ float bflo(unsigned u) { return __uint_as_float(u << 16); }
__device__ __forceinline__ float bfhi(unsigned u) { return __uint_as_float(u & 0xffff0000u); }
template <int CTRL> __device__ __forceinline__ float dpp(float x) {
  return __builtin_bit_cast(float, __builtin_amdgcn_mov_dpp(__builtin_bit_cast(int, x), CTRL, 0xf, 0xf, true));
}
constexpr int XOR1 = 0xB1, XOR2 = 0x4E, ROR4 = 0x124, ROR8 = 0x128;
__device__ __forceinline__ float swap16_sum(float a, float b) {
  auto s = __builtin_amdgcn_permlane16_swap(__float_as_uint(a), __float_as_uint(b), false, false);
  return __uint_as_float(s[0]) + __uint_as_float(s[1]);
}
__device__ __forceinline__ float swap32_sum(float a, float b) {
  auto s = __builtin_amdgcn_permlane32_swap(__float_as_uint(a), __float_as_uint(b), false, false);
  return __uint_as_float(s[0]) + __uint_as_float(s[1]);
}
__device__ __forceinline__ float rows_sum(float x) { x = swap16_sum(x, x); return swap32_sum(x, x); }
__device__ __forceinline__ float rows_max(float x) {
  auto s = __builtin_amdgcn_permlane16_swap(__float_as_uint(x), __float_as_uint(x), false, false);
  x = fmaxf(__uint_as_float(s[0]), __uint_as_float(s[1]));
  auto t = __builtin_amdgcn_permlane32_swap(__float_as_uint(x), __float_as_uint(x), false, false);
  return fmaxf(__uint_as_float(t[0]), __uint_as_float(t[1]));
}
__device__ __forceinline__ float quads_sum(float x) { x += dpp<ROR4>(x); x += dpp<ROR8>(x); return rows_sum(x); }
__device__ __forceinline__ float quads_max(float x) { x = fmaxf(x, dpp<ROR4>(x)); x = fmaxf(x, dpp<ROR8>(x)); return rows_max(x); }
__device__ __forceinline__ float wave_sum(float x) { x += dpp<XOR1>(x); x += dpp<XOR2>(x); return quads_sum(x); }
__device__ __forceinline__ float ex2(float x) { return __builtin_amdgcn_exp2f(x); }
__device__ __forceinline__ float silu(float v) { return v * __builtin_amdgcn_rcpf(1.f + ex2(-v * L2E)); }

template <int N> __device__ __forceinline__ void wait_vm() {
  __builtin_amdgcn_sched_barrier(0);
  asm volatile("s_waitcnt vmcnt(%0)" ::"i"(N) : "memory");
}
__device__ __forceinline__ void wait_lds() { asm volatile("s_waitcnt lgkmcnt(0)" ::: "memory"); }
__device__ __forceinline__ void wg_barrier() {
  asm volatile("s_waitcnt lgkmcnt(0)\n\ts_barrier" ::: "memory");
}
__device__ __forceinline__ void lds_wait_nonzero(unsigned addr) {
#pragma unroll 1
  for (unsigned spin = 0; spin < SPIN_CAP * 16 && *(volatile LAS unsigned*)(size_t)addr == 0; ++spin) __builtin_amdgcn_s_sleep(1);
  asm volatile("" ::: "memory");
}
__device__ __forceinline__ unsigned now32() { return (unsigned)__builtin_amdgcn_s_memrealtime(); }
__device__ __forceinline__ void sleep_until(unsigned due) { while ((int)(now32() - due) < 0) __builtin_amdgcn_s_sleep(2); }
__device__ __forceinline__ rsrc_t make_rsrc(const void* p, int bytes) {
  return __builtin_amdgcn_make_buffer_rsrc((void*)p, (short)0, bytes, 0x00020000);
}
constexpr int SC1 = 16;
__device__ __forceinline__ u32x4 ld_dev(rsrc_t r, unsigned voff, unsigned soff) { return __builtin_amdgcn_raw_buffer_load_b128(r, voff, soff, SC1); }
__device__ __forceinline__ void st_dev(rsrc_t r, u32x4 v, unsigned voff, unsigned soff) { __builtin_amdgcn_raw_buffer_store_b128(v, r, voff, soff, SC1); }
__device__ __forceinline__ bool pair_ok(const u32x4& g, unsigned tag) { return (g[1] == tag) & (g[3] == tag); }
__device__ __forceinline__ bool quad_ok(const u32x4& g, unsigned tag) {
  return (__builtin_amdgcn_perm(g[1], g[0], 0x0c0c0400u) | __builtin_amdgcn_perm(g[3], g[2], 0x04000c0cu)) == tag;
}
__device__ __forceinline__ float quad_val(unsigned w) { return __uint_as_float(w & 0xffffff00u); }
struct QuadOut {
  unsigned t0, t1, t2, t3, voff;
  __device__ __forceinline__ QuadOut(unsigned tag, int lane) : t0(tag & 255u), t1((tag >> 8) & 255u), t2((tag >> 16) & 255u), t3(tag >> 24), voff(lane & 1 ? OOB : lane * 8) {
    asm volatile("" : "+v"(t0), "+v"(t1), "+v"(t2), "+v"(t3), "+v"(voff));
  }
  __device__ __forceinline__ void store(rsrc_t r, float fa, float fb, unsigned soff) const {
    const unsigned a = __float_as_uint(fa) + 0x80u, b = __float_as_uint(fb) + 0x80u;
    const unsigned c = __float_as_uint(dpp<XOR1>(__uint_as_float(a))), d = __float_as_uint(dpp<XOR1>(__uint_as_float(b)));
    st_dev(r, u32x4{(a & 0xffffff00u) | t0, (b & 0xffffff00u) | t1, (c & 0xffffff00u) | t2, (d & 0xffffff00u) | t3}, voff, soff);
  }
};
__device__ __forceinline__ float quad_sum(float v0, float v1, float v2, float v3) {
  float x = swap16_sum(swap32_sum(v0, v2), swap32_sum(v1, v3));
  x += dpp<ROR8>(x); x += dpp<ROR4>(x); x += dpp<XOR2>(x); x += dpp<XOR1>(x);
  return x;
}
__device__ __forceinline__ s16x4 tr_read(unsigned addr) { return __builtin_amdgcn_ds_read_tr16_b64_v4i16((LAS s16x4*)(size_t)addr); }
template <typename T> __device__ __forceinline__ T lds_ld(unsigned addr) { return *(const LAS T*)(size_t)addr; }
template <typename T> __device__ __forceinline__ void lds_st(unsigned addr, T v) { *(LAS T*)(size_t)addr = v; }

__device__ __forceinline__ void dma_piece(rsrc_t r, unsigned voff, unsigned soff, unsigned dst) {
  asm volatile("s_mov_b32 m0, %0\n\ts_nop 0\n\tbuffer_load_dwordx4 %1, %2, %3 offen nt lds" ::"s"(dst), "v"(voff), "s"(r), "s"(soff) : "memory");
}
__device__ __forceinline__ void dma_half(rsrc_t r, unsigned v0, unsigned v1, unsigned v2, unsigned v3, unsigned soff, unsigned dst) {
  asm volatile(
      "s_mov_b32 m0, %0\n\ts_nop 0\n\t"
      "buffer_load_dwordx4 %1, %5, %6 offen nt lds\n\t"
      "buffer_load_dwordx4 %2, %5, %6 offen offset:1024 nt lds\n\t"
      "buffer_load_dwordx4 %3, %5, %6 offen offset:2048 nt lds\n\t"
      "buffer_load_dwordx4 %4, %5, %6 offen offset:3072 nt lds"
      ::"s"(dst), "v"(v0), "v"(v1), "v"(v2), "v"(v3), "s"(r), "s"(soff)
      : "memory");
}
template <int A> __device__ __forceinline__ f32x4 mfma4(s16x4 a, s16x4 b, f32x4 c) { return __builtin_amdgcn_mfma_f32_4x4x4bf16_1k(a, b, c, 4, A, 0); }
template <int A, int N> struct Proj {
  static __device__ __forceinline__ void run(const s16x4 (&av)[2], unsigned ba, f32x4 (&c)[4]) {
    c[A & 3] = mfma4<A & 15>(av[A >> 4], tr_read(ba + A * 512), c[A & 3]);
    if constexpr (A + 1 < N) Proj<A + 1, N>::run(av, ba, c);
  }
};
template <int A> struct ProjReg {
  static __device__ __forceinline__ void run(s16x4 a, const s16x4 (&w)[16], f32x4 (&c)[2]) {
    c[A & 1] = mfma4<A>(a, w[A], c[A & 1]);
    if constexpr (A + 1 < 16) ProjReg<A + 1>::run(a, w, c);
  }
};
__device__ __forceinline__ f32x2 proj64(unsigned a_img, int a_stride, unsigned w_img, int lane) {
  const int q = (lane & 15) >> 2, p = lane & 3;
  const unsigned aa = a_img + (lane & 1) * a_stride + (lane >> 2) * 8;
  const s16x4 av[2] = {lds_ld<s16x4>(aa), lds_ld<s16x4>(aa + 128)};
  const unsigned ba = w_img + q * 128 + (((2 * (lane >> 4) + (p >> 1)) ^ ((q >> 1) << 2)) << 4) + (p & 1) * 8;
  f32x4 c[4] = {};
  Proj<0, 32>::run(av, ba, c);
  return f32x2{(c[0][0] + c[1][0]) + (c[2][0] + c[3][0]), (c[0][1] + c[1][1]) + (c[2][1] + c[3][1])};
}
__device__ __forceinline__ f32x4 gather_quarters(rsrc_t rws, unsigned base, int quarter_bytes, unsigned tag, int lane) {
  u32x4 g;
#pragma unroll 1
  for (unsigned spin = 0; spin < SPIN_CAP; ++spin) {
    asm volatile("" ::: "memory");
    g = ld_dev(rws, (lane >> 4) * quarter_bytes + (lane & 7) * 16, base);
    if (__all(quad_ok(g, tag))) break;
    __builtin_amdgcn_s_sleep(8);
  }
  return f32x4{rows_sum(quad_val(g[0])), rows_sum(quad_val(g[1])), rows_sum(quad_val(g[2])), rows_sum(quad_val(g[3]))};
}

template <int N> using Int = std::integral_constant<int, N>;
struct KFrag { bf16x8 k[4]; };
struct VFrag { s16x4 v[4][2]; };

__device__ __forceinline__ void read_k(KFrag& t, const unsigned (&ka)[4], unsigned off) {
#pragma unroll
  for (int s = 0; s < 4; ++s) t.k[s] = lds_ld<bf16x8>(ka[s] + off);
  asm volatile("s_waitcnt lgkmcnt(0)" : "+v"(t.k[0]), "+v"(t.k[1]), "+v"(t.k[2]), "+v"(t.k[3])::"memory");
}
__device__ __forceinline__ void read_v(VFrag& t, const unsigned (&va)[2], unsigned off) {
#pragma unroll
  for (int a = 0; a < 4; ++a)
#pragma unroll
    for (int h = 0; h < 2; ++h) t.v[a][h] = tr_read(va[h] + off + a * 1024);
  asm volatile("s_waitcnt lgkmcnt(0)"
               : "+v"(t.v[0][0]), "+v"(t.v[0][1]), "+v"(t.v[1][0]), "+v"(t.v[1][1]), "+v"(t.v[2][0]), "+v"(t.v[2][1]), "+v"(t.v[3][0]), "+v"(t.v[3][1])
               :
               : "memory");
}
__device__ __forceinline__ void score_tile(const KFrag& t, const bf16x8 (&qf)[4], float& m, unsigned dst, bool writer) {
  f32x4 sc = {0.f, 0.f, 0.f, 0.f};
#pragma unroll
  for (int s = 0; s < 4; ++s) sc = __builtin_amdgcn_mfma_f32_16x16x32_bf16(t.k[s], qf[s], sc, 0, 0, 0);
  m = fmaxf(fmaxf(m, fmaxf(sc[0], sc[1])), fmaxf(sc[2], sc[3]));
  const unsigned r0 = pk2(sc[0], sc[1]), r1 = pk2(sc[2], sc[3]);
  if (writer) lds_st<u32x2>(dst, u32x2{pk2(bflo(r0) * SCALE, bfhi(r0) * SCALE), pk2(bflo(r1) * SCALE, bfhi(r1) * SCALE)});
}
__device__ __forceinline__ s16x4 probs(unsigned src, float M, float f) {
  const u32x2 xp = lds_ld<u32x2>(src);
  const u32x2 pp = {pk2(f * ex2((bflo(xp[0]) - M) * L2E), f * ex2((bfhi(xp[0]) - M) * L2E)), pk2(f * ex2((bflo(xp[1]) - M) * L2E), f * ex2((bfhi(xp[1]) - M) * L2E))};
  return __builtin_bit_cast(s16x4, pp);
}
__device__ __forceinline__ void value_tile(const VFrag& t, s16x4 pa, f32x4 (&o)[2]) {
#pragma unroll
  for (int h = 0; h < 2; ++h) o[h] = mfma4<0>(pa, t.v[0][h], o[h]);
#pragma unroll
  for (int h = 0; h < 2; ++h) o[h] = mfma4<4>(pa, t.v[1][h], o[h]);
#pragma unroll
  for (int h = 0; h < 2; ++h) o[h] = mfma4<8>(pa, t.v[2][h], o[h]);
#pragma unroll
  for (int h = 0; h < 2; ++h) o[h] = mfma4<12>(pa, t.v[3][h], o[h]);
}

__global__ void __launch_bounds__(NTHR, 2)
fused_decode_kernel(const u16* __restrict__ x, const u16* __restrict__ kc, const u16* __restrict__ vc, const u16* __restrict__ wq,
                    const u16* __restrict__ wo, const u16* __restrict__ w1, const u16* __restrict__ w2, u16* __restrict__ out, char* ws) {
  unsigned long long pc;
  asm volatile("s_getpc_b64 %0" : "=s"(pc));
  extern __shared__ __attribute__((aligned(16))) unsigned char lds[];
  const unsigned lds0 = (unsigned)(size_t)(LAS unsigned char*)lds;
  const int lane = threadIdx.x & 63, wave = __builtin_amdgcn_readfirstlane(threadIdx.x >> 6), wg = blockIdx.x;
  const int xg = wg & 7, xi = wg >> 3;
  asm volatile("" ::"s"(x), "s"(kc), "s"(vc), "s"(wq), "s"(wo), "s"(w1), "s"(w2), "s"(out), "s"(ws));
  const rsrc_t rws = make_rsrc(ws, WS_END);
  const unsigned sw128 = ((lane & 7) ^ (((lane >> 4) & 1) << 2)) << 4;

  if (wave < 4) {
    const rsrc_t rk = make_rsrc(kc, KV_BYTES), rv = make_rsrc(vc, KV_BYTES);
    const int g = lane >> 4, c = lane & 15;
    unsigned vk[4];
#pragma unroll
    for (int u = 0; u < 4; ++u) vk[u] = g * 256 + ((c ^ (4 * u + g)) << 4);
    const unsigned vv = g * 256 + ((c ^ (g << 2)) << 4);
    const unsigned ring = lds0 + L_RING + wave * (NSLOT * 4096);
    const unsigned tile0 = (unsigned)wg * (2048 * 256) + wave * 4096;
    unsigned ka[4], va[2];
#pragma unroll
    for (int s = 0; s < 4; ++s) ka[s] = ring + c * 256 + (((4 * s + g) ^ c) << 4);
#pragma unroll
    for (int h = 0; h < 2; ++h) va[h] = ring + (c >> 2) * 256 + (((8 * h + 2 * g + ((c & 3) >> 1)) ^ ((c >> 2) << 2)) << 4) + (c & 1) * 8;
    const unsigned sca = lds0 + L_SC + wave * 4096 + (g * 4 + (c & 3)) * 8;
    auto issue_k = [&](int t, unsigned off) { dma_half(rk, vk[0], vk[1], vk[2], vk[3], tile0 + t * 16384, ring + off); };
    auto issue_v = [&](int t, unsigned off) { dma_half(rv, vv, vv, vv, vv, tile0 + t * 16384, ring + off); };

    wg_barrier();
#pragma unroll
    for (int u = 0; u < AHEAD; ++u) { wait_vm<WIN>(); issue_k(u, u * 4096); }
    KFrag pk[NPRE];
#pragma unroll
    for (int u = 0; u < NPRE; ++u) {
      wait_vm<WIN>();
      issue_k(u + AHEAD, ((u + AHEAD) % NSLOT) * 4096);
      read_k(pk[u], ka, (u % NSLOT) * 4096);
    }
    lds_wait_nonzero(lds0 + L_QR);
    bf16x8 qf[4];
#pragma unroll
    for (int s = 0; s < 4; ++s) qf[s] = lds_ld<bf16x8>(lds0 + L_QI + (c & 3) * 256 + (4 * s + g) * 16);
    const unsigned tag = lds_ld<unsigned>(lds0 + L_TAG);

    unsigned prev = now32();
    bool far = false;
    auto landed = [&](int u) {
      const u32x2 plan = *(volatile LAS u32x2*)(size_t)(lds0 + L_TL);
      if (far) wait_vm<WIN_FAR>(); else wait_vm<WIN>();
      unsigned tn = now32();
      *(volatile LAS u32x2*)(size_t)(lds0 + L_TP + wave * 8) = u32x2{(unsigned)u, tn};
      if (u == CPA) *(volatile LAS unsigned*)(size_t)(lds0 + L_TS + wave * 4) = tn | 1u;
      const unsigned tl = __builtin_amdgcn_readfirstlane(plan[0]);
      const int d = (int)(tl - prev);
      if (tl != 0 && d > 0) {
        const float n = (float)(NUNIT - AHEAD - u), lag = n * __uint_as_float(plan[1]) - (float)d;
        far = __builtin_amdgcn_readfirstlane(lag > FAR_BEHIND);
        const unsigned due = prev + (unsigned)__builtin_amdgcn_readfirstlane(lag > BEHIND ? 0 : (int)((float)d * __builtin_amdgcn_rcpf(n)));
        while ((int)(tn - due) < 0) { __builtin_amdgcn_s_sleep(1); tn = now32(); }
      }
      prev = tn + OVER;
    };
    static_assert(WIN <= 16 && WIN_FAR <= 16 && 2 * NPRE <= NTILE - AHEAD && CPA >= NPRE, "unit schedule");
    float m = M_INIT;
    KFrag kf;
#pragma unroll
    for (int u = NPRE; u < 2 * NPRE; ++u) {
      landed(u); issue_k(u + AHEAD, ((u + AHEAD) % NSLOT) * 4096);
      read_k(kf, ka, (u % NSLOT) * 4096);
      score_tile(kf, qf, m, sca + u * 128, c < 4);
      score_tile(pk[u - NPRE], qf, m, sca + (u - NPRE) * 128, c < 4);
    }
    unsigned off = ((2 * NPRE) % NSLOT) * 4096, offp = ((2 * NPRE + AHEAD) % NSLOT) * 4096;
    auto next_slot = [&]() { offp = off; off = off == (NSLOT - 1) * 4096 ? 0u : off + 4096; };
#pragma unroll 1
    for (int u = 2 * NPRE; u < NTILE - AHEAD; ++u) {
      landed(u); issue_k(u + AHEAD, offp);
      read_k(kf, ka, off);
      score_tile(kf, qf, m, sca + u * 128, c < 4);
      next_slot();
    }
#pragma unroll 1
    for (int u = NTILE - AHEAD; u < NTILE; ++u) {
      landed(u); issue_v(u + AHEAD - NTILE, offp);
      read_k(kf, ka, off);
      score_tile(kf, qf, m, sca + u * 128, c < 4);
      next_slot();
    }
    const float M = rbf(rbf(rows_max(m)) * SCALE);
    if (lane < 4) lds_st<float>(lds0 + L_ML + lane * 16 + wave * 4, M);
    wait_lds();
    if (lane == 0) *(volatile LAS unsigned*)(size_t)(lds0 + L_MR + wave * 4) = 1u;
    f32x4 o[2] = {};
    VFrag vf;
#pragma unroll 1
    for (int u = NTILE; u < NUNIT - AHEAD; ++u) {
      landed(u); issue_v(u + AHEAD - NTILE, offp);
      const s16x4 pa = probs(sca + (u - NTILE) * 128, M, 1.f);
      read_v(vf, va, off);
      value_tile(vf, pa, o);
      next_slot();
    }
    const int ks = xg * 8 + (xi >> 2), cs = xi & 3;
    if (wave < 2) {
      const rsrc_t ro = make_rsrc(wo, HQD * DM * 2);
#pragma unroll
      for (int i = 0; i < 8; ++i)
        dma_piece(ro, (lane >> 3) * (DM * 2) + sw128, (ks * 64 + 8 * i) * (DM * 2) + cs * 256 + wave * 128, lds0 + L_WO + wave * 8192 + i * 1024);
    }
    const bool late_w2 = wave >= 2 && xi >= 14 && xi < 28;
    if (late_w2) {
      const int fi = xg * 14 + xi - 14;
      const rsrc_t r2 = make_rsrc(w2, DM * FF * 2);
#pragma unroll
      for (int i = 0; i < 8; ++i)
        dma_piece(r2, (lane >> 3) * (DM * 2) + sw128, ((fi >> 3) * 128 + (wave & 1) * 64 + 8 * i) * (DM * 2) + (fi & 7) * 128, lds0 + L_WF + (wave & 1) * 8192 + i * 1024);
    }
    lds_wait_nonzero(lds0 + L_GR);
    const f32x2 gs = lds_ld<f32x2>(lds0 + L_GS + (c & 3) * 8);
    const float f = ex2((M - gs[0]) * L2E) * gs[1];
    {
      const float f0 = dpp<0x00>(f), f1 = dpp<0x55>(f), f2 = dpp<0xAA>(f), f3 = dpp<0xFF>(f);
#pragma unroll
      for (int h = 0; h < 2; ++h) { o[h][0] *= f0; o[h][1] *= f1; o[h][2] *= f2; o[h][3] *= f3; }
    }
    const QuadOut qo(tag, lane);
    unsigned s_p1 = WS_P1 + wg * 2048 + wave * 512, s_p2 = WS_P2 + ks * (DM * 8) + (cs * 128 + wave * 64) * 8;
    unsigned a_mw = ring + lane * 4, a_mr = lds0 + L_RING + (wave * 128 + 2 * lane) * 4;
    unsigned a_ow = lds0 + L_OS + (wave >> 1) * 128 + ((wave & 1) * 32 + 4 * (lane & 7) + g) * 2, a_or = lds0 + L_OS + (lane & 1) * 128 + (lane >> 2) * 8;
    asm volatile("" : "+s"(s_p1), "+s"(s_p2), "+v"(a_mw), "+v"(a_mr), "+v"(a_ow), "+v"(a_or));
    auto last = [&](auto j) {
      constexpr int u = NUNIT - AHEAD + j.value, younger = (NUNIT - 1 - u) * 4;
      s16x4 pa = probs(sca + (u - NTILE) * 128, M, f);
      asm volatile("" : "+v"(pa));
      if (wave < 2 || late_w2) wait_vm<younger + 8>(); else wait_vm<younger>();
      read_v(vf, va, (u % NSLOT) * 4096);
      value_tile(vf, pa, o);
    };
    last(Int<0>{}); last(Int<1>{}); last(Int<2>{}); last(Int<3>{}); last(Int<4>{});

#pragma unroll
    for (int h = 0; h < 2; ++h)
#pragma unroll
      for (int r = 0; r < 4; ++r) lds_st<float>(a_mw + (r * 128 + h * 64) * 4, o[h][r]);
    wg_barrier();
    {
      f32x2 ov[4];
#pragma unroll
      for (int w = 0; w < 4; ++w) ov[w] = lds_ld<f32x2>(a_mr + w * (NSLOT * 4096));
      const f32x2 sum = (ov[0] + ov[1]) + (ov[2] + ov[3]);
      qo.store(rws, sum[0], sum[1], s_p1);
    }
    s16x4 wf[16];
    if (wave < 2) {
      const unsigned wa = lds0 + L_WO + wave * 8192 + (c >> 2) * 128 + (((2 * g + ((c & 3) >> 1)) ^ ((c >> 3) << 2)) << 4) + (c & 1) * 8;
      wait_vm<1>();
#pragma unroll
      for (int a = 0; a < 16; ++a) wf[a] = tr_read(wa + a * 512);
    }
    if (late_w2) {
      wait_vm<1>();
      if (lane == 0) *(volatile LAS unsigned*)(size_t)(lds0 + L_WR + (wave & 1) * 4) = 1u;
    }
    {
      const int b = wave >> 1, half = wave & 1;
      const unsigned vo = (lane >> 3) * 2048 + (lane & 7) * 16;
      const unsigned so = WS_P1 + (b * 8 + xg) * 16 * 2048 + ((ks >> 1) & 3) * 512 + ((ks & 1) * 2 + half) * 128;
      sleep_until(__builtin_amdgcn_readfirstlane(*(volatile LAS unsigned*)(size_t)(lds0 + L_TL)) + POLL_AT);
      u32x4 gv[2] = {};
      bool h0 = false, h1 = false;
#pragma unroll 1
      for (unsigned spin = 0; spin < SPIN_CAP; ++spin) {
        asm volatile("" ::: "memory");
        if (!h0) gv[0] = ld_dev(rws, vo, so);
        if (!h1) gv[1] = ld_dev(rws, vo + 8 * 2048, so);
        h0 = quad_ok(gv[0], tag); h1 = quad_ok(gv[1], tag);
        if (__all(h0 & h1)) break;
      }
      float u[4];
#pragma unroll
      for (int j = 0; j < 4; ++j) u[j] = quad_val(gv[0][j]) + quad_val(gv[1][j]);
      float e = swap16_sum(swap32_sum(u[0], u[2]), swap32_sum(u[1], u[3]));
      e += dpp<ROR8>(e);
      lds_st<__bf16>(a_ow, (__bf16)e);
    }
    wg_barrier();
    if (wave < 2) {
      const s16x4 oa = lds_ld<s16x4>(a_or);
      f32x4 acc[2] = {};
      ProjReg<0>::run(oa, wf, acc);
      qo.store(rws, acc[0][0] + acc[1][0], acc[0][1] + acc[1][1], s_p2);
    }
  } else {
    __builtin_amdgcn_s_setprio(3);
    const int cg = xg * 32 + xi;
    const int qk = cg >> 6, qc = cg & 63;
    const bool f1 = xi < 14, f2 = xi >= 14 && xi < 28;
    const int fi = xg * 14 + (xi < 14 ? xi : xi - 14);
    const int hk = fi / 28, hc = fi % 28;
    const int gk = fi >> 3, gc = fi & 7;
    const unsigned vout = (((lane >> 4) & 1) * DM + cg * 2 + (lane >> 5)) * 2;
    const rsrc_t rx = make_rsrc(x, 2 * DM * 2);
    const rsrc_t rout = make_rsrc(out, 2 * DM * 2);
    unsigned ev, xo;
    u32x4 x0, x1, code;
    asm volatile(
        "s_nop 4\n\t"
        "buffer_load_dwordx4 %0, %5, %6, 0 offen\n\t"
        "buffer_load_dwordx4 %1, %5, %6, 0 offen offset:1024\n\t"
        "buffer_load_dword %2, off, %7, 0 sc1\n\t"
        "buffer_load_ushort %3, %8, %6, 0 offen\n\t"
        "buffer_load_dwordx4 %4, %5, %9, %10 offen"
        : "=&v"(x0), "=&v"(x1), "=&v"(ev), "=&v"(xo), "=&v"(code)
        : "v"(lane * 16), "s"(rx), "s"(rws), "v"(vout), "s"(make_rsrc((const void*)(pc & ~255ull), CODE_KB * 1024)), "s"(xi * 1024)
        : "memory");
    {
      const rsrc_t rq = make_rsrc(wq, DM * HQD * 2);
#pragma unroll
      for (int i = 0; i < 16; ++i) dma_piece(rq, (lane >> 3) * (HQD * 2) + sw128, (qk * 128 + 8 * i) * (HQD * 2) + qc * 128, lds0 + L_WQ + i * 1024);
      __builtin_amdgcn_sched_barrier(0);
      if (f1) {
        const rsrc_t r1 = make_rsrc(w1, DM * FF * 2);
#pragma unroll
        for (int i = 0; i < 16; ++i) dma_piece(r1, (lane >> 3) * (FF * 2) + sw128, (hk * 128 + 8 * i) * (FF * 2) + hc * 128, lds0 + L_WF + i * 1024);
      }
    }
    if (lane < 28) lds_st<unsigned>(lds0 + L_TAG + lane * 4, 0u);
    wg_barrier();
    const unsigned th0 = now32();
    if (f1) wait_vm<32>(); else wait_vm<16>();
    asm volatile("" : "+v"(x0), "+v"(x1), "+v"(ev), "+v"(xo), "+v"(code)::"memory");
    const unsigned epoch = max(__builtin_amdgcn_readfirstlane(ev) + 1u, 1u), tag = epoch * 0x9E3779B1u;
    {
      float a[2][8];
#pragma unroll
      for (int j = 0; j < 4; ++j) { a[0][2 * j] = bflo(x0[j]); a[0][2 * j + 1] = bfhi(x0[j]); a[1][2 * j] = bflo(x1[j]); a[1][2 * j + 1] = bfhi(x1[j]); }
#pragma unroll
      for (int b = 0; b < 2; ++b) {
        const float mean = wave_sum(((a[b][0] + a[b][1]) + (a[b][2] + a[b][3])) + ((a[b][4] + a[b][5]) + (a[b][6] + a[b][7]))) * (1.f / DM);
        float q = 0.f;
#pragma unroll
        for (int j = 0; j < 8; ++j) q = fmaf(a[b][j] - mean, a[b][j] - mean, q);
        const float var = wave_sum(q) * (1.f / DM);
        const float mb = rbf(mean), inv = __builtin_amdgcn_rcpf(rbf(__builtin_amdgcn_sqrtf(rbf(rbf(var) + 1e-5f))));
        u32x4 xn;
#pragma unroll
        for (int j = 0; j < 4; ++j) xn[j] = pk2(rbf(a[b][2 * j] - mb) * inv, rbf(a[b][2 * j + 1] - mb) * inv);
        lds_st<u32x4>(lds0 + L_XN + b * 1024 + lane * 16, xn);
      }
    }
    if (lane == 0) lds_st<unsigned>(lds0 + L_TAG, tag);
    const QuadOut qo(tag, lane);
    if (f1) wait_vm<16>(); else wait_vm<0>();
    wait_lds();
    {
      const f32x2 y = proj64(lds0 + L_XN + qk * 256, 1024, lds0 + L_WQ, lane);
      st_dev(rws, u32x4{__float_as_uint(y[0]), tag, __float_as_uint(y[1]), tag}, lane * 16, WS_QP + (qk * HQD + qc * 64) * 16);
    }
    if (f1) {
      wait_vm<1>();
      const f32x2 y = proj64(lds0 + L_XN + hk * 256, 1024, lds0 + L_WF, lane);
      qo.store(rws, y[0], y[1], WS_HP + (hk * FF + hc * 64) * 8);
    }
    {
      u32x4 gp;
#pragma unroll 1
      for (unsigned spin = 0; spin < SPIN_CAP; ++spin) {
        asm volatile("" ::: "memory");
        gp = ld_dev(rws, (lane >> 4) * (HQD * 16) + (lane & 15) * 16, WS_QP + (qc * 64 + qk * 16) * 16);
        if (__all(pair_ok(gp, tag))) break;
        __builtin_amdgcn_s_sleep(8);
      }
      const unsigned q = pk2(rows_sum(__uint_as_float(gp[0])), rows_sum(__uint_as_float(gp[2])));
      if (lane < 16) __builtin_amdgcn_raw_buffer_store_b64(u32x2{q, tag}, rws, lane * 8, WS_Q + (qc * 64 + qk * 16) * 8, SC1);
    }
    if (f1) {
      const f32x4 h = gather_quarters(rws, WS_HP + (hc * 64 + hk * 16) * 8, FF * 8, tag, lane);
      if (lane < 8) st_dev(rws, u32x4{pk2(silu(rbf(h[0])), silu(rbf(h[1]))), tag, pk2(silu(rbf(h[2])), silu(rbf(h[3]))), tag}, lane * 16, WS_H + (hc * 64 + hk * 16) * 8);
    }
    {
      const int b = wg >> 7, kvh = (wg >> 4) & 7;
      u32x4 gq[4];
#pragma unroll 1
      for (unsigned spin = 0; spin < SPIN_CAP; ++spin) {
        asm volatile("" ::: "memory");
#pragma unroll
        for (int i = 0; i < 4; ++i) gq[i] = ld_dev(rws, lane * 16 + i * 1024, WS_Q + kvh * 4096);
        if (__all(pair_ok(gq[0], tag) & pair_ok(gq[1], tag) & pair_ok(gq[2], tag) & pair_ok(gq[3], tag))) break;
        __builtin_amdgcn_s_sleep(8);
      }
      const unsigned sel = b ? 0x07060302u : 0x05040100u;
#pragma unroll
      for (int i = 0; i < 4; ++i) lds_st<unsigned>(lds0 + L_QI + i * 256 + lane * 4, __builtin_amdgcn_perm(gq[i][2], gq[i][0], sel));
    }
    wait_lds();
    if (lane == 0) *(volatile LAS unsigned*)(size_t)(lds0 + L_QR) = 1u;

    if (f2) {
      u32x4 gh;
#pragma unroll 1
      for (unsigned spin = 0; spin < SPIN_CAP; ++spin) {
        asm volatile("" ::: "memory");
        gh = ld_dev(rws, lane * 16, WS_H + gk * 1024);
        if (__all(pair_ok(gh, tag))) break;
        __builtin_amdgcn_s_sleep(32);
      }
      lds_st<unsigned>(lds0 + L_HS + lane * 4, __builtin_amdgcn_perm(gh[2], gh[0], 0x05040100u));
      lds_st<unsigned>(lds0 + L_HS + 256 + lane * 4, __builtin_amdgcn_perm(gh[2], gh[0], 0x07060302u));
    }
    {
      sleep_until(th0 + TS2);
      u32x4 ta;
#pragma unroll 1
      for (unsigned spin = 0; spin < SPIN_CAP; ++spin) {
        ta = *(volatile LAS u32x4*)(size_t)(lds0 + L_TS);
        if (__all((ta[0] != 0) & (ta[1] != 0) & (ta[2] != 0) & (ta[3] != 0))) break;
        __builtin_amdgcn_s_sleep(8);
      }
      const u32x4 p0 = *(volatile LAS u32x4*)(size_t)(lds0 + L_TP), p1 = *(volatile LAS u32x4*)(size_t)(lds0 + L_TP + 16);
      {
        const unsigned a = th0 + (((ta[0] - th0) + (ta[1] - th0) + (ta[2] - th0) + (ta[3] - th0)) >> 2);
        const unsigned b = th0 + (((p0[1] - th0) + (p0[3] - th0) + (p1[1] - th0) + (p1[3] - th0)) >> 2);
        const unsigned c4 = p0[0] + p0[2] + p1[0] + p1[2];
        if (lane == 0) st_dev(rws, u32x4{a, tag, (b << 8) | c4, tag}, 0, WS_PC + wg * 128);
      }
      sleep_until(now32() + TSD);
      u32x4 gp;
#pragma unroll 1
      for (unsigned spin = 0; spin < SPIN_CAP; ++spin) {
        asm volatile("" ::: "memory");
        gp = ld_dev(rws, (lane & (NSAMP - 1)) * 128, WS_PC + (wg & ~(NSAMP - 1)) * 128);
        if (__all(pair_ok(gp, tag))) break;
        __builtin_amdgcn_s_sleep(24);
      }
      const float tb = (float)((int)((gp[2] & 0xffffff00u) - (th0 << 8)) >> 8), ta_ = (float)(int)(gp[0] - th0);
      const float cb = 0.25f * (float)(gp[2] & 0xffu);
      const float r = fmaxf(cb - (float)CPA, 0.25f) * __builtin_amdgcn_rcpf(fmaxf(tb - ta_, 25.f)) * RBOOST;
      const float inv = __builtin_amdgcn_rcpf(wave_sum(r));
      const float fin = fminf(fmaxf(wave_sum((float)(NUNIT - 4) - cb + r * tb) * inv, 0.f), 8000.f);
      if (lane == 0) *(volatile LAS u32x2*)(size_t)(lds0 + L_TL) = u32x2{(th0 + (unsigned)((int)fin - PACE_OFF)) | 1u, __float_as_uint(64.f * inv)};
    }
    {
#pragma unroll 1
      for (unsigned spin = 0; spin < SPIN_CAP * 4; ++spin) {
        const u32x4 mr = *(volatile LAS u32x4*)(size_t)(lds0 + L_MR);
        if (__all((mr[0] & mr[1] & mr[2] & mr[3]) != 0)) break;
        __builtin_amdgcn_s_sleep(8);
      }
      asm volatile("" ::: "memory");
      __builtin_amdgcn_s_setprio(0);
      const f32x4 ma = lds_ld<f32x4>(lds0 + L_ML + (lane & 1) * 32), mb = lds_ld<f32x4>(lds0 + L_ML + (lane & 1) * 32 + 16);
      const float Ma = fmaxf(fmaxf(ma[0], ma[1]), fmaxf(ma[2], ma[3])), Mb = fmaxf(fmaxf(mb[0], mb[1]), fmaxf(mb[2], mb[3]));
      float La = 0.f, Lb = 0.f;
#pragma unroll 4
      for (int i = 0; i < 16; ++i) {
        const u32x4 xp = lds_ld<u32x4>(lds0 + L_SC + i * 1024 + lane * 16);
        La += (ex2((bflo(xp[0]) - Ma) * L2E) + ex2((bfhi(xp[0]) - Ma) * L2E)) + (ex2((bflo(xp[1]) - Ma) * L2E) + ex2((bfhi(xp[1]) - Ma) * L2E));
        Lb += (ex2((bflo(xp[2]) - Mb) * L2E) + ex2((bfhi(xp[2]) - Mb) * L2E)) + (ex2((bflo(xp[3]) - Mb) * L2E) + ex2((bfhi(xp[3]) - Mb) * L2E));
      }
      La += dpp<XOR2>(La); Lb += dpp<XOR2>(Lb);
      La = quads_sum(La); Lb = quads_sum(Lb);
      __builtin_amdgcn_s_setprio(3);
      if (lane < 4) st_dev(rws, u32x4{__float_as_uint(lane & 2 ? Mb : Ma), tag, __float_as_uint(lane & 2 ? Lb : La), tag}, ((lane & 1) * 2 + (lane >> 1)) * 16, WS_ML + wg * 128);
      sleep_until(now32() + ML_DELAY);
      u32x4 gm;
#pragma unroll 1
      for (unsigned spin = 0; spin < SPIN_CAP; ++spin) {
        asm volatile("" ::: "memory");
        gm = ld_dev(rws, (lane >> 2) * 128 + (lane & 3) * 16, WS_ML + (wg & ~15) * 128);
        if (__all(pair_ok(gm, tag))) break;
        __builtin_amdgcn_s_sleep(32);
      }
      const float ms = __uint_as_float(gm[0]), Mg = quads_max(ms);
      const float inv = __builtin_amdgcn_rcpf(quads_sum(ex2((ms - Mg) * L2E) * __uint_as_float(gm[2])));
      if (lane < 4) lds_st<f32x2>(lds0 + L_GS + lane * 8, f32x2{Mg, inv});
      wait_lds();
      if (lane == 0) *(volatile LAS unsigned*)(size_t)(lds0 + L_GR) = 1u;
    }
    wg_barrier();
    if (f2) {
      lds_wait_nonzero(lds0 + L_WR); lds_wait_nonzero(lds0 + L_WR + 4);
      const f32x2 y = proj64(lds0 + L_HS, 256, lds0 + L_WF, lane);
      qo.store(rws, y[0], y[1], WS_FG + (gk * DM + gc * 64) * 8);
    }
    float xr = bflo(xo);
    unsigned vst = (lane & 15) == 0 ? vout : OOB;
    asm volatile("" : "+v"(xr), "+v"(vst) : "s"(rout));
    wg_barrier();
    float gate;
    {
      u32x4 gf;
#pragma unroll 1
      for (unsigned spin = 0; spin < SPIN_CAP; ++spin) {
        asm volatile("" ::: "memory");
        gf = ld_dev(rws, (lane < 14 ? lane : 0) * (DM * 8), WS_FG + cg * 16);
        if (__all(quad_ok(gf, tag))) break;
      }
      if (lane >= 14) gf = u32x4{0u, 0u, 0u, 0u};
      gate = rbf(silu(rbf(quad_sum(quad_val(gf[0]), quad_val(gf[1]), quad_val(gf[2]), quad_val(gf[3])))));
      asm volatile("" : "+v"(gate));
    }
    {
      u32x4 gs = {};
      bool have = false;
#pragma unroll 1
      for (unsigned spin = 0; spin < SPIN_CAP; ++spin) {
        asm volatile("" ::: "memory");
        if (!have) gs = ld_dev(rws, lane * (DM * 8), WS_P2 + cg * 16);
        have = quad_ok(gs, tag);
        if (__all(have)) break;
      }
      const float s = rbf(quad_sum(quad_val(gs[0]), quad_val(gs[1]), quad_val(gs[2]), quad_val(gs[3])));
      __builtin_amdgcn_raw_buffer_store_b16(__builtin_bit_cast(u16, (__bf16)(xr + rbf(s * gate))), rout, vst, 0, 0);
      if (wg == 0 && lane == 0) __builtin_amdgcn_raw_buffer_store_b32(epoch, rws, 0, WS_EPOCH, SC1);
    }
  }
}

}

extern "C" void kernel_launch(void* const* d_in, const int* in_sizes, int n_in, void* d_out, int out_size, void* d_ws, size_t ws_size,
                              hipStream_t stream) {
  (void)hipFuncSetAttribute((const void*)fused_decode_kernel, hipFuncAttributeMaxDynamicSharedMemorySize, L_END);
  fused_decode_kernel<<<NWG, NTHR, L_END, stream>>>((const u16*)d_in[0], (const u16*)d_in[1], (const u16*)d_in[2], (const u16*)d_in[3],
                                                    (const u16*)d_in[4], (const u16*)d_in[6], (const u16*)d_in[7], (u16*)d_out, (char*)d_ws);
}
```

```cpp
#include <hip/hip_runtime.h>
#include <hip/hip_bf16.h>
#include <type_traits>

namespace {

typedef unsigned short u16;
typedef short s16x4 __attribute__((ext_vector_type(4)));
typedef __bf16 bf16x8 __attribute__((ext_vector_type(8)));
typedef __bf16 bf16x2 __attribute__((ext_vector_type(2)));
typedef float f32x2 __attribute__((ext_vector_type(2)));
typedef float f32x4 __attribute__((ext_vector_type(4)));
typedef unsigned u32x2 __attribute__((ext_vector_type(2)));
typedef unsigned u32x4 __attribute__((ext_vector_type(4)));
typedef __amdgpu_buffer_rsrc_t rsrc_t;
#define LAS __attribute__((address_space(3)))

constexpr int NWG = 256, NTHR = 320;
constexpr int DM = 512, HQD = 4096, SK = 32768, DH = 128, FF = 1792;
constexpr int NSLOT = 6, AHEAD = NSLOT - 1, NTILE = 32, NUNIT = 2 * NTILE, NPRE = 11;
constexpr int WIN = 4, WIN2 = 8;
constexpr int CODE_KB = 29;
constexpr int CPA = 13, TS2 = 1430, TSD = 100, PACE_OFF = 79, OVER = 4, NSAMP = 32, POLL_AT = 200, ML_DELAY = 100;
constexpr float RBOOST = 0.99f, DELTA = 55.f;
constexpr int KV_BYTES = 2 * 8 * SK * DH * 2;
constexpr float SCALE = 0.08838834764831845f, L2E = 1.4426950408889634f;
constexpr float M_INIT = -3.0e38f;
constexpr unsigned SPIN_CAP = 1u << 18;
constexpr unsigned OOB = 0x80000000u;

constexpr int WS_EPOCH = 0;
constexpr int WS_Q = 4096;
constexpr int WS_H = WS_Q + HQD * 8;
constexpr int WS_ML = 65536;
constexpr int WS_PC = WS_ML + NWG * 128;
constexpr int WS_QP = WS_PC + NWG * 128;
constexpr int WS_HP = WS_QP + 4 * HQD * 16;
constexpr int WS_FG = WS_HP + 4 * FF * 8;
constexpr int WS_P2 = WS_FG + 14 * DM * 8;
constexpr int WS_P1 = WS_P2 + 64 * DM * 8;
constexpr int WS_END = WS_P1 + NWG * 2048;
static_assert(WS_H + FF * 8 <= WS_ML, "workspace map");

constexpr int L_RING = 0;
constexpr int L_WQ = 4 * NSLOT * 4096;
constexpr int L_SC = L_WQ;
constexpr int L_WF = L_WQ + 16384;
constexpr int L_WO = L_WF + 16384;
constexpr int L_XN = L_WO + 16384;
constexpr int L_QI = L_XN + 2048;
constexpr int L_OS = L_QI + 1024;
constexpr int L_HS = L_OS + 256;
constexpr int L_ML = L_HS + 512;
constexpr int L_TAG = L_ML + 128;
constexpr int L_QR = L_TAG + 4;
constexpr int L_TL = L_TAG + 16;
constexpr int L_TS = L_TL + 16;
constexpr int L_TP = L_TS + 16;
constexpr int L_MR = L_TP + 32;
constexpr int L_GR = L_MR + 16;
constexpr int L_WR = L_GR + 8;
constexpr int L_GS = L_GR + 16;
constexpr int L_END = L_GS + 32;
static_assert(L_GS - L_TAG == 28 * 4 && L_END <= 160 * 1024, "LDS map");

__device__ __forceinline__ float rbf(float f) { return (float)(__bf16)f; }
__device__ __forceinline__ unsigned pk2(float lo, float hi) {
  bf16x2 v = {(__bf16)lo, (__bf16)hi};
  return __builtin_bit_cast(unsigned, v);
}
__device__ __forceinline__ float bflo(unsigned u) { return __uint_as_float(u << 16); }
__device__ __forceinline__ float bfhi(unsigned u) { return __uint_as_float(u & 0xffff0000u); }
template <int CTRL> __device__ __forceinline__ float dpp(float x) {
  return __builtin_bit_cast(float, __builtin_amdgcn_mov_dpp(__builtin_bit_cast(int, x), CTRL, 0xf, 0xf, true));
}
constexpr int XOR1 = 0xB1, XOR2 = 0x4E, ROR4 = 0x124, ROR8 = 0x128;
__device__ __forceinline__ float swap16_sum(float a, float b) {
  auto s = __builtin_amdgcn_permlane16_swap(__float_as_uint(a), __float_as_uint(b), false, false);
  return __uint_as_float(s[0]) + __uint_as_float(s[1]);
}
__device__ __forceinline__ float swap32_sum(float a, float b) {
  auto s = __builtin_amdgcn_permlane32_swap(__float_as_uint(a), __float_as_uint(b), false, false);
  return __uint_as_float(s[0]) + __uint_as_float(s[1]);
}
__device__ __forceinline__ float rows_sum(float x) { x = swap16_sum(x, x); return swap32_sum(x, x); }
__device__ __forceinline__ float rows_max(float x) {
  auto s = __builtin_amdgcn_permlane16_swap(__float_as_uint(x), __float_as_uint(x), false, false);
  x = fmaxf(__uint_as_float(s[0]), __uint_as_float(s[1]));
  auto t = __builtin_amdgcn_permlane32_swap(__float_as_uint(x), __float_as_uint(x), false, false);
  return fmaxf(__uint_as_float(t[0]), __uint_as_float(t[1]));
}
__device__ __forceinline__ float quads_sum(float x) { x += dpp<ROR4>(x); x += dpp<ROR8>(x); return rows_sum(x); }
__device__ __forceinline__ float quads_max(float x) { x = fmaxf(x, dpp<ROR4>(x)); x = fmaxf(x, dpp<ROR8>(x)); return rows_max(x); }
__device__ __forceinline__ float wave_sum(float x) { x += dpp<XOR1>(x); x += dpp<XOR2>(x); return quads_sum(x); }
__device__ __forceinline__ float ex2(float x) { return __builtin_amdgcn_exp2f(x); }
__device__ __forceinline__ float silu(float v) { return v * __builtin_amdgcn_rcpf(1.f + ex2(-v * L2E)); }

template <int N> __device__ __forceinline__ void wait_vm() {
  __builtin_amdgcn_sched_barrier(0);
  asm volatile("s_waitcnt vmcnt(%0)" ::"i"(N) : "memory");
}
__device__ __forceinline__ void wait_lds() { asm volatile("s_waitcnt lgkmcnt(0)" ::: "memory"); }
__device__ __forceinline__ void wg_barrier() {
  asm volatile("s_waitcnt lgkmcnt(0)\n\ts_barrier" ::: "memory");
}
__device__ __forceinline__ void lds_wait_nonzero(unsigned addr) {
#pragma unroll 1
  for (unsigned spin = 0; spin < SPIN_CAP * 16 && *(volatile LAS unsigned*)(size_t)addr == 0; ++spin) __builtin_amdgcn_s_sleep(1);
  asm volatile("" ::: "memory");
}
__device__ __forceinline__ unsigned now32() { return (unsigned)__builtin_amdgcn_s_memrealtime(); }
__device__ __forceinline__ void sleep_until(unsigned due) { while ((int)(now32() - due) < 0) __builtin_amdgcn_s_sleep(2); }
__device__ __forceinline__ rsrc_t make_rsrc(const void* p, int bytes) {
  return __builtin_amdgcn_make_buffer_rsrc((void*)p, (short)0, bytes, 0x00020000);
}
constexpr int SC1 = 16;
__device__ __forceinline__ u32x4 ld_dev(rsrc_t r, unsigned voff, unsigned soff) { return __builtin_amdgcn_raw_buffer_load_b128(r, voff, soff, SC1); }
__device__ __forceinline__ void st_dev(rsrc_t r, u32x4 v, unsigned voff, unsigned soff) { __builtin_amdgcn_raw_buffer_store_b128(v, r, voff, soff, SC1); }
__device__ __forceinline__ bool pair_ok(const u32x4& g, unsigned tag) { return (g[1] == tag) & (g[3] == tag); }
__device__ __forceinline__ bool quad_ok(const u32x4& g, unsigned tag) {
  return (__builtin_amdgcn_perm(g[1], g[0], 0x0c0c0400u) | __builtin_amdgcn_perm(g[3], g[2], 0x04000c0cu)) == tag;
}
__device__ __forceinline__ float quad_val(unsigned w) { return __uint_as_float(w & 0xffffff00u); }
struct QuadOut {
  unsigned t0, t1, t2, t3, voff;
  __device__ __forceinline__ QuadOut(unsigned tag, int lane) : t0(tag & 255u), t1((tag >> 8) & 255u), t2((tag >> 16) & 255u), t3(tag >> 24), voff(lane & 1 ? OOB : lane * 8) {
    asm volatile("" : "+v"(t0), "+v"(t1), "+v"(t2), "+v"(t3), "+v"(voff));
  }
  __device__ __forceinline__ void store(rsrc_t r, float fa, float fb, unsigned soff) const {
    const unsigned a = __float_as_uint(fa) + 0x80u, b = __float_as_uint(fb) + 0x80u;
    const unsigned c = __float_as_uint(dpp<XOR1>(__uint_as_float(a))), d = __float_as_uint(dpp<XOR1>(__uint_as_float(b)));
    st_dev(r, u32x4{(a & 0xffffff00u) | t0, (b & 0xffffff00u) | t1, (c & 0xffffff00u) | t2, (d & 0xffffff00u) | t3}, voff, soff);
  }
};
__device__ __forceinline__ float quad_sum(float v0, float v1, float v2, float v3) {
  float x = swap16_sum(swap32_sum(v0, v2), swap32_sum(v1, v3));
  x += dpp<ROR8>(x); x += dpp<ROR4>(x); x += dpp<XOR2>(x); x += dpp<XOR1>(x);
  return x;
}
__device__ __forceinline__ s16x4 tr_read(unsigned addr) { return __builtin_amdgcn_ds_read_tr16_b64_v4i16((LAS s16x4*)(size_t)addr); }
template <typename T> __device__ __forceinline__ T lds_ld(unsigned addr) { return *(const LAS T*)(size_t)addr; }
template <typename T> __device__ __forceinline__ void lds_st(unsigned addr, T v) { *(LAS T*)(size_t)addr = v; }

__device__ __forceinline__ void dma_piece(rsrc_t r, unsigned voff, unsigned soff, unsigned dst) {
  asm volatile("s_mov_b32 m0, %0\n\ts_nop 0\n\tbuffer_load_dwordx4 %1, %2, %3 offen nt lds" ::"s"(dst), "v"(voff), "s"(r), "s"(soff) : "memory");
}
__device__ __forceinline__ void dma_half(rsrc_t r, unsigned v0, unsigned v1, unsigned v2, unsigned v3, unsigned soff, unsigned dst) {
  asm volatile(
      "s_mov_b32 m0, %0\n\ts_nop 0\n\t"
      "buffer_load_dwordx4 %1, %5, %6 offen nt lds\n\t"
      "buffer_load_dwordx4 %2, %5, %6 offen offset:1024 nt lds\n\t"
      "buffer_load_dwordx4 %3, %5, %6 offen offset:2048 nt lds\n\t"
      "buffer_load_dwordx4 %4, %5, %6 offen offset:3072 nt lds"
      ::"s"(dst), "v"(v0), "v"(v1), "v"(v2), "v"(v3), "s"(r), "s"(soff)
      : "memory");
}
template <int A> __device__ __forceinline__ f32x4 mfma4(s16x4 a, s16x4 b, f32x4 c) { return __builtin_amdgcn_mfma_f32_4x4x4bf16_1k(a, b, c, 4, A, 0); }
template <int A, int N> struct Proj {
  static __device__ __forceinline__ void run(const s16x4 (&av)[2], unsigned ba, f32x4 (&c)[4]) {
    c[A & 3] = mfma4<A & 15>(av[A >> 4], tr_read(ba + A * 512), c[A & 3]);
    if constexpr (A + 1 < N) Proj<A + 1, N>::run(av, ba, c);
  }
};
template <int A> struct ProjReg {
  static __device__ __forceinline__ void run(s16x4 a, const s16x4 (&w)[16], f32x4 (&c)[2]) {
    c[A & 1] = mfma4<A>(a, w[A], c[A & 1]);
    if constexpr (A + 1 < 16) ProjReg<A + 1>::run(a, w, c);
  }
};
__device__ __forceinline__ f32x2 proj64(unsigned a_img, int a_stride, unsigned w_img, int lane) {
  const int q = (lane & 15) >> 2, p = lane & 3;
  const unsigned aa = a_img + (lane & 1) * a_stride + (lane >> 2) * 8;
  const s16x4 av[2] = {lds_ld<s16x4>(aa), lds_ld<s16x4>(aa + 128)};
  const unsigned ba = w_img + q * 128 + (((2 * (lane >> 4) + (p >> 1)) ^ ((q >> 1) << 2)) << 4) + (p & 1) * 8;
  f32x4 c[4] = {};
  Proj<0, 32>::run(av, ba, c);
  return f32x2{(c[0][0] + c[1][0]) + (c[2][0] + c[3][0]), (c[0][1] + c[1][1]) + (c[2][1] + c[3][1])};
}
__device__ __forceinline__ f32x4 gather_quarters(rsrc_t rws, unsigned base, int quarter_bytes, unsigned tag, int lane) {
  u32x4 g;
#pragma unroll 1
  for (unsigned spin = 0; spin < SPIN_CAP; ++spin) {
    asm volatile("" ::: "memory");
    g = ld_dev(rws, (lane >> 4) * quarter_bytes + (lane & 7) * 16, base);
    if (__all(quad_ok(g, tag))) break;
    __builtin_amdgcn_s_sleep(8);
  }
  return f32x4{rows_sum(quad_val(g[0])), rows_sum(quad_val(g[1])), rows_sum(quad_val(g[2])), rows_sum(quad_val(g[3]))};
}

template <int N> using Int = std::integral_constant<int, N>;
struct KFrag { bf16x8 k[4]; };
struct VFrag { s16x4 v[4][2]; };

__device__ __forceinline__ void read_k(KFrag& t, const unsigned (&ka)[4], unsigned off) {
#pragma unroll
  for (int s = 0; s < 4; ++s) t.k[s] = lds_ld<bf16x8>(ka[s] + off);
  asm volatile("s_waitcnt lgkmcnt(0)" : "+v"(t.k[0]), "+v"(t.k[1]), "+v"(t.k[2]), "+v"(t.k[3])::"memory");
}
__device__ __forceinline__ void read_v(VFrag& t, const unsigned (&va)[2], unsigned off) {
#pragma unroll
  for (int a = 0; a < 4; ++a)
#pragma unroll
    for (int h = 0; h < 2; ++h) t.v[a][h] = tr_read(va[h] + off + a * 1024);
  asm volatile("s_waitcnt lgkmcnt(0)"
               : "+v"(t.v[0][0]), "+v"(t.v[0][1]), "+v"(t.v[1][0]), "+v"(t.v[1][1]), "+v"(t.v[2][0]), "+v"(t.v[2][1]), "+v"(t.v[3][0]), "+v"(t.v[3][1])
               :
               : "memory");
}
__device__ __forceinline__ void score_tile(const KFrag& t, const bf16x8 (&qf)[4], float& m, unsigned dst, bool writer) {
  f32x4 sc = {0.f, 0.f, 0.f, 0.f};
#pragma unroll
  for (int s = 0; s < 4; ++s) sc = __builtin_amdgcn_mfma_f32_16x16x32_bf16(t.k[s], qf[s], sc, 0, 0, 0);
  m = fmaxf(fmaxf(m, fmaxf(sc[0], sc[1])), fmaxf(sc[2], sc[3]));
  const unsigned r0 = pk2(sc[0], sc[1]), r1 = pk2(sc[2], sc[3]);
  if (writer) lds_st<u32x2>(dst, u32x2{pk2(bflo(r0) * SCALE, bfhi(r0) * SCALE), pk2(bflo(r1) * SCALE, bfhi(r1) * SCALE)});
}
__device__ __forceinline__ s16x4 probs(unsigned src, float M, float f) {
  const u32x2 xp = lds_ld<u32x2>(src);
  const u32x2 pp = {pk2(f * ex2((bflo(xp[0]) - M) * L2E), f * ex2((bfhi(xp[0]) - M) * L2E)), pk2(f * ex2((bflo(xp[1]) - M) * L2E), f * ex2((bfhi(xp[1]) - M) * L2E))};
  return __builtin_bit_cast(s16x4, pp);
}
__device__ __forceinline__ void value_tile(const VFrag& t, s16x4 pa, f32x4 (&o)[2]) {
#pragma unroll
  for (int h = 0; h < 2; ++h) o[h] = mfma4<0>(pa, t.v[0][h], o[h]);
#pragma unroll
  for (int h = 0; h < 2; ++h) o[h] = mfma4<4>(pa, t.v[1][h], o[h]);
#pragma unroll
  for (int h = 0; h < 2; ++h) o[h] = mfma4<8>(pa, t.v[2][h], o[h]);
#pragma unroll
  for (int h = 0; h < 2; ++h) o[h] = mfma4<12>(pa, t.v[3][h], o[h]);
}

__global__ void __launch_bounds__(NTHR, 2)
fused_decode_kernel(const u16* __restrict__ x, const u16* __restrict__ kc, const u16* __restrict__ vc, const u16* __restrict__ wq,
                    const u16* __restrict__ wo, const u16* __restrict__ w1, const u16* __restrict__ w2, u16* __restrict__ out, char* ws) {
  unsigned long long pc;
  asm volatile("s_getpc_b64 %0" : "=s"(pc));
  extern __shared__ __attribute__((aligned(16))) unsigned char lds[];
  const unsigned lds0 = (unsigned)(size_t)(LAS unsigned char*)lds;
  const int lane = threadIdx.x & 63, wave = __builtin_amdgcn_readfirstlane(threadIdx.x >> 6), wg = blockIdx.x;
  const int xg = wg & 7, xi = wg >> 3;
  asm volatile("" ::"s"(x), "s"(kc), "s"(vc), "s"(wq), "s"(wo), "s"(w1), "s"(w2), "s"(out), "s"(ws));
  const rsrc_t rws = make_rsrc(ws, WS_END);
  const unsigned sw128 = ((lane & 7) ^ (((lane >> 4) & 1) << 2)) << 4;

  if (wave < 4) {
    const rsrc_t rk = make_rsrc(kc, KV_BYTES), rv = make_rsrc(vc, KV_BYTES);
    const int g = lane >> 4, c = lane & 15;
    unsigned vk[4];
#pragma unroll
    for (int u = 0; u < 4; ++u) vk[u] = g * 256 + ((c ^ (4 * u + g)) << 4);
    const unsigned vv = g * 256 + ((c ^ (g << 2)) << 4);
    const unsigned ring = lds0 + L_RING + wave * (NSLOT * 4096);
    const unsigned tile0 = (unsigned)wg * (2048 * 256) + wave * 4096;
    unsigned ka[4], va[2];
#pragma unroll
    for (int s = 0; s < 4; ++s) ka[s] = ring + c * 256 + (((4 * s + g) ^ c) << 4);
#pragma unroll
    for (int h = 0; h < 2; ++h) va[h] = ring + (c >> 2) * 256 + (((8 * h + 2 * g + ((c & 3) >> 1)) ^ ((c >> 2) << 2)) << 4) + (c & 1) * 8;
    const unsigned sca = lds0 + L_SC + wave * 4096 + (g * 4 + (c & 3)) * 8;
    auto issue_k = [&](int t, unsigned off) { dma_half(rk, vk[0], vk[1], vk[2], vk[3], tile0 + t * 16384, ring + off); };
    auto issue_v = [&](int t, unsigned off) { dma_half(rv, vv, vv, vv, vv, tile0 + t * 16384, ring + off); };

    wg_barrier();
#pragma unroll
    for (int u = 0; u < AHEAD; ++u) { wait_vm<WIN>(); issue_k(u, u * 4096); }
    KFrag pk[NPRE];
#pragma unroll
    for (int u = 0; u < NPRE; ++u) {
      wait_vm<WIN>();
      issue_k(u + AHEAD, ((u + AHEAD) % NSLOT) * 4096);
      read_k(pk[u], ka, (u % NSLOT) * 4096);
    }
    lds_wait_nonzero(lds0 + L_QR);
    bf16x8 qf[4];
#pragma unroll
    for (int s = 0; s < 4; ++s) qf[s] = lds_ld<bf16x8>(lds0 + L_QI + (c & 3) * 256 + (4 * s + g) * 16);
    const unsigned tag = lds_ld<unsigned>(lds0 + L_TAG);

    unsigned prev = now32();
    auto landed = [&](int u, auto win) {
      const u32x2 plan = *(volatile LAS u32x2*)(size_t)(lds0 + L_TL);
      wait_vm<win.value>();
      unsigned tn = now32();
      *(volatile LAS u32x2*)(size_t)(lds0 + L_TP + wave * 8) = u32x2{(unsigned)u, tn};
      if (u == CPA) *(volatile LAS unsigned*)(size_t)(lds0 + L_TS + wave * 4) = tn | 1u;
      const unsigned tl = __builtin_amdgcn_readfirstlane(plan[0]);
      const int d = (int)(tl - prev);
      if (tl != 0 && d > 0) {
        const float n = (float)(NUNIT - AHEAD - u);
        const bool behind = (float)d + DELTA < n * __uint_as_float(plan[1]);
        const unsigned due = prev + (unsigned)__builtin_amdgcn_readfirstlane(behind ? 0 : (int)((float)d * __builtin_amdgcn_rcpf(n)));
        while ((int)(tn - due) < 0) { __builtin_amdgcn_s_sleep(1); tn = now32(); }
      }
      prev = tn + OVER;
    };
    static_assert(WIN <= 16 && WIN2 <= 16 && 2 * NPRE <= NTILE - AHEAD && CPA >= NPRE, "unit schedule");
    float m = M_INIT;
    KFrag kf;
#pragma unroll
    for (int u = NPRE; u < 2 * NPRE; ++u) {
      landed(u, Int<WIN>{}); issue_k(u + AHEAD, ((u + AHEAD) % NSLOT) * 4096);
      read_k(kf, ka, (u % NSLOT) * 4096);
      score_tile(kf, qf, m, sca + u * 128, c < 4);
      score_tile(pk[u - NPRE], qf, m, sca + (u - NPRE) * 128, c < 4);
    }
    unsigned off = ((2 * NPRE) % NSLOT) * 4096, offp = ((2 * NPRE + AHEAD) % NSLOT) * 4096;
    auto next_slot = [&]() { offp = off; off = off == (NSLOT - 1) * 4096 ? 0u : off + 4096; };
#pragma unroll 1
    for (int u = 2 * NPRE; u < NTILE - AHEAD; ++u) {
      landed(u, Int<WIN>{}); issue_k(u + AHEAD, offp);
      read_k(kf, ka, off);
      score_tile(kf, qf, m, sca + u * 128, c < 4);
      next_slot();
    }
#pragma unroll 1
    for (int u = NTILE - AHEAD; u < NTILE; ++u) {
      landed(u, Int<WIN2>{}); issue_v(u + AHEAD - NTILE, offp);
      read_k(kf, ka, off);
      score_tile(kf, qf, m, sca + u * 128, c < 4);
      next_slot();
    }
    const float M = rbf(rbf(rows_max(m)) * SCALE);
    if (lane < 4) lds_st<float>(lds0 + L_ML + lane * 16 + wave * 4, M);
    wait_lds();
    if (lane == 0) *(volatile LAS unsigned*)(size_t)(lds0 + L_MR + wave * 4) = 1u;
    f32x4 o[2] = {};
    VFrag vf;
#pragma unroll 1
    for (int u = NTILE; u < NUNIT - AHEAD; ++u) {
      landed(u, Int<WIN2>{}); issue_v(u + AHEAD - NTILE, offp);
      const s16x4 pa = probs(sca + (u - NTILE) * 128, M, 1.f);
      read_v(vf, va, off);
      value_tile(vf, pa, o);
      next_slot();
    }
    const int ks = xg * 8 + (xi >> 2), cs = xi & 3;
    if (wave < 2) {
      const rsrc_t ro = make_rsrc(wo, HQD * DM * 2);
#pragma unroll
      for (int i = 0; i < 8; ++i)
        dma_piece(ro, (lane >> 3) * (DM * 2) + sw128, (ks * 64 + 8 * i) * (DM * 2) + cs * 256 + wave * 128, lds0 + L_WO + wave * 8192 + i * 1024);
    }
    const bool late_w2 = wave >= 2 && xi >= 14 && xi < 28;
    if (late_w2) {
      const int fi = xg * 14 + xi - 14;
      const rsrc_t r2 = make_rsrc(w2, DM * FF * 2);
#pragma unroll
      for (int i = 0; i < 8; ++i)
        dma_piece(r2, (lane >> 3) * (DM * 2) + sw128, ((fi >> 3) * 128 + (wave & 1) * 64 + 8 * i) * (DM * 2) + (fi & 7) * 128, lds0 + L_WF + (wave & 1) * 8192 + i * 1024);
    }
    lds_wait_nonzero(lds0 + L_GR);
    const f32x2 gs = lds_ld<f32x2>(lds0 + L_GS + (c & 3) * 8);
    const float f = ex2((M - gs[0]) * L2E) * gs[1];
    {
      const float f0 = dpp<0x00>(f), f1 = dpp<0x55>(f), f2 = dpp<0xAA>(f), f3 = dpp<0xFF>(f);
#pragma unroll
      for (int h = 0; h < 2; ++h) { o[h][0] *= f0; o[h][1] *= f1; o[h][2] *= f2; o[h][3] *= f3; }
    }
    const QuadOut qo(tag, lane);
    unsigned s_p1 = WS_P1 + wg * 2048 + wave * 512, s_p2 = WS_P2 + ks * (DM * 8) + (cs * 128 + wave * 64) * 8;
    unsigned a_mw = ring + lane * 4, a_mr = lds0 + L_RING + (wave * 128 + 2 * lane) * 4;
    unsigned a_ow = lds0 + L_OS + (wave >> 1) * 128 + ((wave & 1) * 32 + 4 * (lane & 7) + g) * 2, a_or = lds0 + L_OS + (lane & 1) * 128 + (lane >> 2) * 8;
    asm volatile("" : "+s"(s_p1), "+s"(s_p2), "+v"(a_mw), "+v"(a_mr), "+v"(a_ow), "+v"(a_or));
    auto last = [&](auto j) {
      constexpr int u = NUNIT - AHEAD + j.value, younger = (NUNIT - 1 - u) * 4;
      s16x4 pa = probs(sca + (u - NTILE) * 128, M, f);
      asm volatile("" : "+v"(pa));
      if (wave < 2 || late_w2) wait_vm<younger + 8>(); else wait_vm<younger>();
      read_v(vf, va, (u % NSLOT) * 4096);
      value_tile(vf, pa, o);
    };
    last(Int<0>{}); last(Int<1>{}); last(Int<2>{}); last(Int<3>{}); last(Int<4>{});

#pragma unroll
    for (int h = 0; h < 2; ++h)
#pragma unroll
      for (int r = 0; r < 4; ++r) lds_st<float>(a_mw + (r * 128 + h * 64) * 4, o[h][r]);
    wg_barrier();
    {
      f32x2 ov[4];
#pragma unroll
      for (int w = 0; w < 4; ++w) ov[w] = lds_ld<f32x2>(a_mr + w * (NSLOT * 4096));
      const f32x2 sum = (ov[0] + ov[1]) + (ov[2] + ov[3]);
      qo.store(rws, sum[0], sum[1], s_p1);
    }
    s16x4 wf[16];
    if (wave < 2) {
      const unsigned wa = lds0 + L_WO + wave * 8192 + (c >> 2) * 128 + (((2 * g + ((c & 3) >> 1)) ^ ((c >> 3) << 2)) << 4) + (c & 1) * 8;
      wait_vm<1>();
#pragma unroll
      for (int a = 0; a < 16; ++a) wf[a] = tr_read(wa + a * 512);
    }
    if (late_w2) {
      wait_vm<1>();
      if (lane == 0) *(volatile LAS unsigned*)(size_t)(lds0 + L_WR + (wave & 1) * 4) = 1u;
    }
    {
      const int b = wave >> 1, half = wave & 1;
      const unsigned vo = (lane >> 3) * 2048 + (lane & 7) * 16;
      const unsigned so = WS_P1 + (b * 8 + xg) * 16 * 2048 + ((ks >> 1) & 3) * 512 + ((ks & 1) * 2 + half) * 128;
      sleep_until(__builtin_amdgcn_readfirstlane(*(volatile LAS unsigned*)(size_t)(lds0 + L_TL)) + POLL_AT);
      u32x4 gv[2] = {};
      bool h0 = false, h1 = false;
#pragma unroll 1
      for (unsigned spin = 0; spin < SPIN_CAP; ++spin) {
        asm volatile("" ::: "memory");
        if (!h0) gv[0] = ld_dev(rws, vo, so);
        if (!h1) gv[1] = ld_dev(rws, vo + 8 * 2048, so);
        h0 = quad_ok(gv[0], tag); h1 = quad_ok(gv[1], tag);
        if (__all(h0 & h1)) break;
      }
      float u[4];
#pragma unroll
      for (int j = 0; j < 4; ++j) u[j] = quad_val(gv[0][j]) + quad_val(gv[1][j]);
      float e = swap16_sum(swap32_sum(u[0], u[2]), swap32_sum(u[1], u[3]));
      e += dpp<ROR8>(e);
      lds_st<__bf16>(a_ow, (__bf16)e);
    }
    wg_barrier();
    if (wave < 2) {
      const s16x4 oa = lds_ld<s16x4>(a_or);
      f32x4 acc[2] = {};
      ProjReg<0>::run(oa, wf, acc);
      qo.store(rws, acc[0][0] + acc[1][0], acc[0][1] + acc[1][1], s_p2);
    }
  } else {
    __builtin_amdgcn_s_setprio(3);
    const int cg = xg * 32 + xi;
    const int qk = cg >> 6, qc = cg & 63;
    const bool f1 = xi < 14, f2 = xi >= 14 && xi < 28;
    const int fi = xg * 14 + (xi < 14 ? xi : xi - 14);
    const int hk = fi / 28, hc = fi % 28;
    const int gk = fi >> 3, gc = fi & 7;
    const unsigned vout = (((lane >> 4) & 1) * DM + cg * 2 + (lane >> 5)) * 2;
    const rsrc_t rx = make_rsrc(x, 2 * DM * 2);
    const rsrc_t rout = make_rsrc(out, 2 * DM * 2);
    unsigned ev, xo;
    u32x4 x0, x1, code;
    asm volatile(
        "s_nop 4\n\t"
        "buffer_load_dwordx4 %0, %5, %6, 0 offen\n\t"
        "buffer_load_dwordx4 %1, %5, %6, 0 offen offset:1024\n\t"
        "buffer_load_dword %2, off, %7, 0 sc1\n\t"
        "buffer_load_ushort %3, %8, %6, 0 offen\n\t"
        "buffer_load_dwordx4 %4, %5, %9, %10 offen"
        : "=&v"(x0), "=&v"(x1), "=&v"(ev), "=&v"(xo), "=&v"(code)
        : "v"(lane * 16), "s"(rx), "s"(rws), "v"(vout), "s"(make_rsrc((const void*)(pc & ~255ull), CODE_KB * 1024)), "s"(xi * 1024)
        : "memory");
    {
      const rsrc_t rq = make_rsrc(wq, DM * HQD * 2);
#pragma unroll
      for (int i = 0; i < 16; ++i) dma_piece(rq, (lane >> 3) * (HQD * 2) + sw128, (qk * 128 + 8 * i) * (HQD * 2) + qc * 128, lds0 + L_WQ + i * 1024);
      __builtin_amdgcn_sched_barrier(0);
      if (f1) {
        const rsrc_t r1 = make_rsrc(w1, DM * FF * 2);
#pragma unroll
        for (int i = 0; i < 16; ++i) dma_piece(r1, (lane >> 3) * (FF * 2) + sw128, (hk * 128 + 8 * i) * (FF * 2) + hc * 128, lds0 + L_WF + i * 1024);
      }
    }
    if (lane < 28) lds_st<unsigned>(lds0 + L_TAG + lane * 4, 0u);
    wg_barrier();
    const unsigned th0 = now32();
    if (f1) wait_vm<32>(); else wait_vm<16>();
    asm volatile("" : "+v"(x0), "+v"(x1), "+v"(ev), "+v"(xo), "+v"(code)::"memory");
    const unsigned epoch = max(__builtin_amdgcn_readfirstlane(ev) + 1u, 1u), tag = epoch * 0x9E3779B1u;
    {
      float a[2][8];
#pragma unroll
      for (int j = 0; j < 4; ++j) { a[0][2 * j] = bflo(x0[j]); a[0][2 * j + 1] = bfhi(x0[j]); a[1][2 * j] = bflo(x1[j]); a[1][2 * j + 1] = bfhi(x1[j]); }
#pragma unroll
      for (int b = 0; b < 2; ++b) {
        const float mean = wave_sum(((a[b][0] + a[b][1]) + (a[b][2] + a[b][3])) + ((a[b][4] + a[b][5]) + (a[b][6] + a[b][7]))) * (1.f / DM);
        float q = 0.f;
#pragma unroll
        for (int j = 0; j < 8; ++j) q = fmaf(a[b][j] - mean, a[b][j] - mean, q);
        const float var = wave_sum(q) * (1.f / DM);
        const float mb = rbf(mean), inv = __builtin_amdgcn_rcpf(rbf(__builtin_amdgcn_sqrtf(rbf(rbf(var) + 1e-5f))));
        u32x4 xn;
#pragma unroll
        for (int j = 0; j < 4; ++j) xn[j] = pk2(rbf(a[b][2 * j] - mb) * inv, rbf(a[b][2 * j + 1] - mb) * inv);
        lds_st<u32x4>(lds0 + L_XN + b * 1024 + lane * 16, xn);
      }
    }
    if (lane == 0) lds_st<unsigned>(lds0 + L_TAG, tag);
    const QuadOut qo(tag, lane);
    if (f1) wait_vm<16>(); else wait_vm<0>();
    wait_lds();
    {
      const f32x2 y = proj64(lds0 + L_XN + qk * 256, 1024, lds0 + L_WQ, lane);
      st_dev(rws, u32x4{__float_as_uint(y[0]), tag, __float_as_uint(y[1]), tag}, lane * 16, WS_QP + (qk * HQD + qc * 64) * 16);
    }
    if (f1) {
      wait_vm<1>();
      const f32x2 y = proj64(lds0 + L_XN + hk * 256, 1024, lds0 + L_WF, lane);
      qo.store(rws, y[0], y[1], WS_HP + (hk * FF + hc * 64) * 8);
    }
    {
      u32x4 gp;
#pragma unroll 1
      for (unsigned spin = 0; spin < SPIN_CAP; ++spin) {
        asm volatile("" ::: "memory");
        gp = ld_dev(rws, (lane >> 4) * (HQD * 16) + (lane & 15) * 16, WS_QP + (qc * 64 + qk * 16) * 16);
        if (__all(pair_ok(gp, tag))) break;
        __builtin_amdgcn_s_sleep(8);
      }
      const unsigned q = pk2(rows_sum(__uint_as_float(gp[0])), rows_sum(__uint_as_float(gp[2])));
      if (lane < 16) __builtin_amdgcn_raw_buffer_store_b64(u32x2{q, tag}, rws, lane * 8, WS_Q + (qc * 64 + qk * 16) * 8, SC1);
    }
    if (f1) {
      const f32x4 h = gather_quarters(rws, WS_HP + (hc * 64 + hk * 16) * 8, FF * 8, tag, lane);
      if (lane < 8) st_dev(rws, u32x4{pk2(silu(rbf(h[0])), silu(rbf(h[1]))), tag, pk2(silu(rbf(h[2])), silu(rbf(h[3]))), tag}, lane * 16, WS_H + (hc * 64 + hk * 16) * 8);
    }
    {
      const int b = wg >> 7, kvh = (wg >> 4) & 7;
      u32x4 gq[4];
#pragma unroll 1
      for (unsigned spin = 0; spin < SPIN_CAP; ++spin) {
        asm volatile("" ::: "memory");
#pragma unroll
        for (int i = 0; i < 4; ++i) gq[i] = ld_dev(rws, lane * 16 + i * 1024, WS_Q + kvh * 4096);
        if (__all(pair_ok(gq[0], tag) & pair_ok(gq[1], tag) & pair_ok(gq[2], tag) & pair_ok(gq[3], tag))) break;
        __builtin_amdgcn_s_sleep(8);
      }
      const unsigned sel = b ? 0x07060302u : 0x05040100u;
#pragma unroll
      for (int i = 0; i < 4; ++i) lds_st<unsigned>(lds0 + L_QI + i * 256 + lane * 4, __builtin_amdgcn_perm(gq[i][2], gq[i][0], sel));
    }
    wait_lds();
    if (lane == 0) *(volatile LAS unsigned*)(size_t)(lds0 + L_QR) = 1u;

    if (f2) {
      u32x4 gh;
#pragma unroll 1
      for (unsigned spin = 0; spin < SPIN_CAP; ++spin) {
        asm volatile("" ::: "memory");
        gh = ld_dev(rws, lane * 16, WS_H + gk * 1024);
        if (__all(pair_ok(gh, tag))) break;
        __builtin_amdgcn_s_sleep(32);
      }
      lds_st<unsigned>(lds0 + L_HS + lane * 4, __builtin_amdgcn_perm(gh[2], gh[0], 0x05040100u));
      lds_st<unsigned>(lds0 + L_HS + 256 + lane * 4, __builtin_amdgcn_perm(gh[2], gh[0], 0x07060302u));
    }
    {
      sleep_until(th0 + TS2);
      u32x4 ta;
#pragma unroll 1
      for (unsigned spin = 0; spin < SPIN_CAP; ++spin) {
        ta = *(volatile LAS u32x4*)(size_t)(lds0 + L_TS);
        if (__all((ta[0] != 0) & (ta[1] != 0) & (ta[2] != 0) & (ta[3] != 0))) break;
        __builtin_amdgcn_s_sleep(8);
      }
      const u32x4 p0 = *(volatile LAS u32x4*)(size_t)(lds0 + L_TP), p1 = *(volatile LAS u32x4*)(size_t)(lds0 + L_TP + 16);
      {
        const unsigned a = th0 + (((ta[0] - th0) + (ta[1] - th0) + (ta[2] - th0) + (ta[3] - th0)) >> 2);
        const unsigned b = th0 + (((p0[1] - th0) + (p0[3] - th0) + (p1[1] - th0) + (p1[3] - th0)) >> 2);
        const unsigned c4 = p0[0] + p0[2] + p1[0] + p1[2];
        if (lane == 0) st_dev(rws, u32x4{a, tag, (b << 8) | c4, tag}, 0, WS_PC + wg * 128);
      }
      sleep_until(now32() + TSD);
      u32x4 gp;
#pragma unroll 1
      for (unsigned spin = 0; spin < SPIN_CAP; ++spin) {
        asm volatile("" ::: "memory");
        gp = ld_dev(rws, (lane & (NSAMP - 1)) * 128, WS_PC + (wg & ~(NSAMP - 1)) * 128);
        if (__all(pair_ok(gp, tag))) break;
        __builtin_amdgcn_s_sleep(24);
      }
      const float tb = (float)((int)((gp[2] & 0xffffff00u) - (th0 << 8)) >> 8), ta_ = (float)(int)(gp[0] - th0);
      const float cb = 0.25f * (float)(gp[2] & 0xffu);
      const float r = fmaxf(cb - (float)CPA, 0.25f) * __builtin_amdgcn_rcpf(fmaxf(tb - ta_, 25.f)) * RBOOST;
      const float inv = __builtin_amdgcn_rcpf(wave_sum(r));
      const float fin = fminf(fmaxf(wave_sum((float)(NUNIT - 4) - cb + r * tb) * inv, 0.f), 8000.f);
      if (lane == 0) *(volatile LAS u32x2*)(size_t)(lds0 + L_TL) = u32x2{(th0 + (unsigned)((int)fin - PACE_OFF)) | 1u, __float_as_uint(64.f * inv)};
    }
    {
#pragma unroll 1
      for (unsigned spin = 0; spin < SPIN_CAP * 4; ++spin) {
        const u32x4 mr = *(volatile LAS u32x4*)(size_t)(lds0 + L_MR);
        if (__all((mr[0] & mr[1] & mr[2] & mr[3]) != 0)) break;
        __builtin_amdgcn_s_sleep(8);
      }
      asm volatile("" ::: "memory");
      __builtin_amdgcn_s_setprio(0);
      const f32x4 ma = lds_ld<f32x4>(lds0 + L_ML + (lane & 1) * 32), mb = lds_ld<f32x4>(lds0 + L_ML + (lane & 1) * 32 + 16);
      const float Ma = fmaxf(fmaxf(ma[0], ma[1]), fmaxf(ma[2], ma[3])), Mb = fmaxf(fmaxf(mb[0], mb[1]), fmaxf(mb[2], mb[3]));
      float La = 0.f, Lb = 0.f;
#pragma unroll 4
      for (int i = 0; i < 16; ++i) {
        const u32x4 xp = lds_ld<u32x4>(lds0 + L_SC + i * 1024 + lane * 16);
        La += (ex2((bflo(xp[0]) - Ma) * L2E) + ex2((bfhi(xp[0]) - Ma) * L2E)) + (ex2((bflo(xp[1]) - Ma) * L2E) + ex2((bfhi(xp[1]) - Ma) * L2E));
        Lb += (ex2((bflo(xp[2]) - Mb) * L2E) + ex2((bfhi(xp[2]) - Mb) * L2E)) + (ex2((bflo(xp[3]) - Mb) * L2E) + ex2((bfhi(xp[3]) - Mb) * L2E));
      }
      La += dpp<XOR2>(La); Lb += dpp<XOR2>(Lb);
      La = quads_sum(La); Lb = quads_sum(Lb);
      __builtin_amdgcn_s_setprio(3);
      if (lane < 4) st_dev(rws, u32x4{__float_as_uint(lane & 2 ? Mb : Ma), tag, __float_as_uint(lane & 2 ? Lb : La), tag}, ((lane & 1) * 2 + (lane >> 1)) * 16, WS_ML + wg * 128);
      sleep_until(now32() + ML_DELAY);
      u32x4 gm;
#pragma unroll 1
      for (unsigned spin = 0; spin < SPIN_CAP; ++spin) {
        asm volatile("" ::: "memory");
        gm = ld_dev(rws, (lane >> 2) * 128 + (lane & 3) * 16, WS_ML + (wg & ~15) * 128);
        if (__all(pair_ok(gm, tag))) break;
        __builtin_amdgcn_s_sleep(32);
      }
      const float ms = __uint_as_float(gm[0]), Mg = quads_max(ms);
      const float inv = __builtin_amdgcn_rcpf(quads_sum(ex2((ms - Mg) * L2E) * __uint_as_float(gm[2])));
      if (lane < 4) lds_st<f32x2>(lds0 + L_GS + lane * 8, f32x2{Mg, inv});
      wait_lds();
      if (lane == 0) *(volatile LAS unsigned*)(size_t)(lds0 + L_GR) = 1u;
    }
    wg_barrier();
    if (f2) {
      lds_wait_nonzero(lds0 + L_WR); lds_wait_nonzero(lds0 + L_WR + 4);
      const f32x2 y = proj64(lds0 + L_HS, 256, lds0 + L_WF, lane);
      qo.store(rws, y[0], y[1], WS_FG + (gk * DM + gc * 64) * 8);
    }
    float xr = bflo(xo);
    unsigned vst = (lane & 15) == 0 ? vout : OOB;
    asm volatile("" : "+v"(xr), "+v"(vst) : "s"(rout));
    wg_barrier();
    float gate;
    {
      u32x4 gf;
#pragma unroll 1
      for (unsigned spin = 0; spin < SPIN_CAP; ++spin) {
        asm volatile("" ::: "memory");
        gf = ld_dev(rws, (lane < 14 ? lane : 0) * (DM * 8), WS_FG + cg * 16);
        if (__all(quad_ok(gf, tag))) break;
      }
      if (lane >= 14) gf = u32x4{0u, 0u, 0u, 0u};
      gate = rbf(silu(rbf(quad_sum(quad_val(gf[0]), quad_val(gf[1]), quad_val(gf[2]), quad_val(gf[3])))));
      asm volatile("" : "+v"(gate));
    }
    {
      u32x4 gs = {};
      bool have = false;
#pragma unroll 1
      for (unsigned spin = 0; spin < SPIN_CAP; ++spin) {
        asm volatile("" ::: "memory");
        if (!have) gs = ld_dev(rws, lane * (DM * 8), WS_P2 + cg * 16);
        have = quad_ok(gs, tag);
        if (__all(have)) break;
      }
      const float s = rbf(quad_sum(quad_val(gs[0]), quad_val(gs[1]), quad_val(gs[2]), quad_val(gs[3])));
      __builtin_amdgcn_raw_buffer_store_b16(__builtin_bit_cast(u16, (__bf16)(xr + rbf(s * gate))), rout, vst, 0, 0);
      if (wg == 0 && lane == 0) __builtin_amdgcn_raw_buffer_store_b32(epoch, rws, 0, WS_EPOCH, SC1);
    }
  }
}

}

extern "C" void kernel_launch(void* const* d_in, const int* in_sizes, int n_in, void* d_out, int out_size, void* d_ws, size_t ws_size,
                              hipStream_t stream) {
  (void)hipFuncSetAttribute((const void*)fused_decode_kernel, hipFuncAttributeMaxDynamicSharedMemorySize, L_END);
  fused_decode_kernel<<<NWG, NTHR, L_END, stream>>>((const u16*)d_in[0], (const u16*)d_in[1], (const u16*)d_in[2], (const u16*)d_in[3],
                                                    (const u16*)d_in[4], (const u16*)d_in[6], (const u16*)d_in[7], (u16*)d_out, (char*)d_ws);
}
```
